```python
import torch

def _softmax(x, axis=-1):
    return torch.softmax(x, dim=axis)

def _gelu(x):
    return torch.nn.functional.gelu(x, approximate='tanh')

def _silu(x):
    return torch.nn.functional.silu(x)

def _sigmoid(x):
    return torch.sigmoid(x)

def _layernorm(x, axis=-1, eps=1e-05):
    m = x.mean(dim=axis, keepdim=True)
    v = x.var(dim=axis, keepdim=True, unbiased=False)
    return (x - m) / torch.sqrt(v + eps)

def _scatter_add(tgt, idx, src, axis=0):
    return tgt.clone().index_add_(axis, idx, src)

def _segment_sum(src, seg, n):
    out = torch.zeros((n,) + src.shape[1:], dtype=src.dtype)
    return out.index_add_(0, seg, src)

def _pad(x, pw):
    flat = tuple((v for pair in reversed(pw) for v in pair))
    return torch.nn.functional.pad(x, flat)
kernel_name = 'adaLN_cond__sliding_block__geglu_ffn__prompt'

def setup_inputs():

    _w = lambda a: (lambda t: t.to(torch.bfloat16) if t.is_floating_point() else t)(torch.as_tensor(a))

    def _A_setup():
        B, S, D = (1, 2048, 512)
        return {'x': _w((lambda *s: torch.randn(*s, dtype=torch.bfloat16) * 0.05)(B, S, D)), 'wqkv': _w((lambda *s: torch.randn(*s, dtype=torch.bfloat16) * 0.05)(D, 3 * D)), 'wo': _w((lambda *s: torch.randn(*s, dtype=torch.bfloat16) * 0.05)(D, D))}
    _A_in = _A_setup()

    def _B_setup():
        B, S, D, F = (1, 2048, 512, 1792)
        return {'x': _w((lambda *s: torch.randn(*s, dtype=torch.bfloat16) * 0.05)(B, S, D)), 'w_gate': _w((lambda *s: torch.randn(*s, dtype=torch.bfloat16) * 0.05)(D, F)), 'w_up': _w((lambda *s: torch.randn(*s, dtype=torch.bfloat16) * 0.05)(D, F)), 'w_down': _w((lambda *s: torch.randn(*s, dtype=torch.bfloat16) * 0.05)(F, D))}
    _B_in = _B_setup()
    return {'A_x': _A_in['x'], 'A_wqkv': _A_in['wqkv'], 'A_wo': _A_in['wo'], 'B_x': _B_in['x'], 'B_w_gate': _B_in['w_gate'], 'B_w_up': _B_in['w_up'], 'B_w_down': _B_in['w_down'], 'c': _w((lambda *s: torch.randn(*s, dtype=torch.bfloat16) * 0.1)(1, 512) * 8.0), 'w_mod': _w((lambda *s: torch.randn(*s, dtype=torch.bfloat16) * 0.1)(512, 6 * 512) * 512 ** (-0.5) * 8.0)}

def reference(A_x, A_wqkv, A_wo, B_x, B_w_gate, B_w_up, B_w_down, c, w_mod):

    def _A_ref(x, wqkv, wo):
        B, S, D = x.shape
        H = 32
        Dh = D // H
        W = 256
        qkv = (lambda x, *d: x.permute(*d))((x @ wqkv).reshape(B, S, 3, H, Dh), 2, 0, 3, 1, 4)
        q, k, v = (qkv[0], qkv[1], qkv[2])
        scores = torch.einsum('bhqd,bhkd->bhqk', q, k) * Dh ** (-0.5)
        idx = torch.arange(S)
        mask = (idx[:, None] - idx[None, :] >= 0) & (idx[:, None] - idx[None, :] < W)
        scores = torch.where(mask[None, None], scores, -1000000000.0)
        p = _softmax(scores, axis=-1)
        o = torch.einsum('bhqk,bhkd->bhqd', p, v)
        return (lambda x, *d: x.permute(*d))(o, 0, 2, 1, 3).reshape(B, S, D) @ wo

    def _B_ref(x, w_gate, w_up, w_down):
        return _gelu(x @ w_gate) * (x @ w_up) @ w_down
    x = A_x
    mod = (_silu(c) @ w_mod).reshape(c.shape[0], 6, 1, -1)
    sa, ca, ga, sb, cb, gb = (mod[:, 0].clone(), mod[:, 1].clone(), mod[:, 2].clone(), mod[:, 3].clone(), mod[:, 4].clone(), mod[:, 5].clone())
    h = _layernorm(x, axis=-1) * (1 + ca) + sa
    x = x + ga * _A_ref(h, A_wqkv, A_wo)
    h = _layernorm(x, axis=-1) * (1 + cb) + sb
    x = x + gb * _B_ref(h, B_w_gate, B_w_up, B_w_down)
    return x
```

```cpp
#include <hip/hip_runtime.h>
#include <hip/hip_bf16.h>
typedef unsigned short u16;
__device__ __forceinline__ float bf2f(u16 b){ return __uint_as_float(((unsigned)b)<<16); }
__device__ __forceinline__ u16 f2bf(float f){ unsigned u=__float_as_uint(f); u += 0x7FFFu + ((u>>16)&1u); return (u16)(u>>16); }
__device__ __forceinline__ float rb(float f){ return bf2f(f2bf(f)); }

__global__ void k_mod(const u16* c, const u16* wm, float* mod){
  int n = blockIdx.x*blockDim.x+threadIdx.x; if(n>=3072) return;
  float a=0; for(int k=0;k<512;k++){ float cv=bf2f(c[k]); float s=rb(cv/(1.f+__expf(-cv))); a+= s*bf2f(wm[k*3072+n]); }
  mod[n]=rb(a);
}
__global__ void k_ln(const float* x, const float* sh, const float* sc, float* h){
  int row=blockIdx.x; int t=threadIdx.x; __shared__ float red[512];
  float v=x[row*512+t]; red[t]=v; __syncthreads();
  for(int s=256;s>0;s>>=1){ if(t<s) red[t]+=red[t+s]; __syncthreads(); }
  float m=red[0]/512.f; __syncthreads();
  float d=v-m; red[t]=d*d; __syncthreads();
  for(int s=256;s>0;s>>=1){ if(t<s) red[t]+=red[t+s]; __syncthreads(); }
  float var=red[0]/512.f;
  float z=d*rsqrtf(var+1e-5f);
  h[row*512+t]=rb(z*rb(1.f+sc[t])+sh[t]);
}
__global__ void k_cvt(const u16* x, float* o, int n){ int i=blockIdx.x*blockDim.x+threadIdx.x; if(i<n) o[i]=bf2f(x[i]); }
__global__ void k_gemm(const float* A, const u16* W, float* C, int M,int N,int K){
  __shared__ float As[16][17]; __shared__ float Ws[16][17];
  int tx=threadIdx.x&15, ty=threadIdx.x>>4; int row=blockIdx.y*16+ty, col=blockIdx.x*16+tx; float a=0;
  for(int k0=0;k0<K;k0+=16){ As[ty][tx]=A[row*K+k0+tx]; Ws[ty][tx]=bf2f(W[(k0+ty)*N+col]); __syncthreads();
    for(int k=0;k<16;k++) a+=As[ty][k]*Ws[k][tx]; __syncthreads(); }
  C[row*N+col]=rb(a);
}
__global__ void k_attn(const float* qkv, float* o){
  int q=blockIdx.x*64+threadIdx.x; int h=blockIdx.y; float qv[16]; for(int d=0;d<16;d++) qv[d]=qkv[q*1536+h*16+d];
  int k0=q-255; if(k0<0)k0=0; float m=-1e30f;
  for(int k=k0;k<=q;k++){ float s=0; for(int d=0;d<16;d++) s+=qv[d]*qkv[k*1536+512+h*16+d]; s*=0.25f; m=fmaxf(m,s);}
  float l=0; float acc[16]; for(int d=0;d<16;d++)acc[d]=0;
  for(int k=k0;k<=q;k++){ float s=0; for(int d=0;d<16;d++) s+=qv[d]*qkv[k*1536+512+h*16+d]; s*=0.25f; float p=__expf(s-m); l+=p; for(int d=0;d<16;d++) acc[d]+=p*qkv[k*1536+1024+h*16+d]; }
  for(int d=0;d<16;d++) o[q*512+h*16+d]=rb(acc[d]/l);
}
__global__ void k_res(float* x, const float* a, const float* g, int n){ int i=blockIdx.x*blockDim.x+threadIdx.x; if(i<n) x[i]=rb(x[i]+rb(g[i&511]*a[i])); }
__global__ void k_glu(float* g, const float* u, int n){ int i=blockIdx.x*blockDim.x+threadIdx.x; if(i<n){ float x=g[i]; float t=tanhf(0.7978845608f*(x+0.044715f*x*x*x)); g[i]=rb(rb(0.5f*x*(1.f+t))*u[i]); } }
__global__ void k_out(const float* x, u16* o, int n){ int i=blockIdx.x*blockDim.x+threadIdx.x; if(i<n) o[i]=f2bf(x[i]); }

extern "C" void kernel_launch(void* const* d_in, const int* in_sizes, int n_in, void* d_out, int out_size, void* d_ws, size_t ws_size, hipStream_t stream) {
  const u16* x=(const u16*)d_in[0]; const u16* wqkv=(const u16*)d_in[1]; const u16* wo=(const u16*)d_in[2];
  const u16* wg=(const u16*)d_in[4]; const u16* wu=(const u16*)d_in[5]; const u16* wd=(const u16*)d_in[6];
  const u16* c=(const u16*)d_in[7]; const u16* wm=(const u16*)d_in[8];
  float* ws=(float*)d_ws; float* mod=ws; float* xf=ws+4096; float* h=xf+2048*512; float* qkv=h+2048*512; float* o=qkv+2048*1536; float* a=o+2048*512; float* g=a+2048*512; float* u=g+2048*1792;
  int n=2048*512;
  k_mod<<<12,256,0,stream>>>(c,wm,mod);
  k_cvt<<<n/256,256,0,stream>>>(x,xf,n);
  k_ln<<<2048,512,0,stream>>>(xf,mod,mod+512,h);
  k_gemm<<<dim3(96,128),256,0,stream>>>(h,wqkv,qkv,2048,1536,512);
  k_attn<<<dim3(32,32),64,0,stream>>>(qkv,o);
  k_gemm<<<dim3(32,128),256,0,stream>>>(o,wo,a,2048,512,512);
  k_res<<<n/256,256,0,stream>>>(xf,a,mod+1024,n);
  k_ln<<<2048,512,0,stream>>>(xf,mod+1536,mod+2048,h);
  k_gemm<<<dim3(112,128),256,0,stream>>>(h,wg,g,2048,1792,512);
  k_gemm<<<dim3(112,128),256,0,stream>>>(h,wu,u,2048,1792,512);
  k_glu<<<2048*1792/256,256,0,stream>>>(g,u,2048*1792);
  k_gemm<<<dim3(32,128),256,0,stream>>>(g,wd,a,2048,512,1792);
  k_res<<<n/256,256,0,stream>>>(xf,a,mod+2560,n);
  k_out<<<n/256,256,0,stream>>>(xf,(u16*)d_out,n);
}
```

```cpp
#include <hip/hip_runtime.h>
#include <stdint.h>

typedef unsigned short u16;
typedef unsigned int u32;
typedef unsigned long long u64;
typedef __attribute__((ext_vector_type(8))) __bf16 bf16x8;
typedef __attribute__((ext_vector_type(2))) __bf16 bf16x2;
typedef __attribute__((ext_vector_type(4))) short s16x4;
typedef __attribute__((ext_vector_type(8))) short s16x8;
typedef __attribute__((ext_vector_type(16))) float f32x16;
typedef __attribute__((ext_vector_type(4))) float f32x4;
typedef __attribute__((ext_vector_type(2))) float f32x2;
typedef __attribute__((ext_vector_type(4))) u32 u32x4;
typedef __attribute__((ext_vector_type(2))) u32 u32x2;
#define LAS __attribute__((address_space(3)))
#define GAS __attribute__((address_space(1)))
#define DI __device__ __forceinline__
#define RLX_AGENT __ATOMIC_RELAXED, __HIP_MEMORY_SCOPE_AGENT

constexpr u32 WS_EPOCH = 0;
constexpr u32 WS_ARR = 1024;
constexpr u32 WS_MODG = 4096;
constexpr u32 WS_FKV = 16384;
constexpr u32 WS_FO = WS_FKV + 256 * 64;
constexpr u32 WS_FX = WS_FO + 64 * 128;
constexpr u32 WS_FA = WS_FX + 128 * 128;
constexpr u32 WS_ST = 65536;
constexpr u32 WS_KV = 262144;
constexpr u32 WS_O = WS_KV + 4194304;
constexpr u32 WS_X1 = WS_O + 2097152;
constexpr u32 WS_ACT = WS_X1 + 2097152;
constexpr u32 WS_DBG = WS_ACT + 7340032;
constexpr u32 WS_BYTES = WS_DBG + 1048576;
static_assert(WS_FA + 64 * 128 <= WS_ST, "flags overlap");

constexpr u32 KB = 1024;
constexpr u32 L_WO = 28 * KB;
constexpr u32 L_KV = 92 * KB;
constexpr u32 L_Q = 140 * KB;
constexpr u32 L_AR1 = 116 * KB;
constexpr u32 L_AR4 = 56 * KB;
constexpr u32 L_MISC = 148 * KB;
constexpr u32 L_C1 = L_MISC, L_SH = L_MISC + 2048, L_C1B = L_MISC + 4096, L_SHB = L_MISC + 6144, M_C1B = 4096;
constexpr u32 L_ONES = L_MISC + 8192;
constexpr u32 L_GA = L_ONES + 256, M_GA = 8192 + 256, M_GB = 8192 + 512;
constexpr u32 L_TAG = L_ONES + 768, L_JUNK = L_ONES + 1536;
constexpr u32 L_RED = L_MISC + 10240;
constexpr u32 LDS_BYTES = 163840;
constexpr u32 wq_base(int c) { return c < 6 ? c * 12288u : 92 * KB + (c - 6) * 12288u; }
constexpr u32 gu_slot(int s) { return s == 0 ? 0u : s == 1 ? 92 * KB : s == 2 ? 120 * KB : 28 * KB; }
constexpr u32 wd_base(int i) { return i < 3 ? i * 8192u : i < 10 ? 92 * KB + (i - 3) * 8192u : 28 * KB + (i - 10) * 8192u; }
constexpr u32 CODE_LINES = 14;

constexpr int SPIN_MAX = 1 << 17;
constexpr float NEGBIG = -1e30f;
constexpr float QSCALE = 0.25f * 1.4426950408889634f;

template <int I> struct IC { static constexpr int value = I; };
template <int I, int N, class F> DI void static_for(F&& f) { if constexpr (I < N) { f(IC<I>{}); static_for<I + 1, N>(f); } }
DI float bf_lo(u32 v) { return __uint_as_float(v << 16); }
DI float bf_hi(u32 v) { return __uint_as_float(v & 0xffff0000u); }
DI u32 pk(float a, float b) { f32x2 v = {a, b}; return __builtin_bit_cast(u32, __builtin_convertvector(v, bf16x2)); }
DI float rb(float a) { return bf_lo(pk(a, a)); }
template <int O> DI u32x4 pack8(const f32x16& a) { return (u32x4){pk(a[O], a[O + 1]), pk(a[O + 2], a[O + 3]), pk(a[O + 4], a[O + 5]), pk(a[O + 6], a[O + 7])}; }
DI bf16x8 asbf(u32x4 v) { return __builtin_bit_cast(bf16x8, v); }
#define MFMA(a, b, c) __builtin_amdgcn_mfma_f32_32x32x16_bf16((a), (b), (c), 0, 0, 0)
template <int CTRL, int RM = 0xf> DI float dpp(float v) { return __builtin_bit_cast(float, __builtin_amdgcn_update_dpp(0, __builtin_bit_cast(int, v), CTRL, RM, 0xf, true)); }
DI float sum8(float v) { v += dpp<0xB1>(v); v += dpp<0x4E>(v); v += dpp<0x141>(v); return v; }
DI float dot2(u32 a, u32 b, float c) { return __builtin_amdgcn_fdot2_f32_bf16(__builtin_bit_cast(bf16x2, a), __builtin_bit_cast(bf16x2, b), c, false); }

DI u32x4 lds_ld128(LAS char* L, u32 off) { return *(LAS u32x4*)(L + off); }
DI f32x4 lds_ldf4(LAS char* L, u32 off) { return *(LAS f32x4*)(L + off); }
DI void lds_st128(LAS char* L, u32 off, u32x4 v) { *(LAS u32x4*)(L + off) = v; }
DI bf16x8 lds_tr(LAS char* L, u32 off0, u32 off1) {
  s16x4 a = __builtin_amdgcn_ds_read_tr16_b64_v4i16((LAS s16x4*)(L + off0));
  s16x4 b = __builtin_amdgcn_ds_read_tr16_b64_v4i16((LAS s16x4*)(L + off1));
  return __builtin_bit_cast(bf16x8, __builtin_shufflevector(a, b, 0, 1, 2, 3, 4, 5, 6, 7));
}
DI u32x4 make_srd(const void* p, u32 bytes) { const u64 a = (u64)p; return (u32x4){(u32)a, (u32)(a >> 32), bytes, 0x00020000u}; }
DI void glds(const u32x4& srd, u32 voff, u32 soff, u32 ldsaddr) {
  asm volatile("s_mov_b32 m0, %3\n\ts_nop 0\n\tbuffer_load_dwordx4 %0, %1, %2 offen lds" ::"v"(voff), "s"(srd), "s"(soff), "s"(ldsaddr) : "memory");
}
DI void touch(const u32x4& srd, u32 voff, u32 ldsaddr) {
  asm volatile("s_mov_b32 m0, %2\n\ts_nop 0\n\tbuffer_load_dword %0, %1, 0 offen lds" ::"v"(voff), "s"(srd), "s"(ldsaddr) : "memory");
}
#define BAR() asm volatile("s_waitcnt lgkmcnt(0)\n\ts_barrier" ::: "memory")
#define WAITV_BAR(N) asm volatile("s_waitcnt vmcnt(%0) lgkmcnt(0)\n\ts_barrier" ::"i"(N) : "memory")
#define WAITV(N) asm volatile("s_waitcnt vmcnt(%0)" ::"i"(N) : "memory")
#define SB() __builtin_amdgcn_sched_barrier(0)
#define PIN(x) asm volatile("" : "+v"(x))
#define PINS(x) asm volatile("" : "+s"(x))
#define ORDER() __builtin_amdgcn_fence(__ATOMIC_ACQUIRE, "wavefront")

DI void wait_flags(const GAS u32* p, u32 tag) {
  for (int it = 0; it < SPIN_MAX; ++it) {
    u32 v = __hip_atomic_load(p, RLX_AGENT);
    if (__all(v == tag)) break;
    __builtin_amdgcn_s_sleep(1);
  }
  ORDER();
}
template <int N> DI void gather_gran(const GAS u64* g, u32 tag, u32 (&v)[N], int stride) {
  for (int it = 0; it < SPIN_MAX; ++it) {
    bool ok = true;
#pragma unroll
    for (int k = 0; k < N; ++k) { u64 x = __hip_atomic_load(g + k * stride, RLX_AGENT); v[k] = (u32)x; ok &= (u32)(x >> 32) == tag; }
    if (__all(ok)) break;
    __builtin_amdgcn_s_sleep(1);
  }
  ORDER();
}

#define STAMPC(n) do {} while (0)
#define STAMP(n) do {} while (0)

DI void ln_scal(float mean, float var, float& r, float& mr) {
  float mb = rb(mean), vb = rb(fmaxf(var, 0.f));
  float sd = rb(__builtin_amdgcn_sqrtf(rb(vb + 1e-5f)));
  r = __builtin_amdgcn_rcpf(sd);
  mr = mb * r;
}
DI float fma1(float a, float b, float c) { float r = fmaf(a, b, c); asm("" : "+v"(r)); return r; }
typedef __attribute__((ext_vector_type(2))) _Float16 f16x2;
DI u32x4 znorm8(u32x4 x, float r, float mr) {
  u32x4 o;
#pragma unroll
  for (int e = 0; e < 4; ++e) {
    const f32x2 v = {fmaf(bf_lo(x[e]), r, -mr), fmaf(bf_hi(x[e]), r, -mr)};
    o[e] = __builtin_bit_cast(u32, __builtin_convertvector(v, f16x2));
  }
  return o;
}
template <bool FULL> DI u32x4 xform8(u32x4 x, float r, float mr, f32x4 ca, f32x4 cb, f32x4 sa, f32x4 sb) {
  float v[8];
#pragma unroll
  for (int e = 0; e < 4; ++e) {
    if (FULL) { v[2 * e] = fma1(bf_lo(x[e]), r, -mr); v[2 * e + 1] = fma1(bf_hi(x[e]), r, -mr); }
    else { const u32 xe = x[e]; const f16x2 z = __builtin_bit_cast(f16x2, xe); v[2 * e] = (float)z[0]; v[2 * e + 1] = (float)z[1]; }
  }
#pragma unroll
  for (int e = 0; e < 4; ++e) { v[e] = fma1(v[e], ca[e], sa[e]); v[4 + e] = fma1(v[4 + e], cb[e], sb[e]); }
  return (u32x4){pk(v[0], v[1]), pk(v[2], v[3]), pk(v[4], v[5]), pk(v[6], v[7])};
}
DI float geglu(float g, float u) {
  const float K1 = -2.0f * 0.7978845608028654f * 1.4426950408889634f, K3 = K1 * 0.044715f;
  float e = __builtin_amdgcn_exp2f(g * fmaf(g * g, K3, K1));
  return g * __builtin_amdgcn_rcpf(1.0f + e) * u;
}

__global__ __launch_bounds__(512, 2) void adaln_block(const u16* __restrict__ x, const u16* __restrict__ wqkv, const u16* __restrict__ wo,
                                                      const u16* __restrict__ wg, const u16* __restrict__ wu, const u16* __restrict__ wd,
                                                      const u16* __restrict__ cvec, const u16* __restrict__ wm, u16* __restrict__ out,
                                                      char* __restrict__ ws) {
  __shared__ __attribute__((aligned(1024))) char lds[LDS_BYTES];
  LAS char* L = (LAS char*)lds;
  const u32 lbase = (u32)(uintptr_t)L;
  const int tid = threadIdx.x, lane = tid & 63;
  const int w = __builtin_amdgcn_readfirstlane(tid >> 6), w4 = w & 3;
  const bool mw = w < 4;
  const int b = blockIdx.x, xcd = b & 7, ii = b >> 3;
  const int t1 = 15 - (4 * (xcd >> 1) + (ii >> 3)), p1 = 8 * (xcd & 1) + (ii & 7);
  const int t3 = 2 * xcd + (ii >> 4), hf = (ii >> 3) & 1, cg = ii & 7;
  const int t4 = 8 * (xcd >> 2) + (ii >> 2), j4 = 4 * (xcd & 3) + (ii & 3);
  const int rot4 = ii >> 2, rot5 = 7 * (ii >> 3);
  const int r32 = lane & 31, hh = lane >> 5, blk = (lane >> 4) & 1, q4 = (lane & 15) >> 2, p4 = lane & 3, l3 = lane >> 3, l7 = lane & 7;
  const __amdgpu_buffer_rsrc_t rs = __builtin_amdgcn_make_buffer_rsrc(ws, 0, WS_BYTES, 0x00020000);
  STAMP(0);

  auto mod_load = [&](int g, u32x4 (&M)[8]) {
    const GAS char* mb = (const GAS char*)wm + g * 128 + (64 * w + 8 * l3) * 6144 + l7 * 16;
#pragma unroll
    for (int n = 0; n < 8; ++n) M[n] = *(const GAS u32x4*)(mb + n * 6144);
  };
  auto mod_math = [&](int g, const u32x4 (&M)[8], const u32x4& cv, u32 tag_) {
    f32x2 a[4] = {};
#pragma unroll
    for (int n = 0; n < 8; ++n) {
      const float cf = (n & 1) ? bf_hi(cv[n >> 1]) : bf_lo(cv[n >> 1]);
      const float sl = rb(cf * __builtin_amdgcn_rcpf(1.0f + __builtin_amdgcn_exp2f(-1.4426950408889634f * cf)));
#pragma unroll
      for (int e = 0; e < 4; ++e) a[e] += sl * (f32x2){bf_lo(M[n][e]), bf_hi(M[n][e])};
    }
    float v[8] = {a[0][0], a[0][1], a[1][0], a[1][1], a[2][0], a[2][1], a[3][0], a[3][1]};
#pragma unroll
    for (int e = 0; e < 8; ++e) {
      v[e] += dpp<0x128>(v[e]);
      auto r1_ = __builtin_amdgcn_permlane16_swap(__float_as_uint(v[e]), __float_as_uint(v[e]), false, false);
      v[e] = __uint_as_float(r1_[0]) + __uint_as_float(r1_[1]);
      auto r2_ = __builtin_amdgcn_permlane32_swap(__float_as_uint(v[e]), __float_as_uint(v[e]), false, false);
      v[e] = __uint_as_float(r2_[0]) + __uint_as_float(r2_[1]);
    }
    if (lane < 8) {
      *(LAS f32x4*)(L + L_RED + w * 256 + lane * 32) = (f32x4){v[0], v[1], v[2], v[3]};
      *(LAS f32x4*)(L + L_RED + w * 256 + lane * 32 + 16) = (f32x4){v[4], v[5], v[6], v[7]};
    }
    BAR();
    if (w == 2) {
      float s = 0.f;
#pragma unroll
      for (int k = 0; k < 8; ++k) s += *(LAS float*)(L + L_RED + k * 256 + lane * 4);
      const float o = dpp<0xB1>(s);
      if (!(lane & 1)) __hip_atomic_store((GAS u64*)(ws + WS_MODG) + 32 * g + (lane >> 1), ((u64)tag_ << 32) | pk(s, o), RLX_AGENT);
    }
  };
  const GAS u32x4* cvp = (const GAS u32x4*)((const GAS char*)cvec + (64 * w + 8 * l3) * 2);

  u32 tag = 0;
  if (mw) tag = __builtin_amdgcn_readfirstlane(__hip_atomic_load((const GAS u32*)(ws + WS_EPOCH), RLX_AGENT)) + 1u;
  if (t1 == 15) {
    u32x4 M[8];
    mod_load(p1, M);
    const u32x4 cv = *cvp;
    mod_math(p1, M, cv, tag);
  }
  STAMP(1);
  u32x4 X[32];
  float rr[4], mr[4];
  const u32 xo = l3 * 1024u + l7 * 16u;
  if (mw) {
    if (w == 3) {
      u64 pc;
      asm volatile("s_getpc_b64 %0" : "=s"(pc));
      touch(make_srd((const void*)pc, CODE_LINES * 4096), (ii * CODE_LINES + (lane < CODE_LINES ? lane : 0)) * 128u, lbase + L_JUNK);
      if (lane < 32) lds_st128(L, L_ONES + (lane & 15) * 16 + (lane >> 4) * 1024, (u32x4){0x3f803f80u, 0x3f803f80u, 0x3f803f80u, 0x3f803f80u});
      if (lane == 0) *(LAS u32*)(L + L_TAG) = tag;
    }
    BAR();
    STAMP(2);
    if (w < 3) {
      const u32 vo = (lane >> 2) * 3072u + (lane & 3) * 16u;
      const u32x4 srd = make_srd(wqkv, 512 * 3072);
      u32 go[4];
#pragma unroll
      for (int e = 0; e < 4; ++e) { const int q = 4 * w + e; go[e] = (q / 3) * 16 * 3072 + (q % 3) * 1024 + 64 * p1; }
#pragma unroll
      for (int c = 0; c < 8; ++c)
#pragma unroll
        for (int e = 0; e < 4; ++e) glds(srd, vo, go[e] + c * 196608, lbase + wq_base(c) + (4 * w + e) * 1024);
    } else {
      if (lane == 0) __hip_atomic_store((GAS u32*)(ws + WS_ARR) + b, tag, RLX_AGENT);
      u32 v[8];
      gather_gran<8>((const GAS u64*)(ws + WS_MODG) + lane, tag, v, 64);
#pragma unroll
      for (int k = 0; k < 4; ++k) {
        *(LAS f32x2*)(L + L_SH + (lane + 64 * k) * 8) = (f32x2){bf_lo(v[k]), bf_hi(v[k])};
        *(LAS f32x2*)(L + L_C1 + (lane + 64 * k) * 8) = (f32x2){rb(1.f + bf_lo(v[4 + k])), rb(1.f + bf_hi(v[4 + k]))};
      }
    }
    STAMP(3);
  } else {
    const char* xb = (const char*)x + (t1 * 128 + 32 * w4) * 1024;
#pragma unroll
    for (int c = 0; c < 8; ++c)
#pragma unroll
      for (int g = 0; g < 4; ++g)
        asm volatile("global_load_dwordx4 %0, %1, %2 offset:%3" : "=v"(X[4 * c + g]) : "v"(xo), "s"(xb + g * 8192), "i"(c * 128) : "memory");
    BAR();
    STAMP(2);
    float sm[4] = {}, sq[4] = {};
#pragma unroll
    for (int c = 0; c < 8; ++c) {
      asm volatile("s_waitcnt vmcnt(%4) ; AW %0 %1 %2 %3" : "+v"(X[4 * c]), "+v"(X[4 * c + 1]), "+v"(X[4 * c + 2]), "+v"(X[4 * c + 3])
                   : "i"(28 - 4 * c) : "memory");
#pragma unroll
      for (int e = 0; e < 4; ++e)
#pragma unroll
        for (int g = 0; g < 4; ++g) { sm[g] = dot2(X[4 * c + g][e], 0x3f803f80u, sm[g]); sq[g] = dot2(X[4 * c + g][e], X[4 * c + g][e], sq[g]); }
    }
    tag = __builtin_amdgcn_readfirstlane(*(LAS u32*)(L + L_TAG));
#pragma unroll
    for (int g = 0; g < 4; ++g) {
      const float me = sum8(sm[g]) * (1.f / 512.f);
      ln_scal(me, sum8(sq[g]) * (1.f / 512.f) - me * me, rr[g], mr[g]);
    }
#pragma unroll
    for (int n = 0; n < 32; ++n) { X[n] = znorm8(X[n], rr[n & 3], mr[n & 3]); PIN(X[n]); }
    STAMP(3);
  }
  BAR();
  STAMP(4);

  u32 awE = L_AR1 + (32 * w4 + l3) * 128 + ((l7 ^ l3) << 4), awO = awE ^ 16u;
  u32 ard[4];
  {
    const int row = 32 * w4 + r32, f = (row & 7) ^ ((row >> 3) & 1);
#pragma unroll
    for (int s = 0; s < 4; ++s) { ard[s] = L_AR1 + row * 128 + ((((2 * s) | hh) ^ f) << 4); PIN(ard[s]); }
  }
  PIN(awE); PIN(awO);
  u32 cso = L_MISC + l7 * 32;
  PIN(cso);
#define XFORM(F, c, COFF)                                                                                                     \
  do {                                                                                                                        \
    const f32x4 ca_ = lds_ldf4(L, (COFF)), cb_ = lds_ldf4(L, (COFF) + 16), sa_ = lds_ldf4(L, (COFF) + 2048), sb_ = lds_ldf4(L, (COFF) + 2064); \
    _Pragma("unroll") for (int g = 0; g < 4; ++g) {                                                                           \
      PIN(X[4 * (c) + g]);                                                                                                    \
      lds_st128(L, ((g & 1) ? awO : awE) + g * 1024 + ((c) & 1) * 16384, xform8<F>(X[4 * (c) + g], rr[g], mr[g], ca_, cb_, sa_, sb_)); \
    }                                                                                                                         \
  } while (0)

  const u32x4 srd_g = make_srd(wg, 512 * 3584), srd_u = make_srd(wu, 512 * 3584), srd_d = make_srd(wd, 1792 * 1024), srd_o = make_srd(wo, 512 * 1024);

  u32x4 f0, f1, f2, f3, f4, f5;
  if (mw) {
    f32x16 aq = {}, ak = {}, av = {};
    const u32 wl = (8 * hh + q4) * 64 + 32 * blk + 8 * p4;
    u32 wlh = wl + 60 * KB;
    PIN(wlh);
    bf16x8 hf_[3], qf_[3], kf_[3], vf_[3];
#define P1O(n) (((n) < 20 ? wl + wq_base((n) >> 2) : wlh + (wq_base((n) >> 2) - 60 * KB)) + ((n) & 3) * 3072)
#define P1H(n) hf_[(n) % 3] = asbf(lds_ld128(L, ard[(n) & 3] + (((n) >> 2) & 1) * 16384))
    WAITV_BAR(28);
#pragma unroll
    for (int n = 0; n < 2; ++n) {
      P1H(n); qf_[n] = lds_tr(L, P1O(n), P1O(n) + 256); kf_[n] = lds_tr(L, P1O(n) + 1024, P1O(n) + 1280); vf_[n] = lds_tr(L, P1O(n) + 2048, P1O(n) + 2304);
    }
#pragma unroll
    for (int n = 0; n < 32; ++n) {
      const int m = n + 2;
      if ((m & 3) == 0 && m < 32) WAITV_BAR(28 - m);
      SB();
      ak = MFMA(kf_[n % 3], hf_[n % 3], ak);
      if (m < 32) { P1H(m); kf_[m % 3] = lds_tr(L, P1O(m) + 1024, P1O(m) + 1280); }
      SB();
      av = MFMA(hf_[n % 3], vf_[n % 3], av);
      if (m < 32) vf_[m % 3] = lds_tr(L, P1O(m) + 2048, P1O(m) + 2304);
      SB();
      aq = MFMA(qf_[n % 3], hf_[n % 3], aq);
      if (m < 32) qf_[m % 3] = lds_tr(L, P1O(m), P1O(m) + 256);
      PIN(ak); PIN(av); PIN(aq);
    }
    STAMP(5);
    f0 = pack8<0>(ak); f1 = pack8<8>(ak); f2 = pack8<0>(av); f3 = pack8<8>(av);
    const u32 o = WS_KV + ((t1 * 16 + p1) * 4 + w) * 4096 + lane * 16;
    __builtin_amdgcn_raw_buffer_store_b128(f0, rs, o, 0, 16);
    __builtin_amdgcn_raw_buffer_store_b128(f1, rs, o + 1024, 0, 16);
    __builtin_amdgcn_raw_buffer_store_b128(f2, rs, o + 2048, 0, 16);
    __builtin_amdgcn_raw_buffer_store_b128(f3, rs, o + 3072, 0, 16);
    aq *= QSCALE;
    f4 = pack8<0>(aq); f5 = pack8<8>(aq);
  } else {
    const u32x4 srd_a = w4 == 0 ? srd_o : w4 == 1 ? srd_g : w4 == 2 ? srd_u : srd_d;
    u32 ta0, ta1;
    if (w4 == 1 || w4 == 2) {
      const int e1 = 64 + lane;
      ta0 = (16 * ii + lane / 7) * 3584 + (xcd & 3) * 896 + (lane % 7) * 128;
      ta1 = lane < 48 ? (16 * ii + e1 / 7) * 3584 + (xcd & 3) * 896 + (e1 % 7) * 128 : 0x7fffffffu;
    } else {
      ta0 = ii * (w4 == 0 ? 16384 : 57344) + lane * 128;
      ta1 = ta0 + 8192;
    }
    const u32 ta2 = ii * 57344 + (2 + w4) * 8192 + lane * 128, ta3 = w4 == 0 ? ii * 57344 + 6 * 8192 + lane * 128 : 0x7fffffffu;
    XFORM(false, 0, cso);
    BAR();
#pragma unroll
    for (int c = 0; c < 7; ++c) {
      XFORM(false, c + 1, cso + (c + 1) * 256);
      if (c == 1) touch(srd_a, ta0, lbase + L_JUNK);
      if (c == 2) touch(srd_a, ta1, lbase + L_JUNK);
      if (c == 3) touch(srd_d, ta2, lbase + L_JUNK);
      if (c == 4) touch(srd_d, ta3, lbase + L_JUNK);
      BAR();
    }
    STAMP(5);
  }
  BAR();
  u32 guo[7];
#pragma unroll
  for (int n = 0; n < 7; ++n) {
    const int pc = w4 + 4 * n, up = pc >= 14, e = (pc - 14 * up) * 64 + lane, rho = e / 14, cc = e - 14 * rho;
    guo[n] = (u32)((up ? (rho ^ 4) : rho) * 3584 + cc * 16);
  }
  const u32x4 srd_3 = w4 < 2 ? srd_g : srd_u;
  auto load_gu = [&](int c, u32 slot) {
    int r_ = rot4; PINS(r_);
    const u32 so = j4 * 224 + ((c + r_) & 7) * (64 * 3584), ld = lbase + slot + w4 * 1024;
#pragma unroll
    for (int n = 0; n < 7; ++n) glds(n < 3 ? srd_g : n == 3 ? srd_3 : srd_u, guo[n], so, ld + n * 4096);
  };
  const u32 w8o = l3 * 1024u + ((l7 ^ (((lane >> 4) & 1) << 2)) << 4);
  const int orow = t3 * 128 + hf * 64 + 8 * w + l3;
  if (mw) {
    const u32 o = L_KV + (8 + w) * 4096 + lane * 16, oq = L_Q + w * 2048 + lane * 16;
    lds_st128(L, o, f0); lds_st128(L, o + 1024, f1); lds_st128(L, o + 2048, f2); lds_st128(L, o + 3072, f3);
    lds_st128(L, oq, f4); lds_st128(L, oq + 1024, f5);
    BAR();
    WAITV(0);
    if (lane == 0) __hip_atomic_store((GAS u32*)(ws + WS_FKV) + (t1 * 16 + p1) * 16 + w, tag, RLX_AGENT);
  } else {
    for (int n = w4 * 16; n < w4 * 16 + 16; ++n) glds(srd_o, w8o, cg * 128 + n * 8192, lbase + L_WO + n * 1024);
    BAR();
    load_gu(0, gu_slot(0));
  }
  const u32x4 xr = *(const GAS u32x4*)((const GAS char*)x + orow * 1024 + cg * 128 + l7 * 16);
  u32x4 Ma[8], Mb[8], cv2;
  if (t1 == 0) { mod_load(16 + p1, Ma); mod_load(32 + p1, Mb); cv2 = *cvp; }
  STAMP(6);

  {
    const int qb = mw ? w : 7 - w, hd = w >> 2;
    const bf16x8 qf = asbf(lds_ld128(L, L_Q + qb * 2048 + hd * 1024 + lane * 16));
    const u32 ka = L_KV + hd * 1024 + lane * 16;
    const bool vdat = (r32 >> 4) == hd;
    const u32 va = vdat ? L_KV + 2048 + lane * 16 : L_ONES + (lane & 15) * 16;
    const u32 vs = vdat ? 4096u : 0u;
    f32x16 cm;
#pragma unroll
    for (int g = 0; g < 16; ++g) cm[g] = (g & 3) + 8 * (g >> 2) + 4 * hh <= r32 ? 0.f : NEGBIG;
    const f32x16 zero = {};
    f32x16 O = {};
    float m = NEGBIG;
    auto rowmax = [&](const f32x16& s, float mm) {
#pragma unroll
      for (int g = 0; g < 16; g += 2) mm = fmaxf(fmaxf(mm, s[g]), s[g + 1]);
      return mm;
    };
    auto ldk = [&](int lb) { return asbf(lds_ld128(L, ka + lb * 4096)); };
#pragma nounroll
    for (int st = 0; st < 2; ++st) {
      int lm = 8 + qb, lo = 8, hi = 8 + qb;
      if (st) {
        STAMP(7);
        if (t1 == 0) break;
        if (w < 2) {
          const int tp = t1 - 2 + w;
          u32x4 nb[16] = {};
          if (tp >= 0) {
            wait_flags((const GAS u32*)(ws + WS_FKV) + (tp * 16 + p1) * 16 + (lane & 3), tag);
            const u32 o = WS_KV + (tp * 16 + p1) * 16384 + lane * 16;
#pragma unroll
            for (int n = 0; n < 16; ++n) nb[n] = __builtin_amdgcn_raw_buffer_load_b128(rs, o + n * 1024, 0, 16);
          }
#pragma unroll
          for (int n = 0; n < 16; ++n) lds_st128(L, L_KV + w * 16384 + n * 1024 + lane * 16, nb[n]);
        }
#pragma unroll
        for (int g = 0; g < 16; ++g) { float u = t1 == 1 ? NEGBIG : NEGBIG - cm[g]; asm("" : "+v"(u)); cm[g] = u; }
        lm = qb; lo = t1 == 1 ? 4 : qb + 1; hi = 8;
        BAR();
        STAMP(8);
      }
      bf16x8 kA = ldk(lo), kB = ldk(lo + 1);
      f32x16 sA = MFMA(ldk(lm), qf, cm), sB, sC;
      const bf16x8 k0 = kA;
      float mn = m;
      for (int lb = lo; lb < hi; lb += 2) {
        sB = MFMA(kA, qf, zero);
        sC = MFMA(kB, qf, zero);
        kA = ldk(lb + 2); kB = ldk(lb + 3);
        SB();
        mn = rowmax(sB, mn);
        if (lb + 1 < hi) mn = rowmax(sC, mn);
      }
      mn = rowmax(sA, mn);
      {
        auto r_ = __builtin_amdgcn_permlane32_swap(__float_as_uint(mn), __float_as_uint(mn), false, false);
        mn = fmaxf(__uint_as_float(r_[0]), __uint_as_float(r_[1]));
      }
      u32 vv = va + lm * vs;
      bf16x8 v0 = asbf(lds_ld128(L, vv)), v1 = asbf(lds_ld128(L, vv + 1024));
      f32x16 negm;
#pragma unroll
      for (int g = 0; g < 16; ++g) negm[g] = -mn;
      {
        const float al = __builtin_amdgcn_exp2f(m - mn);
#pragma unroll
        for (int g = 0; g < 16; ++g) { float u = sA[g] - mn, o = O[g] * al; asm("" : "+v"(u)); asm("" : "+v"(o)); sA[g] = u; O[g] = o; }
      }
      m = mn;
      kA = k0;
      vv = va + lo * vs;
#define P2STEP(CUR, NXT)                                                                      \
  {                                                                                           \
    NXT = MFMA(kA, qf, negm);                                                                 \
    kA = ldk(lb + 1);                                                                         \
    const bf16x8 n0_ = asbf(lds_ld128(L, vv)), n1_ = asbf(lds_ld128(L, vv + 1024));           \
    vv += vs;                                                                                 \
    SB();                                                                                     \
    _Pragma("unroll") for (int g = 0; g < 8; ++g) CUR[g] = __builtin_amdgcn_exp2f(CUR[g]);    \
    O = MFMA(v0, asbf(pack8<0>(CUR)), O);                                                     \
    _Pragma("unroll") for (int g = 8; g < 16; ++g) CUR[g] = __builtin_amdgcn_exp2f(CUR[g]);   \
    O = MFMA(v1, asbf(pack8<8>(CUR)), O);                                                     \
    SB();                                                                                     \
    v0 = n0_; v1 = n1_;                                                                       \
  }
      for (int lb = lo;; ++lb) {
        P2STEP(sA, sB);
        if (lb >= hi) break;
        ++lb;
        P2STEP(sB, sA);
        if (lb >= hi) break;
      }
    }
    u32x4 of;
    {
      const float inv = __builtin_amdgcn_rcpf(hd ? O[0] : O[8]);
      float o[8];
#pragma unroll
      for (int g = 0; g < 8; ++g) { o[g] = (hd ? O[8 + g] : O[g]) * inv; asm("" : "+v"(o[g])); }
      of = (u32x4){pk(o[0], o[1]), pk(o[2], o[3]), pk(o[4], o[5]), pk(o[6], o[7])};
    }
    __builtin_amdgcn_raw_buffer_store_b128(of, rs, WS_O + ((t1 * 4 + qb) * 32 + 2 * p1 + hd) * 1024 + lane * 16, 0, 16);
    WAITV(0);
    if (lane == 0) __hip_atomic_store((GAS u32*)(ws + WS_FO) + (t1 * 4 + qb) * 32 + 2 * p1 + hd, tag, RLX_AGENT);
  }
  STAMP(9);
  if (t1 == 0) {
    mod_math(16 + p1, Ma, cv2, tag);
    BAR();
    mod_math(32 + p1, Mb, cv2, tag);
    if (p1 == 0 && w == 7) {
      for (int it = 0; it < SPIN_MAX; ++it) {
        bool ok = true;
#pragma unroll
        for (int k = 0; k < 4; ++k) ok &= __hip_atomic_load((const GAS u32*)(ws + WS_ARR) + lane + 64 * k, RLX_AGENT) == tag;
        if (__all(ok)) break;
        __builtin_amdgcn_s_sleep(1);
      }
      if (lane == 0) __hip_atomic_store((GAS u32*)(ws + WS_EPOCH), tag, RLX_AGENT);
    }
  }
  BAR();

  const u32 wt0 = (4 * hh + q4) * 128 + ((32 * blk + 8 * p4) ^ ((q4 >> 1) << 6)), wt1 = wt0 ^ 64u;
  auto red_write = [&](u32 base, u32 S8, const f32x16& a0, const f32x16& a1) {
    u32 ln = lane; PIN(ln);
    const u32 o = base + ((ln & 31) >> 3) * S8 + (ln & 7) * 256, sw = (ln & 7) ^ (ln >> 5);
#pragma unroll
    for (int g = 0; g < 4; ++g) {
      *(LAS f32x4*)(L + o + (((2 * g) ^ sw) << 4)) = (f32x4){a0[4 * g], a0[4 * g + 1], a0[4 * g + 2], a0[4 * g + 3]};
      *(LAS f32x4*)(L + o + (((2 * g) ^ sw) << 4) + 128) = (f32x4){a1[4 * g], a1[4 * g + 1], a1[4 * g + 2], a1[4 * g + 3]};
    }
  };
  auto red_read = [&](u32 base, u32 S8, u32 SK, f32x4& lo, f32x4& hi) {
    u32 ln = lane; PIN(ln);
    const u32 o0 = base + w * S8 + (ln >> 3) * 256 + (((2 * (ln & 7)) ^ (ln >> 3)) << 4), o1 = o0 ^ 16u;
    lo = lds_ldf4(L, o0); hi = lds_ldf4(L, o1);
#pragma unroll
    for (int k = 1; k < 4; ++k) { lo += lds_ldf4(L, o0 + k * SK); hi += lds_ldf4(L, o1 + k * SK); }
  };
  u32x4 x1p;
  {
    if (mw) {
      bf16x8 wa[8], wb[8];
#pragma unroll
      for (int i = 0; i < 8; ++i) {
        const u32 o = L_WO + w * 16384 + i * 2048;
        wa[i] = lds_tr(L, o + wt0, o + wt0 + 1024); wb[i] = lds_tr(L, o + wt1, o + wt1 + 1024);
      }
      wait_flags((const GAS u32*)(ws + WS_FO) + (t3 * 4 + 2 * hf + (l3 & 1)) * 32 + 8 * w + l7, tag);
      STAMP(10);
      u32x4 of0[8], of1[8];
      const u32 oo = WS_O + ((t3 * 4 + 2 * hf) * 32 + 8 * w) * 1024 + lane * 16;
#pragma unroll
      for (int i = 0; i < 8; ++i) {
        of0[i] = __builtin_amdgcn_raw_buffer_load_b128(rs, oo + i * 1024, 0, 16);
        of1[i] = __builtin_amdgcn_raw_buffer_load_b128(rs, oo + 32768 + i * 1024, 0, 16);
      }
      SB();
      f32x16 a00 = {}, a01 = {}, a10 = {}, a11 = {};
#pragma unroll
      for (int i = 0; i < 8; ++i) {
        a00 = MFMA(wa[i], asbf(of0[i]), a00); a01 = MFMA(wb[i], asbf(of0[i]), a01);
        a10 = MFMA(wa[i], asbf(of1[i]), a10); a11 = MFMA(wb[i], asbf(of1[i]), a11);
      }
      STAMP(11);
      red_write(L_WO + w * 16384, 2048, a00, a01);
      red_write(L_WO + w * 16384 + 8192, 2048, a10, a11);
    } else {
      {
        u32 v[3];
        const GAS u64* g = (const GAS u64*)(ws + WS_MODG);
        for (int it = 0; it < SPIN_MAX; ++it) {
          const u64 x0 = __hip_atomic_load(g + 768 + (tid - 256), RLX_AGENT), x1_ = __hip_atomic_load(g + 1024 + (tid - 256), RLX_AGENT);
          const u64 x2 = __hip_atomic_load(g + (lane < 32 ? 512 : 1248) + cg * 32 + lane, RLX_AGENT);
          v[0] = (u32)x0; v[1] = (u32)x1_; v[2] = (u32)x2;
          if (__all((u32)(x0 >> 32) == tag && (u32)(x1_ >> 32) == tag && (u32)(x2 >> 32) == tag)) break;
          __builtin_amdgcn_s_sleep(1);
        }
        *(LAS f32x2*)(L + L_SHB + (tid - 256) * 8) = (f32x2){bf_lo(v[0]), bf_hi(v[0])};
        *(LAS f32x2*)(L + L_C1B + (tid - 256) * 8) = (f32x2){rb(1.f + bf_lo(v[1])), rb(1.f + bf_hi(v[1]))};
        if (w == 4) *(LAS f32x2*)(L + L_GA + lane * 8) = (f32x2){bf_lo(v[2]), bf_hi(v[2])};
      }
      STAMP(10);
      load_gu(1, gu_slot(1));
      load_gu(2, gu_slot(2));
      STAMP(11);
    }
    BAR();
    f32x4 lo, hi;
    red_read(L_WO, 2048, 16384, lo, hi);
    const f32x4 ga0 = lds_ldf4(L, cso + M_GA), ga1 = lds_ldf4(L, cso + M_GA + 16);
    float v[8];
    v[0] = rb(bf_lo(xr[0]) + rb(ga0[0] * rb(lo[0]))); v[1] = rb(bf_hi(xr[0]) + rb(ga0[1] * rb(lo[1])));
    v[2] = rb(bf_lo(xr[1]) + rb(ga0[2] * rb(lo[2]))); v[3] = rb(bf_hi(xr[1]) + rb(ga0[3] * rb(lo[3])));
    v[4] = rb(bf_lo(xr[2]) + rb(ga1[0] * rb(hi[0]))); v[5] = rb(bf_hi(xr[2]) + rb(ga1[1] * rb(hi[1])));
    v[6] = rb(bf_lo(xr[3]) + rb(ga1[2] * rb(hi[2]))); v[7] = rb(bf_hi(xr[3]) + rb(ga1[3] * rb(hi[3])));
    x1p = (u32x4){pk(v[0], v[1]), pk(v[2], v[3]), pk(v[4], v[5]), pk(v[6], v[7])};
    __builtin_amdgcn_raw_buffer_store_b128(x1p, rs, WS_X1 + orow * 1024 + cg * 128 + l7 * 16, 0, 16);
    const float sm = sum8(((v[0] + v[1]) + (v[2] + v[3])) + ((v[4] + v[5]) + (v[6] + v[7])));
    const float mu = sm * (1.f / 64.f);
    float m2 = 0.f;
#pragma unroll
    for (int k = 0; k < 8; ++k) m2 = fmaf(v[k] - mu, v[k] - mu, m2);
    m2 = sum8(m2);
    if (l7 == 0) __hip_atomic_store((GAS u64*)(ws + WS_ST) + orow * 8 + cg, ((u64)__float_as_uint(m2) << 32) | __float_as_uint(sm), RLX_AGENT);
    WAITV(0);
    if (lane == 0) __hip_atomic_store((GAS u32*)(ws + WS_FX) + (t3 * 8 + 4 * hf + (w >> 1)) * 32 + cg * 2 + (w & 1), tag, RLX_AGENT);
  }
  STAMP(12);
  BAR();

  if (mw) {
    u32 ln = lane; PIN(ln);
    {
      const u32 row = 32 * w4 + (ln & 31), f = (row & 7) ^ ((row >> 3) & 1);
#pragma unroll
      for (int s = 0; s < 4; ++s) { ard[s] = L_AR4 + row * 128 + ((((2 * s) | (ln >> 5)) ^ f) << 4); PIN(ard[s]); }
    }
    const u32 bk = (ln >> 4) & 1, gl = bk * 14336 + (8 * (ln >> 5) + ((ln & 15) >> 2)) * 224 + 8 * (ln & 3);
    const u32 g0 = gl + (bk ? 896u : 0u), g1 = gl + (bk ? 0u : 896u);
    u32 g0h = g0 + 92 * KB, g1h = g1 + 92 * KB;
    PIN(g0h); PIN(g1h);
    f32x16 ag[7] = {};
    bf16x8 hf_[2], wf_[2][7];
#define P4H(n) hf_[(n) & 1] = asbf(lds_ld128(L, ard[(n) & 3] + (((n) >> 2) & 1) * 16384))
#define P4HI(n) (gu_slot(((n) >> 2) & 3) >= 92 * KB)
#define P4O(n) (gu_slot(((n) >> 2) & 3) - (P4HI(n) ? 92 * KB : 0) + ((n) & 3) * 3584)
#define P4W(n, f) wf_[(n) & 1][f] = lds_tr(L, (P4HI(n) ? g0h : g0) + P4O(n) + (f) * 32, (P4HI(n) ? g1h : g1) + P4O(n) + (f) * 32)
    BAR();
    STAMP(14);
    P4H(0);
#pragma unroll
    for (int f = 0; f < 7; ++f) P4W(0, f);
    static_for<0, 32>([&](auto nc) {
      constexpr int n = nc.value, m = n + 1;
      if ((m & 3) == 0) BAR();
      if (m < 32) P4H(m);
#pragma unroll
      for (int f = 0; f < 7; ++f) {
        SB();
        ag[f] = MFMA(wf_[n & 1][f], hf_[n & 1], ag[f]);
        if (m < 32) P4W(m, f);
      }
#pragma unroll
      for (int f = 0; f < 7; ++f) PIN(ag[f]);
    });
    STAMP(15);
    PIN(ln);
    const u32 ao = WS_ACT + ((t4 * 4 + w) * 112 + 7 * j4) * 1024 + ln * 16;
#pragma unroll
    for (int f = 0; f < 7; ++f) {
      u32x4 o;
#pragma unroll
      for (int k = 0; k < 4; ++k) o[k] = pk(geglu(ag[f][2 * k], ag[f][8 + 2 * k]), geglu(ag[f][2 * k + 1], ag[f][9 + 2 * k]));
      __builtin_amdgcn_raw_buffer_store_b128(o, rs, ao + f * 1024, 0, 16);
    }
    WAITV(0);
    if (ln == 0) __hip_atomic_store((GAS u32*)(ws + WS_FA) + (t4 * 4 + w) * 32 + j4, tag, RLX_AGENT);
    STAMP(16);
  } else {
    auto load_wd = [&](int i, int li) {
      int ks = rot5; PINS(ks);
      ks += i; ks -= ks >= 28 ? 28 : 0;
      const u32 so = cg * 128 + (w4 * 448 + ks * 16) * 1024, ld = lbase + wd_base(li) + w4 * 2048;
      glds(srd_d, w8o, so, ld);
      glds(srd_d, w8o, so + 8192, ld + 1024);
    };
    load_gu(3, gu_slot(3));
    awE += L_AR4 - L_AR1; awO += L_AR4 - L_AR1; PIN(awE); PIN(awO);
    wait_flags((const GAS u32*)(ws + WS_FX) + (t4 * 8 + 2 * w4 + ((lane >> 4) & 1)) * 32 + (lane & 15), tag);
    STAMP(13);
    u32x2 st[4];
#pragma unroll
    for (int g = 0; g < 4; ++g) st[g] = __builtin_amdgcn_raw_buffer_load_b64(rs, lane * 8, WS_ST + (t4 * 128 + 32 * w4 + 8 * g) * 64, 16);
#pragma unroll
    for (int c = 0; c < 8; ++c) {
      const u32 so = WS_X1 + (t4 * 128 + 32 * w4) * 1024 + ((c + rot4) & 7) * 128;
#pragma unroll
      for (int g = 0; g < 4; ++g) X[4 * c + g] = __builtin_amdgcn_raw_buffer_load_b128(rs, xo, so + g * 8192, 16);
    }
    SB();
#pragma unroll
    for (int g = 0; g < 4; ++g) {
      const float a = __uint_as_float(st[g][0]);
      const float me = sum8(a) * (1.f / 512.f), d = a * (1.f / 64.f) - me;
      ln_scal(me, sum8(fmaf(64.f * d, d, __uint_as_float(st[g][1]))) * (1.f / 512.f), rr[g], mr[g]);
    }
    XFORM(true, 0, cso + M_C1B + rot4 * 256);
    BAR();
    STAMP(14);
#pragma unroll
    for (int c = 0; c < 8; ++c) {
      if (c == 2) {
#pragma unroll
        for (int n = 12; n < 32; ++n) PIN(X[n]);
        load_gu(4, gu_slot(0)); load_gu(5, gu_slot(1));
      }
      if (c == 3) load_gu(6, gu_slot(2));
      if (c == 4) load_gu(7, gu_slot(3));
      if (c == 5) { load_wd(0, 0); load_wd(1, 1); load_wd(2, 2); }
      if (c == 6) { load_wd(3, 3); load_wd(4, 4); load_wd(5, 5); }
      if (c == 7) { load_wd(6, 6); load_wd(7, 7); load_wd(8, 8); load_wd(9, 9); }
      if (c < 7) XFORM(true, c + 1, cso + M_C1B + ((c + 1 + rot4) & 7) * 256);
      if (c == 3 || c == 4) WAITV_BAR(14); else if (c == 5) WAITV_BAR(13); else if (c == 6) WAITV_BAR(12); else BAR();
    }
    STAMP(15);
    for (int i = 10; i < 18; ++i) load_wd(i, i);
    WAITV(16);
    bf16x8 wp0[10], wp1[10];
    u32 dA0 = w4 * 2048 + wt0, dA1 = w4 * 2048 + wt1, dB0 = dA0 + 92 * KB, dB1 = dA1 + 92 * KB, dC0 = dA0 + 28 * KB, dC1 = dA1 + 28 * KB;
    PIN(dA0); PIN(dA1); PIN(dB0); PIN(dB1); PIN(dC0); PIN(dC1);
#define WD0(li) ((li) < 3 ? dA0 + (li) * 8192 : (li) < 10 ? dB0 + ((li) - 3) * 8192 : dC0 + ((li) - 10) * 8192)
#define WD1(li) ((li) < 3 ? dA1 + (li) * 8192 : (li) < 10 ? dB1 + ((li) - 3) * 8192 : dC1 + ((li) - 10) * 8192)
#pragma unroll
    for (int i = 0; i < 10; ++i) { wp0[i] = lds_tr(L, WD0(i), WD0(i) + 1024); wp1[i] = lds_tr(L, WD1(i), WD1(i) + 1024); }
    asm volatile("s_waitcnt lgkmcnt(0)" ::: "memory");
    for (int i = 0; i < 10; ++i) load_wd(18 + i, i);
    STAMP(16);
    wait_flags((const GAS u32*)(ws + WS_FA) + (t3 * 4 + 2 * hf + ((lane >> 2) & 1)) * 32 + 4 * w4 + (lane & 3), tag);
    STAMP(17);
    const u32 ao = WS_ACT + ((t3 * 4 + 2 * hf) * 112 + 28 * w4) * 1024 + lane * 16;
    u32x4 af0[28], af1[28];
    auto ld_act = [&](int i) {
      int ks = i + rot5; ks -= ks >= 28 ? 28 : 0;
      af0[i] = __builtin_amdgcn_raw_buffer_load_b128(rs, ao, ks * 1024, 16);
      af1[i] = __builtin_amdgcn_raw_buffer_load_b128(rs, ao + 112 * 1024, ks * 1024, 16);
    };
#pragma unroll
    for (int i = 0; i < 7; ++i) ld_act(i);
    f32x16 a00 = {}, a01 = {}, a10 = {}, a11 = {};
#pragma unroll
    for (int i = 0; i < 28; ++i) {
      SB();
      a00 = MFMA(wp0[i % 10], asbf(af0[i]), a00);
      a01 = MFMA(wp1[i % 10], asbf(af0[i]), a01);
      SB();
      a10 = MFMA(wp0[i % 10], asbf(af1[i]), a10);
      if (i < 5) { ld_act(7 + 2 * i); ld_act(8 + 2 * i); }
      else if (i + 12 < 28) ld_act(i + 12);
      SB();
      a11 = MFMA(wp1[i % 10], asbf(af1[i]), a11);
      if (i >= 7 && i + 3 < 28) {
        const int li = i + 3 < 18 ? i + 3 : i - 15;
        wp0[(i + 3) % 10] = lds_tr(L, WD0(li), WD0(li) + 1024);
        wp1[(i + 3) % 10] = lds_tr(L, WD1(li), WD1(li) + 1024);
      }
      PIN(a00); PIN(a01); PIN(a10); PIN(a11);
    }
    STAMP(18);
    red_write(28 * KB + w4 * 2048, 8192, a00, a01);
    red_write(28 * KB + w4 * 2048 + 32768, 8192, a10, a11);
  }
  BAR();
  {
    f32x4 lo, hi;
    red_read(28 * KB, 8192, 2048, lo, hi);
    u32 ln = lane; PIN(ln);
    const f32x4 gb0 = lds_ldf4(L, L_MISC + M_GB + (ln & 7) * 32), gb1 = lds_ldf4(L, L_MISC + M_GB + (ln & 7) * 32 + 16);
    u32x4 o;
    o[0] = pk(bf_lo(x1p[0]) + rb(gb0[0] * rb(lo[0])), bf_hi(x1p[0]) + rb(gb0[1] * rb(lo[1])));
    o[1] = pk(bf_lo(x1p[1]) + rb(gb0[2] * rb(lo[2])), bf_hi(x1p[1]) + rb(gb0[3] * rb(lo[3])));
    o[2] = pk(bf_lo(x1p[2]) + rb(gb1[0] * rb(hi[0])), bf_hi(x1p[2]) + rb(gb1[1] * rb(hi[1])));
    o[3] = pk(bf_lo(x1p[3]) + rb(gb1[2] * rb(hi[2])), bf_hi(x1p[3]) + rb(gb1[3] * rb(hi[3])));
    *(GAS u32x4*)((GAS char*)out + (t3 * 128 + hf * 64 + 8 * w) * 1024 + cg * 128 + (ln >> 3) * 1024 + (ln & 7) * 16) = o;
  }
  STAMP(19);
}

extern "C" void kernel_launch(void* const* d_in, const int* in_sizes, int n_in, void* d_out, int out_size, void* d_ws, size_t ws_size,
                              hipStream_t stream) {
  adaln_block<<<256, 512, 0, stream>>>((const u16*)d_in[0], (const u16*)d_in[1], (const u16*)d_in[2], (const u16*)d_in[4], (const u16*)d_in[5],
                                       (const u16*)d_in[6], (const u16*)d_in[7], (const u16*)d_in[8], (u16*)d_out, (char*)d_ws);
}
```

```cpp
#include <hip/hip_runtime.h>
#include <stdint.h>

typedef unsigned short u16;
typedef unsigned int u32;
typedef unsigned long long u64;
typedef __attribute__((ext_vector_type(8))) __bf16 bf16x8;
typedef __attribute__((ext_vector_type(2))) __bf16 bf16x2;
typedef __attribute__((ext_vector_type(4))) short s16x4;
typedef __attribute__((ext_vector_type(8))) short s16x8;
typedef __attribute__((ext_vector_type(16))) float f32x16;
typedef __attribute__((ext_vector_type(4))) float f32x4;
typedef __attribute__((ext_vector_type(2))) float f32x2;
typedef __attribute__((ext_vector_type(4))) u32 u32x4;
typedef __attribute__((ext_vector_type(2))) u32 u32x2;
#define LAS __attribute__((address_space(3)))
#define GAS __attribute__((address_space(1)))
#define DI __device__ __forceinline__
#define RLX_AGENT __ATOMIC_RELAXED, __HIP_MEMORY_SCOPE_AGENT

constexpr u32 WS_EPOCH = 0;
constexpr u32 WS_ARR = 1024;
constexpr u32 WS_MODG = 4096;
constexpr u32 WS_FKV = 16384;
constexpr u32 WS_FO = WS_FKV + 256 * 64;
constexpr u32 WS_FX = WS_FO + 64 * 128;
constexpr u32 WS_FA = WS_FX + 64 * 128;
constexpr u32 WS_FUV = WS_FA + 64 * 128;
constexpr u32 WS_ST = 65536;
constexpr u32 WS_UV = 196608;
constexpr u32 WS_KV = 1048576;
constexpr u32 WS_O = WS_KV + 4194304;
constexpr u32 WS_X1 = WS_O + 2097152;
constexpr u32 WS_ACT = WS_X1 + 2097152;
constexpr u32 WS_DBG = WS_ACT + 7340032;
constexpr u32 WS_BYTES = WS_DBG + 1048576;
static_assert(WS_FUV + 16 * 256 <= WS_ST && WS_UV + 256 * 2048 <= WS_KV, "workspace map");

constexpr int SPIN_MAX = 1 << 17;
constexpr u32 KB = 1024;
constexpr u32 L_WO = 28 * KB;
constexpr u32 L_KV = 92 * KB;
constexpr u32 L_Q = 140 * KB;
constexpr u32 L_AR1 = 72 * KB;
constexpr u32 ar_off(int c) { return c % 3 == 0 ? 0u : c % 3 == 1 ? 44 * KB : 60 * KB; }
constexpr u32 L_YL = 56 * KB;
constexpr u32 L_MISC = 148 * KB;
constexpr u32 L_C1 = L_MISC, L_SH = L_MISC + 2048, L_C1B = L_MISC + 4096, L_SHB = L_MISC + 6144, M_C1B = 4096;
constexpr u32 L_UVT = L_C1, L_BST = L_SH;
constexpr u32 L_ONES = L_MISC + 8192;
constexpr u32 L_GA = L_ONES + 256, M_GA = 8192 + 256, M_GB = 8192 + 512;
constexpr u32 L_DONE = L_ONES + 784, L_LAND = L_ONES + 800, L_MODF = L_ONES + 816;
constexpr u32 L_TAG = L_ONES + 768, L_JUNK = L_ONES + 1536;
constexpr u32 L_RED = L_MISC + 10240;
constexpr u32 LDS_BYTES = 163840;
constexpr u32 wq_base(int c) { return c < 6 ? c * 12288u : 92 * KB + (c - 6) * 12288u; }
constexpr u32 gu_slot(int s) { return s == 0 ? 0u : s == 1 ? 92 * KB : s == 2 ? 120 * KB : 28 * KB; }
constexpr u32 wd_base(int i) { return i < 3 ? i * 8192u : i < 10 ? 92 * KB + (i - 3) * 8192u : 28 * KB + (i - 10) * 8192u; }
constexpr u32 CODE_LINES = 16;

constexpr float NEGBIG = -1e30f;
constexpr float QSCALE = 0.25f * 1.4426950408889634f;

template <int I> struct IC { static constexpr int value = I; };
template <int I, int N, class F> DI void static_for(F&& f) { if constexpr (I < N) { f(IC<I>{}); static_for<I + 1, N>(f); } }
DI float bf_lo(u32 v) { return __uint_as_float(v << 16); }
DI float bf_hi(u32 v) { return __uint_as_float(v & 0xffff0000u); }
DI u32 pk(float a, float b) { f32x2 v = {a, b}; return __builtin_bit_cast(u32, __builtin_convertvector(v, bf16x2)); }
DI float rb(float a) { return bf_lo(pk(a, a)); }
template <int O> DI u32x4 pack8(const f32x16& a) { return (u32x4){pk(a[O], a[O + 1]), pk(a[O + 2], a[O + 3]), pk(a[O + 4], a[O + 5]), pk(a[O + 6], a[O + 7])}; }
DI bf16x8 asbf(u32x4 v) { return __builtin_bit_cast(bf16x8, v); }
#define MFMA(a, b, c) __builtin_amdgcn_mfma_f32_32x32x16_bf16((a), (b), (c), 0, 0, 0)
template <int CTRL, int RM = 0xf> DI float dpp(float v) { return __builtin_bit_cast(float, __builtin_amdgcn_update_dpp(0, __builtin_bit_cast(int, v), CTRL, RM, 0xf, true)); }
DI float sum8(float v) { v += dpp<0xB1>(v); v += dpp<0x4E>(v); v += dpp<0x141>(v); return v; }
DI float dot2(u32 a, u32 b, float c) { return __builtin_amdgcn_fdot2_f32_bf16(__builtin_bit_cast(bf16x2, a), __builtin_bit_cast(bf16x2, b), c, false); }

DI u32x4 lds_ld128(LAS char* L, u32 off) { return *(LAS u32x4*)(L + off); }
DI f32x4 lds_ldf4(LAS char* L, u32 off) { return *(LAS f32x4*)(L + off); }
DI void lds_st128(LAS char* L, u32 off, u32x4 v) { *(LAS u32x4*)(L + off) = v; }
DI bf16x8 lds_tr(LAS char* L, u32 off0, u32 off1) {
  s16x4 a = __builtin_amdgcn_ds_read_tr16_b64_v4i16((LAS s16x4*)(L + off0));
  s16x4 b = __builtin_amdgcn_ds_read_tr16_b64_v4i16((LAS s16x4*)(L + off1));
  return __builtin_bit_cast(bf16x8, __builtin_shufflevector(a, b, 0, 1, 2, 3, 4, 5, 6, 7));
}
DI u32x4 make_srd(const void* p, u32 bytes) { const u64 a = (u64)p; return (u32x4){(u32)a, (u32)(a >> 32), bytes, 0x00020000u}; }
DI void glds(const u32x4& srd, u32 voff, u32 soff, u32 ldsaddr) {
  asm volatile("s_mov_b32 m0, %3\n\ts_nop 0\n\tbuffer_load_dwordx4 %0, %1, %2 offen lds" ::"v"(voff), "s"(srd), "s"(soff), "s"(ldsaddr) : "memory");
}
DI void touch(const u32x4& srd, u32 voff, u32 ldsaddr) {
  asm volatile("s_mov_b32 m0, %2\n\ts_nop 0\n\tbuffer_load_dword %0, %1, 0 offen lds" ::"v"(voff), "s"(srd), "s"(ldsaddr) : "memory");
}
DI void lds_post(u32 addr, u32 val) {
  asm volatile("s_mov_b64 exec, 1\n\tds_write_b32 %0, %1\n\ts_mov_b64 exec, -1" ::"v"(addr), "v"(val) : "memory");
}
DI bool all_ge(u32x4 v, u32 k) { return __builtin_amdgcn_readfirstlane(min(min(v[0], v[1]), min(v[2], v[3]))) >= k; }
DI void lds_wait(LAS char* L, u32 off, u32 k) {
#pragma nounroll
  for (int it = 0; it < (SPIN_MAX << 4); ++it) {
    if (all_ge(*(volatile LAS u32x4*)(L + off), k)) break;
    __builtin_amdgcn_s_sleep(3);
  }
  asm volatile("" ::: "memory");
}
#define BAR() asm volatile("s_waitcnt lgkmcnt(0)\n\ts_barrier" ::: "memory")
#define WAITV_BAR(N) asm volatile("s_waitcnt vmcnt(%0) lgkmcnt(0)\n\ts_barrier" ::"i"(N) : "memory")
#define WAITV(N) asm volatile("s_waitcnt vmcnt(%0)" ::"i"(N) : "memory")
#define SB() __builtin_amdgcn_sched_barrier(0)
#define PIN(x) asm volatile("" : "+v"(x))
#define PINS(x) asm volatile("" : "+s"(x))
#define ORDER() __builtin_amdgcn_fence(__ATOMIC_ACQUIRE, "wavefront")

DI void wait_flags(const GAS u32* p, u32 tag) {
#pragma nounroll
  for (int it = 0; it < SPIN_MAX; ++it) {
    u32 v = __hip_atomic_load(p, RLX_AGENT);
    if (__all(v == tag)) break;
    __builtin_amdgcn_s_sleep(1);
  }
  ORDER();
}
template <int N> DI void gather_gran(const GAS u64* g, u32 tag, u32 (&v)[N], int stride) {
#pragma nounroll
  for (int it = 0; it < SPIN_MAX; ++it) {
    bool ok = true;
#pragma unroll
    for (int k = 0; k < N; ++k) { u64 x = __hip_atomic_load(g + k * stride, RLX_AGENT); v[k] = (u32)x; ok &= (u32)(x >> 32) == tag; }
    if (__all(ok)) break;
    __builtin_amdgcn_s_sleep(1);
  }
  ORDER();
}

#define STAMPC(n) do {} while (0)
#define STAMP(n) do {} while (0)

#define STAMP4(n) do {} while (0)
#define STAMP1(n) do {} while (0)
DI void ln_scal(float mean, float var, float& mb, float& sd) {
  mb = rb(mean);
  sd = rb(__builtin_amdgcn_sqrtf(rb(rb(fmaxf(var, 0.f)) + 1e-5f)));
}
DI float fma1(float a, float b, float c) { float r = fmaf(a, b, c); asm("" : "+v"(r)); return r; }
typedef __attribute__((ext_vector_type(2))) _Float16 f16x2;
DI u32x4 znorm8(u32x4 x, float r, float mr) {
  u32x4 o;
#pragma unroll
  for (int e = 0; e < 4; ++e) {
    const f32x2 v = {fmaf(bf_lo(x[e]), r, -mr), fmaf(bf_hi(x[e]), r, -mr)};
    o[e] = __builtin_bit_cast(u32, __builtin_convertvector(v, f16x2));
  }
  return o;
}
DI u32 xform2(u32 z_, u32 c_, u32 s_) {
  const f16x2 z = __builtin_bit_cast(f16x2, z_), c = __builtin_bit_cast(f16x2, c_), s = __builtin_bit_cast(f16x2, s_);
  return pk(fma1((float)z[0], (float)c[0], (float)s[0]), fma1((float)z[1], (float)c[1], (float)s[1]));
}
DI u32x4 xform8(u32x4 x, u32x4 c, u32x4 s) { return (u32x4){xform2(x[0], c[0], s[0]), xform2(x[1], c[1], s[1]), xform2(x[2], c[2], s[2]), xform2(x[3], c[3], s[3])}; }
DI u32 pkh(float a, float b) { const f32x2 v = {a, b}; return __builtin_bit_cast(u32, __builtin_convertvector(v, f16x2)); }
DI f32x2 geglu2(f32x2 ag, f32x2 au, float k1, float k3, float i2) {
  const f32x2 a = ag * (ag * ag * k3 + k1);
  const f32x2 d = (f32x2){__builtin_amdgcn_exp2f(a[0]), __builtin_amdgcn_exp2f(a[1])} * i2 + i2;
  return ag * au * (f32x2){__builtin_amdgcn_rcpf(d[0]), __builtin_amdgcn_rcpf(d[1])};
}

__global__ __launch_bounds__(512, 2) void adaln_block(const u16* __restrict__ x, const u16* __restrict__ wqkv, const u16* __restrict__ wo,
                                                      const u16* __restrict__ wg, const u16* __restrict__ wu, const u16* __restrict__ wd,
                                                      const u16* __restrict__ cvec, const u16* __restrict__ wm, u16* __restrict__ out,
                                                      char* __restrict__ ws) {
  __shared__ __attribute__((aligned(1024))) char lds[LDS_BYTES];
  LAS char* L = (LAS char*)lds;
  const u32 lbase = (u32)(uintptr_t)L;
  const int tid = threadIdx.x, lane = tid & 63;
  const int w = __builtin_amdgcn_readfirstlane(tid >> 6), w4 = w & 3;
  const bool mw = w < 4;
  const int b = blockIdx.x, xcd = b & 7, ii = b >> 3;
  const int t1 = 15 - (4 * (xcd >> 1) + (ii >> 3)), p1 = 8 * (xcd & 1) + (ii & 7);
  const int t3 = 2 * xcd + (ii >> 4), hf = (ii >> 3) & 1, cg = ii & 7;
  const int t4 = 8 * (xcd >> 2) + (ii >> 2), j4 = 4 * (xcd & 3) + (ii & 3);

  const int rot1 = (p1 + 2 * t1) & 7;
  const int rot4 = ii >> 2, rot5 = 7 * (ii >> 3);
  const int r32 = lane & 31, hh = lane >> 5, blk = (lane >> 4) & 1, q4 = (lane & 15) >> 2, p4 = lane & 3, l3 = lane >> 3, l7 = lane & 7;
  const __amdgpu_buffer_rsrc_t rs = __builtin_amdgcn_make_buffer_rsrc(ws, 0, WS_BYTES, 0x00020000);
  STAMP(0);

  u32 tag = 0;
  if (tid < 12) *(LAS u32*)(L + L_DONE + tid * 4) = 0u;
  BAR();
  const int orow = t3 * 128 + hf * 64 + 8 * w + l3;
  STAMP(1);
  u32x4 X[24];
  const __amdgpu_buffer_rsrc_t rx = __builtin_amdgcn_make_buffer_rsrc((void*)x, 0, 2048 * 1024, 0x00020000);
  const u32 xo = l3 * 1024u + l7 * 16u;
  u32x4 xr;
  if (mw) {
    const u32x4 srd = make_srd(wqkv, 512 * 3072);
    const int mg = (15 - t1) * 16 + p1;
    u32x4 M[16], cv[2];
    if (t1 >= 13) {
      const GAS char* mb = (const GAS char*)wm + mg * 128 + (128 * w + 16 * l3) * 6144 + l7 * 16;
      const GAS u32x4* cp = (const GAS u32x4*)((const GAS char*)cvec + (128 * w + 16 * l3) * 2);
      cv[0] = cp[0]; cv[1] = cp[1];
#pragma unroll
      for (int n = 0; n < 16; ++n) M[n] = *(const GAS u32x4*)(mb + n * 6144);
    }
    const u32 ep = __hip_atomic_load((const GAS u32*)(ws + WS_EPOCH), RLX_AGENT);
#pragma unroll
    for (int c = 0; c < 8; ++c) X[c] = __builtin_amdgcn_raw_buffer_load_b128(rx, xo, (t1 * 128 + 32 * w4 + 24) * 1024 + ((c + rot1) & 7) * 128, 0);
    xr = *(const GAS u32x4*)((const GAS char*)x + orow * 1024 + cg * 128 + l7 * 16);
    SB();
    if (t1 >= 13) {
      f32x2 a[4] = {};
#pragma unroll
      for (int n = 0; n < 16; ++n) {
        const float cf = (n & 1) ? bf_hi(cv[n >> 3][(n >> 1) & 3]) : bf_lo(cv[n >> 3][(n >> 1) & 3]);
        const float sl = rb(cf * __builtin_amdgcn_rcpf(1.0f + __builtin_amdgcn_exp2f(-1.4426950408889634f * cf)));
#pragma unroll
        for (int e = 0; e < 4; ++e) a[e] += sl * (f32x2){bf_lo(M[n][e]), bf_hi(M[n][e])};
      }
      float v[8] = {a[0][0], a[0][1], a[1][0], a[1][1], a[2][0], a[2][1], a[3][0], a[3][1]};
#pragma unroll
      for (int e = 0; e < 8; ++e) {
        v[e] += dpp<0x128>(v[e]);
        auto r1_ = __builtin_amdgcn_permlane16_swap(__float_as_uint(v[e]), __float_as_uint(v[e]), false, false);
        v[e] = __uint_as_float(r1_[0]) + __uint_as_float(r1_[1]);
        auto r2_ = __builtin_amdgcn_permlane32_swap(__float_as_uint(v[e]), __float_as_uint(v[e]), false, false);
        v[e] = __uint_as_float(r2_[0]) + __uint_as_float(r2_[1]);
      }
      if (lane < 8) {
        *(LAS f32x4*)(L + L_RED + w * 256 + lane * 32) = (f32x4){v[0], v[1], v[2], v[3]};
        *(LAS f32x4*)(L + L_RED + w * 256 + lane * 32 + 16) = (f32x4){v[4], v[5], v[6], v[7]};
      }
      lds_post(lbase + L_MODF + w * 4, 1);
      if (w == 2) {
        lds_wait(L, L_MODF, 1);
        float s = 0.f;
#pragma unroll
        for (int k = 0; k < 4; ++k) s += *(LAS float*)(L + L_RED + k * 256 + lane * 4);
        const float o = dpp<0xB1>(s);
        const u32 tg = __builtin_amdgcn_readfirstlane(ep) + 1u;
        if (!(lane & 1)) __hip_atomic_store((GAS u64*)(ws + WS_MODG) + 32 * mg + (lane >> 1), ((u64)tg << 32) | pk(s, o), RLX_AGENT);
        STAMP(28);
      }
    }
    if (w == 3) {
      u64 pc;
      int len;
      asm volatile("s_getpc_b64 %0\n.Ladaln_pf:\n\ts_mov_b32 %1, .Ladaln_end-.Ladaln_pf" : "=s"(pc), "=s"(len));
      touch(make_srd((const void*)pc, len > 0 ? (u32)len : 0u), (ii * CODE_LINES + (lane < CODE_LINES ? lane : 0)) * 128u, lbase + L_JUNK);
      if (lane < 32) lds_st128(L, L_ONES + (lane & 15) * 16 + (lane >> 4) * 1024, (u32x4){0x3f803f80u, 0x3f803f80u, 0x3f803f80u, 0x3f803f80u});
    }
    tag = __builtin_amdgcn_readfirstlane(ep) + 1u;
    if (w == 3 && lane == 0) *(LAS u32*)(L + L_TAG) = tag;
    BAR();
    STAMP(2);
    float rr, mr;
    {
      float sm = 0.f, sq = 0.f, sm2 = 0.f, sq2 = 0.f;
#pragma unroll
      for (int c = 0; c < 8; ++c) {
        sm = dot2(X[c][0], 0x3f803f80u, sm); sq = dot2(X[c][0], X[c][0], sq); sm2 = dot2(X[c][1], 0x3f803f80u, sm2); sq2 = dot2(X[c][1], X[c][1], sq2);
        sm = dot2(X[c][2], 0x3f803f80u, sm); sq = dot2(X[c][2], X[c][2], sq); sm2 = dot2(X[c][3], 0x3f803f80u, sm2); sq2 = dot2(X[c][3], X[c][3], sq2);
      }
      const float me = sum8(sm + sm2) * (1.f / 512.f);
      float mb, sd;
      ln_scal(me, sum8(sq + sq2) * (1.f / 512.f) - me * me, mb, sd);
      rr = __builtin_amdgcn_rcpf(sd); mr = mb * rr;
      PIN(rr); PIN(mr);
    }
    {
      const u32 vo = (lane >> 2) * 3072u + (lane & 3) * 16u;
      u32 go[4];
#pragma unroll
      for (int e = 0; e < 4; ++e) { const int q = 4 * w + e; go[e] = (q / 3) * 16 * 3072 + (q % 3) * 1024 + 64 * p1; }
#pragma unroll
      for (int c = 0; c < 8; ++c) {
        if (w < 3) {
#pragma unroll
          for (int e = 0; e < 4; ++e) glds(srd, vo, go[e] + ((c + rot1) & 7) * 196608, lbase + wq_base(c) + (4 * w + e) * 1024);
        }
        PIN(X[c]); X[c] = znorm8(X[c], rr, mr); PIN(X[c]);
      }
    }
    if (w == 3) {
      if (lane == 0) __hip_atomic_store((GAS u32*)(ws + WS_ARR) + b, tag, RLX_AGENT);
      u32x4 v[4];
#pragma nounroll
      for (int it = 0; it < SPIN_MAX; ++it) {
        bool ok = true;
#pragma unroll
        for (int k = 0; k < 4; ++k) { v[k] = __builtin_amdgcn_raw_buffer_load_b128(rs, lane * 16, WS_MODG + k * 1024, 16); ok &= v[k][1] == tag && v[k][3] == tag; }
        if (__all(ok)) break;
        __builtin_amdgcn_s_sleep(1);
      }
#pragma unroll
      for (int k = 0; k < 2; ++k) {
        const u32 o = L_C1 + ((lane + 64 * k) >> 1) * 32 + (lane & 1) * 8;
        *(LAS u32x2*)(L + o + 16) = (u32x2){pkh(bf_lo(v[k][0]), bf_hi(v[k][0])), pkh(bf_lo(v[k][2]), bf_hi(v[k][2]))};
        *(LAS u32x2*)(L + o) = (u32x2){pkh(rb(1.f + bf_lo(v[2 + k][0])), rb(1.f + bf_hi(v[2 + k][0]))), pkh(rb(1.f + bf_lo(v[2 + k][2])), rb(1.f + bf_hi(v[2 + k][2])))};
      }
    }
    STAMP(3);
  } else {
#pragma unroll
    for (int c = 0; c < 8; ++c)
#pragma unroll
      for (int g = 0; g < 3; ++g) X[3 * c + g] = __builtin_amdgcn_raw_buffer_load_b128(rx, xo, (t1 * 128 + 32 * w4 + 8 * g) * 1024 + ((c + rot1) & 7) * 128, 0);
    xr = *(const GAS u32x4*)((const GAS char*)x + orow * 1024 + cg * 128 + l7 * 16);
    SB();
    BAR();
    STAMP(2);
    float sm[3] = {}, sq[3] = {};
#pragma unroll
    for (int c = 0; c < 8; ++c) {
      SB();
#pragma unroll
      for (int e = 0; e < 4; ++e)
#pragma unroll
        for (int g = 0; g < 3; ++g) { sm[g] = dot2(X[3 * c + g][e], 0x3f803f80u, sm[g]); sq[g] = dot2(X[3 * c + g][e], X[3 * c + g][e], sq[g]); }
    }
    tag = __builtin_amdgcn_readfirstlane(*(LAS u32*)(L + L_TAG));
    float rr[3], mr[3];
#pragma unroll
    for (int g = 0; g < 3; ++g) {
      const float me = sum8(sm[g]) * (1.f / 512.f);
      float mb, sd;
      ln_scal(me, sum8(sq[g]) * (1.f / 512.f) - me * me, mb, sd);
      rr[g] = __builtin_amdgcn_rcpf(sd); mr[g] = mb * rr[g];
    }
#pragma unroll
    for (int n = 0; n < 24; ++n) { X[n] = znorm8(X[n], rr[n % 3], mr[n % 3]); PIN(X[n]); }
    STAMP(3);
  }
  BAR();
  STAMP(4);
  STAMPC(29);

  u32 cso = L_MISC + l7 * 32;
  PIN(cso);
  const u32x4 srd_g = make_srd(wg, 512 * 3584), srd_u = make_srd(wu, 512 * 3584), srd_d = make_srd(wd, 1792 * 1024), srd_o = make_srd(wo, 512 * 1024);

  u32 guo[7];
  const u32x4 srd_3 = w4 < 2 ? srd_g : srd_u;
  auto load_gu = [&](int c, u32 slot) {
    int r_ = rot4; PINS(r_);
    const u32 so = j4 * 224 + ((c + r_) & 7) * (64 * 3584), ld = lbase + slot + w4 * 1024;
#pragma unroll
    for (int n = 0; n < 7; ++n) glds(n < 3 ? srd_g : n == 3 ? srd_3 : srd_u, guo[n], so, ld + n * 4096);
  };
  const u32 w8o = l3 * 1024u + ((l7 ^ (((lane >> 4) & 1) << 2)) << 4);
  const u32 awE = L_AR1 + (32 * w4 + l3) * 128 + ((l7 ^ l3) << 4), awO = awE ^ 16u;
  u32x4 ca[2], sa[2];
#define CSLOAD(c) do { const u32 o_ = cso + (((c) + rot1) & 7) * 256; ca[(c) & 1] = lds_ld128(L, o_); sa[(c) & 1] = lds_ld128(L, o_ + 16); } while (0)
  if (mw) {
    u32x4 f0, f1, f2, f3, f4, f5;
    f32x16 aq = {}, ak = {}, av = {};
    u32 ard[4];
    {
      const int row = 32 * w4 + r32, f = (row & 7) ^ ((row >> 3) & 1);
#pragma unroll
      for (int s = 0; s < 4; ++s) { ard[s] = L_AR1 + row * 128 + ((((2 * s) | hh) ^ f) << 4); PIN(ard[s]); }
    }
    const u32 wl = (8 * hh + q4) * 64 + 32 * blk + 8 * p4;
    u32 wlh = wl + 60 * KB;
    PIN(wlh);
    bf16x8 hf_[3], qf_[3], kf_[3], vf_[3];
#define P1O(n) (((n) < 20 ? wl + wq_base((n) >> 2) : wlh + (wq_base((n) >> 2) - 60 * KB)) + ((n) & 3) * 3072)
#define P1H(n) hf_[(n) % 3] = asbf(lds_ld128(L, ard[(n) & 3] + ar_off((n) >> 2)))
    CSLOAD(0); CSLOAD(1);
    lds_st128(L, awO + 3072 + ar_off(0), xform8(X[0], ca[0], sa[0]));
    u32x4 hm;
    WAITV_BAR(28);
#pragma unroll
    for (int n = 0; n < 2; ++n) {
      P1H(n); qf_[n] = lds_tr(L, P1O(n), P1O(n) + 256); kf_[n] = lds_tr(L, P1O(n) + 1024, P1O(n) + 1280); vf_[n] = lds_tr(L, P1O(n) + 2048, P1O(n) + 2304);
    }
#pragma unroll
    for (int n = 0; n < 32; ++n) {
      const int m = n + 2;
      const bool bnd = (m & 3) == 0;
      SB();
      ak = MFMA(kf_[n % 3], hf_[n % 3], ak);
      if (m < 32 && !bnd) { P1H(m); kf_[m % 3] = lds_tr(L, P1O(m) + 1024, P1O(m) + 1280); }
      if ((n & 3) < 2 && n < 28) {
        const int cc = n / 4 + 1, hs = n & 1;
        if (!hs) PIN(X[cc & 7]);
        hm[2 * hs] = xform2(X[cc & 7][2 * hs], ca[cc & 1][2 * hs], sa[cc & 1][2 * hs]);
        hm[2 * hs + 1] = xform2(X[cc & 7][2 * hs + 1], ca[cc & 1][2 * hs + 1], sa[cc & 1][2 * hs + 1]);
      }
      SB();
      av = MFMA(hf_[n % 3], vf_[n % 3], av);
      if (m < 32 && bnd) {
        PIN(ak); PIN(av);
        STAMP1(20 + m / 4);
        WAITV_BAR(28 - m);
        P1H(m); kf_[m % 3] = lds_tr(L, P1O(m) + 1024, P1O(m) + 1280);
      }
      if (m < 32) vf_[m % 3] = lds_tr(L, P1O(m) + 2048, P1O(m) + 2304);
      SB();
      aq = MFMA(qf_[n % 3], hf_[n % 3], aq);
      if (m < 32) qf_[m % 3] = lds_tr(L, P1O(m), P1O(m) + 256);
      if ((n & 3) == 1 && n < 28) lds_st128(L, awO + 3072 + ar_off(n / 4 + 1), hm);
      if ((n & 3) == 2 && n < 24) CSLOAD((n + 2) / 4 + 1);
      PIN(ak); PIN(av); PIN(aq);
    }
    STAMP(5);
    STAMPC(30);
    f0 = pack8<0>(ak); f1 = pack8<8>(ak); f2 = pack8<0>(av); f3 = pack8<8>(av);
    const u32 o = WS_KV + ((t1 * 16 + p1) * 4 + w) * 4096 + lane * 16;
    __builtin_amdgcn_raw_buffer_store_b128(f0, rs, o, 0, 16);
    __builtin_amdgcn_raw_buffer_store_b128(f1, rs, o + 1024, 0, 16);
    __builtin_amdgcn_raw_buffer_store_b128(f2, rs, o + 2048, 0, 16);
    __builtin_amdgcn_raw_buffer_store_b128(f3, rs, o + 3072, 0, 16);
    aq *= QSCALE;
    f4 = pack8<0>(aq); f5 = pack8<8>(aq);
    BAR();
    const u32 ok = L_KV + (8 + w) * 4096 + lane * 16, oq = L_Q + w * 2048 + lane * 16;
    lds_st128(L, ok, f0); lds_st128(L, ok + 1024, f1); lds_st128(L, ok + 2048, f2); lds_st128(L, ok + 3072, f3);
    lds_st128(L, oq, f4); lds_st128(L, oq + 1024, f5);
    BAR();
    WAITV(0);
    if (lane == 0) __hip_atomic_store((GAS u32*)(ws + WS_FKV) + (t1 * 16 + p1) * 16 + w, tag, RLX_AGENT);
  } else {
    __builtin_amdgcn_s_setprio(2);
    CSLOAD(0);
#pragma unroll
    for (int c = 0; c < 8; ++c) {
      if (c < 7) CSLOAD(c + 1);
      SB();
#pragma unroll
      for (int g = 0; g < 3; ++g) {
        PIN(X[3 * c + g]);
        lds_st128(L, ((g & 1) ? awO : awE) + g * 1024 + ar_off(c), xform8(X[3 * c + g], ca[c & 1], sa[c & 1]));
      }
      if (c >= 1) STAMP1(19 + c);
      if (c >= 1) asm volatile("s_waitcnt lgkmcnt(3)\n\ts_barrier" ::: "memory");
    }
    BAR();
    __builtin_amdgcn_s_setprio(0);
#pragma unroll
    for (int n = 0; n < 7; ++n) {
      const int pc = w4 + 4 * n, up = pc >= 14, e = (pc - 14 * up) * 64 + lane, rho = e / 14, cc = e - 14 * rho;
      guo[n] = (u32)((up ? (rho ^ 4) : rho) * 3584 + cc * 16);
    }
    STAMP(5);
    if (w4 < 3) {
      const u32x4 srd_a = w4 == 0 ? srd_o : w4 == 1 ? srd_g : srd_u;
      const int e1 = 64 + lane;
      const u32 ta0 = w4 ? (16 * ii + lane / 7) * 3584 + (xcd & 3) * 896 + (lane % 7) * 128 : ii * 16384 + lane * 128;
      const u32 ta1 = w4 ? (lane < 48 ? (16 * ii + e1 / 7) * 3584 + (xcd & 3) * 896 + (e1 % 7) * 128 : 0x7fffffffu) : ta0 + 8192;
      touch(srd_a, ta0, lbase + L_JUNK);
      touch(srd_a, ta1, lbase + L_JUNK);
    }
    BAR();
    BAR();
  }
  STAMP(6);

  {
    const int qb = mw ? w : 7 - w, hd = w >> 2;
    const bf16x8 qf = asbf(lds_ld128(L, L_Q + qb * 2048 + hd * 1024 + lane * 16));
    const u32 ka = L_KV + hd * 1024 + lane * 16;
    const bool vdat = (r32 >> 4) == hd;
    const u32 va = vdat ? L_KV + 2048 + lane * 16 : L_ONES + (lane & 15) * 16;
    const u32 vs = vdat ? 4096u : 0u;
    f32x16 cm;
#pragma unroll
    for (int g = 0; g < 16; ++g) cm[g] = (g & 3) + 8 * (g >> 2) + 4 * hh <= r32 ? 0.f : NEGBIG;
    const f32x16 zero = {};
    f32x16 O = {};
    float m = NEGBIG;
    auto rowmax = [&](const f32x16& s, float mm) {
#pragma unroll
      for (int g = 0; g < 16; g += 2) mm = fmaxf(fmaxf(mm, s[g]), s[g + 1]);
      return mm;
    };
    auto ldk = [&](int lb) { return asbf(lds_ld128(L, ka + lb * 4096)); };
#pragma nounroll
    for (int st = 0; st < 2; ++st) {
      int lm = 8 + qb, lo = 8, hi = 8 + qb;
      if (st) {
        STAMP(7);
        if (!mw) {
          for (int n = w4 * 16; n < w4 * 16 + 16; ++n) glds(srd_o, w8o, cg * 128 + n * 8192, lbase + L_WO + n * 1024);
          load_gu(0, gu_slot(0));
        }
        if (t1 == 0) break;
        if (w < 2) {
          const int tp = t1 - 2 + w;
          u32x4 nb[16] = {};
          if (tp >= 0) {
            wait_flags((const GAS u32*)(ws + WS_FKV) + (tp * 16 + p1) * 16 + (lane & 3), tag);
            const u32 o = WS_KV + (tp * 16 + p1) * 16384 + lane * 16;
#pragma unroll
            for (int n = 0; n < 16; ++n) nb[n] = __builtin_amdgcn_raw_buffer_load_b128(rs, o + n * 1024, 0, 16);
          }
#pragma unroll
          for (int n = 0; n < 16; ++n) lds_st128(L, L_KV + w * 16384 + n * 1024 + lane * 16, nb[n]);
        }
#pragma unroll
        for (int g = 0; g < 16; ++g) { float u = t1 == 1 ? NEGBIG : NEGBIG - cm[g]; asm("" : "+v"(u)); cm[g] = u; }
        lm = qb; lo = t1 == 1 ? 4 : qb + 1; hi = 8;
        BAR();
        STAMP(8);
      }
      bf16x8 kA = ldk(lo), kB = ldk(lo + 1);
      f32x16 sA = MFMA(ldk(lm), qf, cm), sB, sC;
      const bf16x8 k0 = kA;
      float mn = m;
      for (int lb = lo; lb < hi; lb += 2) {
        sB = MFMA(kA, qf, zero);
        sC = MFMA(kB, qf, zero);
        kA = ldk(lb + 2); kB = ldk(lb + 3);
        SB();
        mn = rowmax(sB, mn);
        if (lb + 1 < hi) mn = rowmax(sC, mn);
      }
      mn = rowmax(sA, mn);
      {
        auto r_ = __builtin_amdgcn_permlane32_swap(__float_as_uint(mn), __float_as_uint(mn), false, false);
        mn = fmaxf(__uint_as_float(r_[0]), __uint_as_float(r_[1]));
      }
      u32 vv = va + lm * vs;
      bf16x8 v0 = asbf(lds_ld128(L, vv)), v1 = asbf(lds_ld128(L, vv + 1024));
      f32x16 negm;
#pragma unroll
      for (int g = 0; g < 16; ++g) negm[g] = -mn;
#pragma unroll
      for (int g = 0; g < 16; ++g) { float u = sA[g] - mn; asm("" : "+v"(u)); sA[g] = u; }
      if (st) {
        const float al = __builtin_amdgcn_exp2f(m - mn);
#pragma unroll
        for (int g = 0; g < 16; ++g) { float o = O[g] * al; asm("" : "+v"(o)); O[g] = o; }
      }
      m = mn;
      kA = k0;
      vv = va + lo * vs;
#define P2STEP(CUR, NXT)                                                                      \
  {                                                                                           \
    NXT = MFMA(kA, qf, negm);                                                                 \
    PIN(NXT);                                                                                 \
    kA = ldk(lb + 1);                                                                         \
    const bf16x8 n0_ = asbf(lds_ld128(L, vv)), n1_ = asbf(lds_ld128(L, vv + 1024));           \
    vv += vs;                                                                                 \
    SB();                                                                                     \
    _Pragma("unroll") for (int g = 0; g < 8; ++g) CUR[g] = __builtin_amdgcn_exp2f(CUR[g]);    \
    O = MFMA(v0, asbf(pack8<0>(CUR)), O);                                                     \
    _Pragma("unroll") for (int g = 8; g < 16; ++g) CUR[g] = __builtin_amdgcn_exp2f(CUR[g]);   \
    O = MFMA(v1, asbf(pack8<8>(CUR)), O);                                                     \
    SB();                                                                                     \
    v0 = n0_; v1 = n1_;                                                                       \
  }
      for (int lb = lo;; ++lb) {
        P2STEP(sA, sB);
        if (lb >= hi) break;
        ++lb;
        P2STEP(sB, sA);
        if (lb >= hi) break;
      }
    }
    u32x4 of;
    {
      const float inv = __builtin_amdgcn_rcpf(hd ? O[0] : O[8]);
      float o[8];
#pragma unroll
      for (int g = 0; g < 8; ++g) { o[g] = (hd ? O[8 + g] : O[g]) * inv; asm("" : "+v"(o[g])); }
      of = (u32x4){pk(o[0], o[1]), pk(o[2], o[3]), pk(o[4], o[5]), pk(o[6], o[7])};
    }
    __builtin_amdgcn_raw_buffer_store_b128(of, rs, WS_O + ((t1 * 4 + qb) * 32 + 2 * p1 + hd) * 1024 + lane * 16, 0, 16);
    WAITV(0);
    if (lane == 0) __hip_atomic_store((GAS u32*)(ws + WS_FO) + (t1 * 4 + qb) * 32 + 2 * p1 + hd, tag, RLX_AGENT);
  }
  STAMP(9);
  {
    if (t1 == 0 && p1 == 0 && w == 7) {
#pragma nounroll
      for (int it = 0; it < SPIN_MAX; ++it) {
        bool ok = true;
#pragma unroll
        for (int k = 0; k < 4; ++k) ok &= __hip_atomic_load((const GAS u32*)(ws + WS_ARR) + lane + 64 * k, RLX_AGENT) == tag;
        if (__all(ok)) break;
        __builtin_amdgcn_s_sleep(1);
      }
      if (lane == 0) __hip_atomic_store((GAS u32*)(ws + WS_EPOCH), tag, RLX_AGENT);
    }
  }
  BAR();

  const u32 wt0 = (4 * hh + q4) * 128 + ((32 * blk + 8 * p4) ^ ((q4 >> 1) << 6)), wt1 = wt0 ^ 64u;
  auto red_write = [&](u32 base, u32 S8, const f32x16& a0, const f32x16& a1) {
    u32 ln = lane; PIN(ln);
    const u32 o = base + ((ln & 31) >> 3) * S8 + (ln & 7) * 256, sw = (ln & 7) ^ (ln >> 5);
#pragma unroll
    for (int g = 0; g < 4; ++g) {
      *(LAS f32x4*)(L + o + (((2 * g) ^ sw) << 4)) = (f32x4){a0[4 * g], a0[4 * g + 1], a0[4 * g + 2], a0[4 * g + 3]};
      *(LAS f32x4*)(L + o + (((2 * g) ^ sw) << 4) + 128) = (f32x4){a1[4 * g], a1[4 * g + 1], a1[4 * g + 2], a1[4 * g + 3]};
    }
  };
  auto red_read = [&](u32 base, u32 S8, u32 SK, f32x4& lo, f32x4& hi) {
    u32 ln = lane; PIN(ln);
    const u32 o0 = base + w * S8 + (ln >> 3) * 256 + (((2 * (ln & 7)) ^ (ln >> 3)) << 4), o1 = o0 ^ 16u;
    lo = lds_ldf4(L, o0); hi = lds_ldf4(L, o1);
#pragma unroll
    for (int k = 1; k < 4; ++k) { lo += lds_ldf4(L, o0 + k * SK); hi += lds_ldf4(L, o1 + k * SK); }
  };
  u32x4 x1p;
  {
    if (mw) {
      bf16x8 wa[8], wb[8];
#pragma unroll
      for (int i = 0; i < 8; ++i) {
        const u32 o = L_WO + w * 16384 + i * 2048;
        wa[i] = lds_tr(L, o + wt0, o + wt0 + 1024); wb[i] = lds_tr(L, o + wt1, o + wt1 + 1024);
      }
      wait_flags((const GAS u32*)(ws + WS_FO) + (t3 * 4 + 2 * hf + (l3 & 1)) * 32 + 8 * w + l7, tag);
      STAMP(10);
      u32x4 of0[8], of1[8];
      const u32 oo = WS_O + ((t3 * 4 + 2 * hf) * 32 + 8 * w) * 1024 + lane * 16;
#pragma unroll
      for (int i = 0; i < 8; ++i) {
        of0[i] = __builtin_amdgcn_raw_buffer_load_b128(rs, oo + i * 1024, 0, 16);
        of1[i] = __builtin_amdgcn_raw_buffer_load_b128(rs, oo + 32768 + i * 1024, 0, 16);
      }
      SB();
      f32x16 a00 = {}, a01 = {}, a10 = {}, a11 = {};
#pragma unroll
      for (int i = 0; i < 8; ++i) {
        a00 = MFMA(wa[i], asbf(of0[i]), a00); a01 = MFMA(wb[i], asbf(of0[i]), a01);
        a10 = MFMA(wa[i], asbf(of1[i]), a10); a11 = MFMA(wb[i], asbf(of1[i]), a11);
      }
      STAMP(11);
      red_write(L_WO + w * 16384, 2048, a00, a01);
      red_write(L_WO + w * 16384 + 8192, 2048, a10, a11);
    } else {
      u32x4 q[4];
      {
        u32 v[3];
        const GAS u64* g = (const GAS u64*)(ws + WS_MODG);
        const u32 qo = (((r32 & 1) ? 768 : 1024) + rot4 * 32 + (t4 >> 3) * 16 + 4 * hh) * 8;
#pragma nounroll
        for (int it = 0; it < SPIN_MAX; ++it) {
          const u64 x1_ = __hip_atomic_load(g + 1024 + (tid - 256), RLX_AGENT);
          const u64 x2 = __hip_atomic_load(g + (lane < 32 ? 512 : 1248) + cg * 32 + lane, RLX_AGENT);
          bool ok = (u32)(x1_ >> 32) == tag && (u32)(x2 >> 32) == tag;
#pragma unroll
          for (int k = 0; k < 4; ++k) { q[k] = __builtin_amdgcn_raw_buffer_load_b128(rs, qo + (k >> 1) * 64 + (k & 1) * 16, WS_MODG, 16); ok &= q[k][1] == tag && q[k][3] == tag; }
          v[1] = (u32)x1_; v[2] = (u32)x2;
          if (__all(ok)) break;
          __builtin_amdgcn_s_sleep(1);
        }
        *(LAS f32x2*)(L + L_C1B + (tid - 256) * 8) = (f32x2){rb(1.f + bf_lo(v[1])), rb(1.f + bf_hi(v[1]))};
        if (w == 4) *(LAS f32x2*)(L + L_GA + lane * 8) = (f32x2){bf_lo(v[2]), bf_hi(v[2])};
      }
      STAMP(10);
      {
        const u32 gl = blk * 14336 + (8 * hh + q4) * 224 + 8 * p4 + w4 * 32 + (t4 >> 3) * 7168;
        const u32 g0 = gl + (blk ? 896u : 0u), g1 = gl + (blk ? 0u : 896u);
        const float one = (r32 & 1) ? 0.f : 1.f;
        f32x16 a0 = {}, a1 = {};
#pragma unroll
        for (int s = 0; s < 2; ++s) {
          const u32x4 bv = {pk(one + bf_lo(q[2 * s][0]), one + bf_hi(q[2 * s][0])), pk(one + bf_lo(q[2 * s][2]), one + bf_hi(q[2 * s][2])),
                            pk(one + bf_lo(q[2 * s + 1][0]), one + bf_hi(q[2 * s + 1][0])), pk(one + bf_lo(q[2 * s + 1][2]), one + bf_hi(q[2 * s + 1][2]))};
          a0 = MFMA(lds_tr(L, g0 + s * 3584, g1 + s * 3584), asbf(bv), a0);
          a1 = MFMA(lds_tr(L, g0 + s * 3584 + 128, g1 + s * 3584 + 128), asbf(bv), a1);
        }
        if (r32 < 2) {
          const u32 o = WS_UV + (j4 * 16 + t4) * 2048 + w4 * 256 + r32 * 128 + hh * 16;
#pragma unroll
          for (int g = 0; g < 4; ++g) {
            __builtin_amdgcn_raw_buffer_store_b128(__builtin_bit_cast(u32x4, (f32x4){a0[4 * g], a0[4 * g + 1], a0[4 * g + 2], a0[4 * g + 3]}), rs, o + g * 32, 0, 16);
            if (w4 < 3) __builtin_amdgcn_raw_buffer_store_b128(__builtin_bit_cast(u32x4, (f32x4){a1[4 * g], a1[4 * g + 1], a1[4 * g + 2], a1[4 * g + 3]}), rs, o + 1024 + g * 32, 0, 16);
          }
        }
      }
      WAITV(0);
      if (lane == 0) __hip_atomic_store((GAS u32*)(ws + WS_FUV) + j4 * 64 + t4 * 4 + w4, tag, RLX_AGENT);
      load_gu(1, gu_slot(1));
      load_gu(2, gu_slot(2));
      STAMP(11);
    }
    BAR();
    f32x4 lo, hi;
    red_read(L_WO, 2048, 16384, lo, hi);
    const f32x4 ga0 = lds_ldf4(L, cso + M_GA), ga1 = lds_ldf4(L, cso + M_GA + 16);
    float v[8];
    v[0] = rb(bf_lo(xr[0]) + rb(ga0[0] * rb(lo[0]))); v[1] = rb(bf_hi(xr[0]) + rb(ga0[1] * rb(lo[1])));
    v[2] = rb(bf_lo(xr[1]) + rb(ga0[2] * rb(lo[2]))); v[3] = rb(bf_hi(xr[1]) + rb(ga0[3] * rb(lo[3])));
    v[4] = rb(bf_lo(xr[2]) + rb(ga1[0] * rb(hi[0]))); v[5] = rb(bf_hi(xr[2]) + rb(ga1[1] * rb(hi[1])));
    v[6] = rb(bf_lo(xr[3]) + rb(ga1[2] * rb(hi[2]))); v[7] = rb(bf_hi(xr[3]) + rb(ga1[3] * rb(hi[3])));
    x1p = (u32x4){pk(v[0], v[1]), pk(v[2], v[3]), pk(v[4], v[5]), pk(v[6], v[7])};
    {
      const f32x4 c0 = lds_ldf4(L, cso + M_C1B + cg * 256), c1 = lds_ldf4(L, cso + M_C1B + cg * 256 + 16);
      const u32x4 y = {pk(v[0] * c0[0], v[1] * c0[1]), pk(v[2] * c0[2], v[3] * c0[3]), pk(v[4] * c1[0], v[5] * c1[1]), pk(v[6] * c1[2], v[7] * c1[3])};
      __builtin_amdgcn_raw_buffer_store_b128(y, rs, WS_X1 + ((t3 * 4 + 2 * hf + (w >> 2)) * 32 + 4 * cg + (l7 >> 1)) * 1024 + ((l7 & 1) * 32 + 8 * w4 + l3) * 16, 0, 16);
    }
    const float sm = sum8(((v[0] + v[1]) + (v[2] + v[3])) + ((v[4] + v[5]) + (v[6] + v[7])));
    const float mu = sm * (1.f / 64.f);
    float m2 = 0.f;
#pragma unroll
    for (int k = 0; k < 8; ++k) m2 = fmaf(v[k] - mu, v[k] - mu, m2);
    m2 = sum8(m2);
    if (l7 == 0) __hip_atomic_store((GAS u64*)(ws + WS_ST) + orow * 8 + cg, ((u64)__float_as_uint(m2) << 32) | __float_as_uint(sm), RLX_AGENT);
    WAITV(0);
    if (lane == 0) __hip_atomic_store((GAS u32*)(ws + WS_FX) + (t3 * 4 + 2 * hf + (w >> 2)) * 32 + cg * 4 + w4, tag, RLX_AGENT);
  }
  STAMP(12);
  BAR();

  if (mw) {
    u32 ln = lane; PIN(ln);
    const u32 bk = (ln >> 4) & 1, gl = bk * 14336 + (8 * (ln >> 5) + ((ln & 15) >> 2)) * 224 + 8 * (ln & 3);
    const u32 g0 = gl + (bk ? 896u : 0u), g1 = gl + (bk ? 0u : 896u);
    u32 g0h = g0 + 92 * KB, g1h = g1 + 92 * KB;
    PIN(g0h); PIN(g1h);
    f32x16 ag[7] = {};
    bf16x8 wf_[7];
    u32x4 yf[32];
#define P4HI(n) (gu_slot(((n) >> 2) & 3) >= 92 * KB)
#define P4O(n) (gu_slot(((n) >> 2) & 3) - (P4HI(n) ? 92 * KB : 0) + ((n) & 3) * 3584)
#define P4W(n, f) wf_[f] = lds_tr(L, (P4HI(n) ? g0h : g0) + P4O(n) + (f) * 32, (P4HI(n) ? g1h : g1) + P4O(n) + (f) * 32)
#pragma unroll
    for (int f = 0; f < 7; ++f) P4W(0, f);
    wait_flags((const GAS u32*)(ws + WS_FX) + (t4 * 4 + w) * 32 + (ln & 31), tag);
    STAMP(13);
    auto ld_y = [&](int n) { yf[n] = __builtin_amdgcn_raw_buffer_load_b128(rs, ln * 16, WS_X1 + ((t4 * 4 + w) * 32 + ((n + 4 * rot4) & 31)) * 1024, 16); };
#pragma unroll
    for (int n = 0; n < 5; ++n) ld_y(n);
    const u32 a_done = lbase + L_DONE + w * 4;
    u32x4 land;
    static_for<0, 32>([&](auto nc) {
      constexpr int n = nc.value, m = n + 1;
      if constexpr (n + 2 >= 23 && n + 2 < 32) yf[n + 2] = lds_ld128(L, L_YL + (w * 9 + (n + 2 - 23)) * 1024 + ln * 16);
#pragma unroll
      for (int f = 0; f < 7; ++f) {
        SB();
        ag[f] = MFMA(wf_[f], asbf(yf[n]), ag[f]);
        if ((m & 3) == 0 && f == 0) {
          STAMP4(19 + m / 4);
          lds_post(a_done, m / 4);
          if (m >= 12) { PIN(ag[0]); if (!all_ge(land, m / 4)) lds_wait(L, L_LAND, m / 4); }
        }
        if ((m & 3) == 3 && m >= 11 && f == 0) land = *(volatile LAS u32x4*)(L + L_LAND);
        if (m < 32) P4W(m, f);
        if constexpr (n < 6) { if (f & 1) ld_y(5 + 3 * n + (f >> 1)); }
      }
#pragma unroll
      for (int f = 0; f < 7; ++f) PIN(ag[f]);
    });
    const u32x4 bst = lds_ld128(L, L_BST + (32 * w + (ln & 31)) * 16);
    const u32x4 bfr = {ln < 32 ? bst[0] : 0u, ln < 32 ? bst[1] : 0u, 0u, 0u};
#pragma unroll
    for (int f = 0; f < 7; ++f) {
      const u32x2 uv = *(LAS u32x2*)(L + L_UVT + (f * 32 + (ln & 31)) * 8);
      ag[f] = MFMA(asbf((u32x4){uv[0], uv[1], 0u, 0u}), asbf(bfr), ag[f]);
    }
    const float rs_ = __uint_as_float(bst[2]), sd_ = bf_lo(bst[1]);
    const float k1 = -2.0f * 0.7978845608028654f * 1.4426950408889634f * rs_, k3 = k1 * 0.044715f * rs_ * rs_, i2 = sd_ * sd_;
    STAMP(15);
    PIN(ln);
    const u32 ao = WS_ACT + ((t4 * 4 + w) * 112 + 7 * j4) * 1024 + ln * 16;
#pragma unroll
    for (int f = 0; f < 7; ++f) {
      u32x4 o;
#pragma unroll
      for (int k = 0; k < 4; ++k) { const f32x2 a = geglu2((f32x2){ag[f][2 * k], ag[f][2 * k + 1]}, (f32x2){ag[f][8 + 2 * k], ag[f][9 + 2 * k]}, k1, k3, i2); o[k] = pk(a[0], a[1]); }
      __builtin_amdgcn_raw_buffer_store_b128(o, rs, ao + f * 1024, 0, 16);
    }
    WAITV(0);
    if (ln == 0) __hip_atomic_store((GAS u32*)(ws + WS_FA) + (t4 * 4 + w) * 32 + j4, tag, RLX_AGENT);
    STAMP(16);
  } else {
    auto load_wd = [&](int i, int li) {
      int ks = rot5; PINS(ks);
      ks += i; ks -= ks >= 28 ? 28 : 0;
      const u32 so = cg * 128 + (w4 * 448 + ks * 16) * 1024, ld = lbase + wd_base(li) + w4 * 2048;
      glds(srd_d, w8o, so, ld);
      glds(srd_d, w8o, so + 8192, ld + 1024);
    };
    load_gu(3, gu_slot(3));
    {
      wait_flags((const GAS u32*)(ws + WS_FX) + (t4 * 4 + w4) * 32 + r32, tag);
      lds_post(lbase + L_LAND + w4 * 4, 3);
      STAMP(13);
      const u32 so = (t4 * 128 + 32 * w4 + r32) * 64 + hh * 32;
      const u32x4 s0 = __builtin_amdgcn_raw_buffer_load_b128(rs, so, WS_ST, 16), s1 = __builtin_amdgcn_raw_buffer_load_b128(rs, so + 16, WS_ST, 16);
      u32x4 yl[9];
#pragma unroll
      for (int k = 0; k < 9; ++k) yl[k] = __builtin_amdgcn_raw_buffer_load_b128(rs, lane * 16, WS_X1 + ((t4 * 4 + w4) * 32 + ((23 + k + 4 * rot4) & 31)) * 1024, 16);
      SB();
      auto xsum = [&](float v) {
        auto r_ = __builtin_amdgcn_permlane32_swap(__float_as_uint(v), __float_as_uint(v), false, false);
        return __uint_as_float(r_[0]) + __uint_as_float(r_[1]);
      };
      const float a0 = __uint_as_float(s0[0]), a1 = __uint_as_float(s0[2]), a2 = __uint_as_float(s1[0]), a3 = __uint_as_float(s1[2]);
      const float me = xsum((a0 + a1) + (a2 + a3)) * (1.f / 512.f);
      const float d0 = a0 * (1.f / 64.f) - me, d1 = a1 * (1.f / 64.f) - me, d2 = a2 * (1.f / 64.f) - me, d3 = a3 * (1.f / 64.f) - me;
      const float m2 = (__uint_as_float(s0[1]) + __uint_as_float(s0[3])) + (__uint_as_float(s1[1]) + __uint_as_float(s1[3]));
      float mb, sd;
      ln_scal(me, xsum(m2 + 64.f * ((d0 * d0 + d1 * d1) + (d2 * d2 + d3 * d3))) * (1.f / 512.f), mb, sd);
      if (lane < 32) lds_st128(L, L_BST + (32 * w4 + lane) * 16, (u32x4){pk(-mb, -mb), pk(sd, sd), __float_as_uint(__builtin_amdgcn_rcpf(sd)), 0u});
#pragma unroll
      for (int k = 0; k < 9; ++k) lds_st128(L, L_YL + (w4 * 9 + k) * 1024 + lane * 16, yl[k]);
    }
    {
      const u32 a_land = lbase + L_LAND + w4 * 4;
      STAMP4(20);
      lds_wait(L, L_DONE, 1); load_gu(4, gu_slot(0)); STAMP4(21);
      lds_wait(L, L_DONE, 2); load_gu(5, gu_slot(1)); WAITV(7); lds_post(a_land, 4); STAMP4(22);
      lds_wait(L, L_DONE, 3); load_gu(6, gu_slot(2)); WAITV(7); lds_post(a_land, 5); STAMP4(23);
      lds_wait(L, L_DONE, 4); load_gu(7, gu_slot(3)); WAITV(7); lds_post(a_land, 6); WAITV(0); lds_post(a_land, 7); STAMP4(24);
      wait_flags((const GAS u32*)(ws + WS_FUV) + j4 * 64 + lane, tag);
      {
        u32x4 pt[16];
        const u32 o = lane < 28 ? (w4 * 28 + lane) * 16 : 0x7fffffffu;
#pragma unroll
        for (int k = 0; k < 16; ++k) pt[k] = __builtin_amdgcn_raw_buffer_load_b128(rs, o, WS_UV + (j4 * 16 + k) * 2048, 16);
        SB();
        f32x4 a = {};
#pragma unroll
        for (int k = 0; k < 16; ++k) {
          const f32x4 t_ = __builtin_bit_cast(f32x4, pt[k]);
#pragma unroll
          for (int e = 0; e < 4; ++e) { float y_ = a[e] + t_[e]; asm("" : "+v"(y_)); a[e] = y_; }
        }
        if (lane < 28) {
          const u32 vi = (w4 * 28 + lane) * 4;
#pragma unroll
          for (int e = 0; e < 4; ++e) {
            const float hi = rb(a[e]);
            *(LAS u32*)(L + L_UVT + ((vi >> 6) * 32 + (vi & 31) + e) * 8 + ((vi >> 5) & 1) * 4) = pk(hi, a[e] - hi);
          }
        }
      }
      lds_post(a_land, 8);
      lds_wait(L, L_DONE, 5); load_wd(0, 0); load_wd(1, 1); load_wd(2, 2); STAMP4(25);
      lds_wait(L, L_DONE, 6); load_wd(3, 3); load_wd(4, 4); load_wd(5, 5);
      lds_wait(L, L_DONE, 7); load_wd(6, 6); load_wd(7, 7); load_wd(8, 8); load_wd(9, 9);
      lds_wait(L, L_DONE, 8);
    }
    STAMP(15);
    for (int i = 10; i < 18; ++i) load_wd(i, i);
    WAITV(16);
    bf16x8 wp0[10], wp1[10];
    u32 dA0 = w4 * 2048 + wt0, dA1 = w4 * 2048 + wt1, dB0 = dA0 + 92 * KB, dB1 = dA1 + 92 * KB, dC0 = dA0 + 28 * KB, dC1 = dA1 + 28 * KB;
    PIN(dA0); PIN(dA1); PIN(dB0); PIN(dB1); PIN(dC0); PIN(dC1);
#define WD0(li) ((li) < 3 ? dA0 + (li) * 8192 : (li) < 10 ? dB0 + ((li) - 3) * 8192 : dC0 + ((li) - 10) * 8192)
#define WD1(li) ((li) < 3 ? dA1 + (li) * 8192 : (li) < 10 ? dB1 + ((li) - 3) * 8192 : dC1 + ((li) - 10) * 8192)
#pragma unroll
    for (int i = 0; i < 10; ++i) { wp0[i] = lds_tr(L, WD0(i), WD0(i) + 1024); wp1[i] = lds_tr(L, WD1(i), WD1(i) + 1024); }
    asm volatile("s_waitcnt lgkmcnt(0)" ::: "memory");
    for (int i = 0; i < 10; ++i) load_wd(18 + i, i);
    STAMP(16);
    wait_flags((const GAS u32*)(ws + WS_FA) + (t3 * 4 + 2 * hf + ((lane >> 2) & 1)) * 32 + 4 * w4 + (lane & 3), tag);
    STAMP(17);
    const u32 ao = WS_ACT + ((t3 * 4 + 2 * hf) * 112 + 28 * w4) * 1024 + lane * 16;
    u32x4 af0[28], af1[28];
    auto ld_act = [&](int i) {
      int ks = i + rot5; ks -= ks >= 28 ? 28 : 0;
      af0[i] = __builtin_amdgcn_raw_buffer_load_b128(rs, ao, ks * 1024, 16);
      af1[i] = __builtin_amdgcn_raw_buffer_load_b128(rs, ao + 112 * 1024, ks * 1024, 16);
    };
#pragma unroll
    for (int i = 0; i < 7; ++i) ld_act(i);
    f32x16 a00 = {}, a01 = {}, a10 = {}, a11 = {};
#pragma unroll
    for (int i = 0; i < 28; ++i) {
      SB();
      a00 = MFMA(wp0[i % 10], asbf(af0[i]), a00);
      a01 = MFMA(wp1[i % 10], asbf(af0[i]), a01);
      SB();
      a10 = MFMA(wp0[i % 10], asbf(af1[i]), a10);
      if (i < 5) { ld_act(7 + 2 * i); ld_act(8 + 2 * i); }
      else if (i + 12 < 28) ld_act(i + 12);
      SB();
      a11 = MFMA(wp1[i % 10], asbf(af1[i]), a11);
      if (i >= 7 && i + 3 < 28) {
        const int li = i + 3 < 18 ? i + 3 : i - 15;
        wp0[(i + 3) % 10] = lds_tr(L, WD0(li), WD0(li) + 1024);
        wp1[(i + 3) % 10] = lds_tr(L, WD1(li), WD1(li) + 1024);
      }
      PIN(a00); PIN(a01); PIN(a10); PIN(a11);
    }
    STAMP(18);
    red_write(28 * KB + w4 * 2048, 8192, a00, a01);
    red_write(28 * KB + w4 * 2048 + 32768, 8192, a10, a11);
  }
  BAR();
  {
    f32x4 lo, hi;
    red_read(28 * KB, 8192, 2048, lo, hi);
    u32 ln = lane; PIN(ln);
    const f32x4 gb0 = lds_ldf4(L, L_MISC + M_GB + (ln & 7) * 32), gb1 = lds_ldf4(L, L_MISC + M_GB + (ln & 7) * 32 + 16);
    u32x4 o;
    o[0] = pk(bf_lo(x1p[0]) + rb(gb0[0] * rb(lo[0])), bf_hi(x1p[0]) + rb(gb0[1] * rb(lo[1])));
    o[1] = pk(bf_lo(x1p[1]) + rb(gb0[2] * rb(lo[2])), bf_hi(x1p[1]) + rb(gb0[3] * rb(lo[3])));
    o[2] = pk(bf_lo(x1p[2]) + rb(gb1[0] * rb(hi[0])), bf_hi(x1p[2]) + rb(gb1[1] * rb(hi[1])));
    o[3] = pk(bf_lo(x1p[3]) + rb(gb1[2] * rb(hi[2])), bf_hi(x1p[3]) + rb(gb1[3] * rb(hi[3])));
    *(GAS u32x4*)((GAS char*)out + (t3 * 128 + hf * 64 + 8 * w) * 1024 + cg * 128 + (ln >> 3) * 1024 + (ln & 7) * 16) = o;
  }
  STAMP(19);
  asm volatile(".Ladaln_end:");
}

extern "C" void kernel_launch(void* const* d_in, const int* in_sizes, int n_in, void* d_out, int out_size, void* d_ws, size_t ws_size,
                              hipStream_t stream) {
  adaln_block<<<256, 512, 0, stream>>>((const u16*)d_in[0], (const u16*)d_in[1], (const u16*)d_in[2], (const u16*)d_in[4], (const u16*)d_in[5],
                                       (const u16*)d_in[6], (const u16*)d_in[7], (const u16*)d_in[8], (u16*)d_out, (char*)d_ws);
}
```

```cpp
#define V_TLB 65536
#include <hip/hip_runtime.h>
#include <stdint.h>

typedef unsigned short u16;
typedef unsigned int u32;
typedef unsigned long long u64;
typedef __attribute__((ext_vector_type(8))) __bf16 bf16x8;
typedef __attribute__((ext_vector_type(2))) __bf16 bf16x2;
typedef __attribute__((ext_vector_type(4))) short s16x4;
typedef __attribute__((ext_vector_type(8))) short s16x8;
typedef __attribute__((ext_vector_type(16))) float f32x16;
typedef __attribute__((ext_vector_type(4))) float f32x4;
typedef __attribute__((ext_vector_type(2))) float f32x2;
typedef __attribute__((ext_vector_type(4))) u32 u32x4;
typedef __attribute__((ext_vector_type(2))) u32 u32x2;
#define LAS __attribute__((address_space(3)))
#define GAS __attribute__((address_space(1)))
#define DI __device__ __forceinline__
#define RLX_AGENT __ATOMIC_RELAXED, __HIP_MEMORY_SCOPE_AGENT

constexpr u32 WS_EPOCH = 0;
constexpr u32 WS_ARR = 1024;
constexpr u32 WS_MODG = 4096;
constexpr u32 WS_FKV = 16384;
constexpr u32 WS_FO = WS_FKV + 256 * 64;
constexpr u32 WS_FX = WS_FO + 64 * 128;
constexpr u32 WS_FA = WS_FX + 64 * 128;
constexpr u32 WS_FUV = WS_FA + 64 * 128;
constexpr u32 WS_ST = 65536;
constexpr u32 WS_UV = 196608;
constexpr u32 WS_KV = 1048576;
constexpr u32 WS_O = WS_KV + 4194304;
constexpr u32 WS_X1 = WS_O + 2097152;
constexpr u32 WS_ACT = WS_X1 + 2097152;
constexpr u32 WS_DBG = WS_ACT + 7340032;
constexpr u32 WS_BYTES = WS_DBG + 1048576;
static_assert(WS_FUV + 16 * 256 <= WS_ST && WS_UV + 256 * 2048 <= WS_KV, "workspace map");

constexpr int SPIN_MAX = 1 << 17;
constexpr u32 KB = 1024;
constexpr u32 L_WO = 28 * KB;
constexpr u32 L_KV = 92 * KB;
constexpr u32 L_Q = 140 * KB;
constexpr u32 L_AR1 = 72 * KB;
constexpr u32 ar_off(int c) { return c % 3 == 0 ? 0u : c % 3 == 1 ? 44 * KB : 60 * KB; }
constexpr u32 L_YL = 56 * KB;
constexpr u32 L_MISC = 148 * KB;
constexpr u32 L_C1 = L_MISC, L_SH = L_MISC + 2048, L_C1B = L_MISC + 4096, L_SHB = L_MISC + 6144, M_C1B = 4096;
constexpr u32 L_UVT = L_C1, L_BST = L_SH;
constexpr u32 L_ONES = L_MISC + 8192;
constexpr u32 L_GA = L_ONES + 256, M_GA = 8192 + 256, M_GB = 8192 + 512;
constexpr u32 L_DONE = L_ONES + 784, L_LAND = L_ONES + 800, L_MODF = L_ONES + 816;
constexpr u32 L_TAG = L_ONES + 768, L_JUNK = L_ONES + 1536;
constexpr u32 L_RED = L_MISC + 10240;
constexpr u32 LDS_BYTES = 163840;
constexpr u32 wq_base(int c) { return c < 6 ? c * 12288u : 92 * KB + (c - 6) * 12288u; }
constexpr u32 gu_slot(int s) { return s == 0 ? 0u : s == 1 ? 92 * KB : s == 2 ? 120 * KB : 28 * KB; }
constexpr u32 wd_base(int i) { return i < 3 ? i * 8192u : i < 10 ? 92 * KB + (i - 3) * 8192u : 28 * KB + (i - 10) * 8192u; }
constexpr u32 CODE_LINES = 16;

constexpr float NEGBIG = -1e30f;
constexpr float QSCALE = 0.25f * 1.4426950408889634f;

template <int I> struct IC { static constexpr int value = I; };
template <int I, int N, class F> DI void static_for(F&& f) { if constexpr (I < N) { f(IC<I>{}); static_for<I + 1, N>(f); } }
DI float bf_lo(u32 v) { return __uint_as_float(v << 16); }
DI float bf_hi(u32 v) { return __uint_as_float(v & 0xffff0000u); }
DI u32 pk(float a, float b) { f32x2 v = {a, b}; return __builtin_bit_cast(u32, __builtin_convertvector(v, bf16x2)); }
DI float rb(float a) { return bf_lo(pk(a, a)); }
template <int O> DI u32x4 pack8(const f32x16& a) { return (u32x4){pk(a[O], a[O + 1]), pk(a[O + 2], a[O + 3]), pk(a[O + 4], a[O + 5]), pk(a[O + 6], a[O + 7])}; }
DI bf16x8 asbf(u32x4 v) { return __builtin_bit_cast(bf16x8, v); }
#define MFMA(a, b, c) __builtin_amdgcn_mfma_f32_32x32x16_bf16((a), (b), (c), 0, 0, 0)
template <int CTRL, int RM = 0xf> DI float dpp(float v) { return __builtin_bit_cast(float, __builtin_amdgcn_update_dpp(0, __builtin_bit_cast(int, v), CTRL, RM, 0xf, true)); }
DI float sum8(float v) { v += dpp<0xB1>(v); v += dpp<0x4E>(v); v += dpp<0x141>(v); return v; }
DI float dot2(u32 a, u32 b, float c) { return __builtin_amdgcn_fdot2_f32_bf16(__builtin_bit_cast(bf16x2, a), __builtin_bit_cast(bf16x2, b), c, false); }

DI u32x4 lds_ld128(LAS char* L, u32 off) { return *(LAS u32x4*)(L + off); }
DI f32x4 lds_ldf4(LAS char* L, u32 off) { return *(LAS f32x4*)(L + off); }
DI void lds_st128(LAS char* L, u32 off, u32x4 v) { *(LAS u32x4*)(L + off) = v; }
DI bf16x8 lds_tr(LAS char* L, u32 off0, u32 off1) {
  s16x4 a = __builtin_amdgcn_ds_read_tr16_b64_v4i16((LAS s16x4*)(L + off0));
  s16x4 b = __builtin_amdgcn_ds_read_tr16_b64_v4i16((LAS s16x4*)(L + off1));
  return __builtin_bit_cast(bf16x8, __builtin_shufflevector(a, b, 0, 1, 2, 3, 4, 5, 6, 7));
}
DI u32x4 make_srd(const void* p, u32 bytes) { const u64 a = (u64)p; return (u32x4){(u32)a, (u32)(a >> 32), bytes, 0x00020000u}; }
DI void glds(const u32x4& srd, u32 voff, u32 soff, u32 ldsaddr) {
  asm volatile("s_mov_b32 m0, %3\n\ts_nop 0\n\tbuffer_load_dwordx4 %0, %1, %2 offen lds" ::"v"(voff), "s"(srd), "s"(soff), "s"(ldsaddr) : "memory");
}
DI void touch(const u32x4& srd, u32 voff, u32 ldsaddr) {
  asm volatile("s_mov_b32 m0, %2\n\ts_nop 0\n\tbuffer_load_dword %0, %1, 0 offen lds" ::"v"(voff), "s"(srd), "s"(ldsaddr) : "memory");
}
DI void lds_post(u32 addr, u32 val) {
  asm volatile("s_mov_b64 exec, 1\n\tds_write_b32 %0, %1\n\ts_mov_b64 exec, -1" ::"v"(addr), "v"(val) : "memory");
}
DI bool all_ge(u32x4 v, u32 k) { return __builtin_amdgcn_readfirstlane(min(min(v[0], v[1]), min(v[2], v[3]))) >= k; }
DI void lds_wait(LAS char* L, u32 off, u32 k) {
#pragma nounroll
  for (int it = 0; it < (SPIN_MAX << 4); ++it) {
    if (all_ge(*(volatile LAS u32x4*)(L + off), k)) break;
    __builtin_amdgcn_s_sleep(3);
  }
  asm volatile("" ::: "memory");
}
#define BAR() asm volatile("s_waitcnt lgkmcnt(0)\n\ts_barrier" ::: "memory")
#define WAITV_BAR(N) asm volatile("s_waitcnt vmcnt(%0) lgkmcnt(0)\n\ts_barrier" ::"i"(N) : "memory")
#define WAITV(N) asm volatile("s_waitcnt vmcnt(%0)" ::"i"(N) : "memory")
#define SB() __builtin_amdgcn_sched_barrier(0)
#define PIN(x) asm volatile("" : "+v"(x))
#define PINS(x) asm volatile("" : "+s"(x))
#define ORDER() __builtin_amdgcn_fence(__ATOMIC_ACQUIRE, "wavefront")

DI void wait_flags(const GAS u32* p, u32 tag) {
#pragma nounroll
  for (int it = 0; it < SPIN_MAX; ++it) {
    u32 v = __hip_atomic_load(p, RLX_AGENT);
    if (__all(v == tag)) break;
    __builtin_amdgcn_s_sleep(1);
  }
  ORDER();
}
template <int N> DI void gather_gran(const GAS u64* g, u32 tag, u32 (&v)[N], int stride) {
#pragma nounroll
  for (int it = 0; it < SPIN_MAX; ++it) {
    bool ok = true;
#pragma unroll
    for (int k = 0; k < N; ++k) { u64 x = __hip_atomic_load(g + k * stride, RLX_AGENT); v[k] = (u32)x; ok &= (u32)(x >> 32) == tag; }
    if (__all(ok)) break;
    __builtin_amdgcn_s_sleep(1);
  }
  ORDER();
}

#define STAMPC(n) do {} while (0)
#define STAMP(n) do {} while (0)

#define STAMP4(n) do {} while (0)
#define STAMP1(n) do {} while (0)
DI void ln_scal(float mean, float var, float& mb, float& sd) {
  mb = rb(mean);
  sd = rb(__builtin_amdgcn_sqrtf(rb(rb(fmaxf(var, 0.f)) + 1e-5f)));
}
DI float fma1(float a, float b, float c) { float r = fmaf(a, b, c); asm("" : "+v"(r)); return r; }
typedef __attribute__((ext_vector_type(2))) _Float16 f16x2;
DI u32x4 znorm8(u32x4 x, float r, float mr) {
  u32x4 o;
#pragma unroll
  for (int e = 0; e < 4; ++e) {
    const f32x2 v = {fmaf(bf_lo(x[e]), r, -mr), fmaf(bf_hi(x[e]), r, -mr)};
    o[e] = __builtin_bit_cast(u32, __builtin_convertvector(v, f16x2));
  }
  return o;
}
DI u32 xform2(u32 z_, u32 c_, u32 s_) {
  const f16x2 z = __builtin_bit_cast(f16x2, z_), c = __builtin_bit_cast(f16x2, c_), s = __builtin_bit_cast(f16x2, s_);
  return pk(fma1((float)z[0], (float)c[0], (float)s[0]), fma1((float)z[1], (float)c[1], (float)s[1]));
}
DI u32x4 xform8(u32x4 x, u32x4 c, u32x4 s) { return (u32x4){xform2(x[0], c[0], s[0]), xform2(x[1], c[1], s[1]), xform2(x[2], c[2], s[2]), xform2(x[3], c[3], s[3])}; }
DI u32 pkh(float a, float b) { const f32x2 v = {a, b}; return __builtin_bit_cast(u32, __builtin_convertvector(v, f16x2)); }
DI f32x2 geglu2(f32x2 ag, f32x2 au, float k1, float k3, float i2) {
  const f32x2 a = ag * (ag * ag * k3 + k1);
  const f32x2 d = (f32x2){__builtin_amdgcn_exp2f(a[0]), __builtin_amdgcn_exp2f(a[1])} * i2 + i2;
  return ag * au * (f32x2){__builtin_amdgcn_rcpf(d[0]), __builtin_amdgcn_rcpf(d[1])};
}

__global__ __launch_bounds__(512, 2) void adaln_block(const u16* __restrict__ x, const u16* __restrict__ wqkv, const u16* __restrict__ wo,
                                                      const u16* __restrict__ wg, const u16* __restrict__ wu, const u16* __restrict__ wd,
                                                      const u16* __restrict__ cvec, const u16* __restrict__ wm, u16* __restrict__ out,
                                                      char* __restrict__ ws) {
  __shared__ __attribute__((aligned(1024))) char lds[LDS_BYTES];
  LAS char* L = (LAS char*)lds;
  const u32 lbase = (u32)(uintptr_t)L;
  const int tid = threadIdx.x, lane = tid & 63;
  const int w = __builtin_amdgcn_readfirstlane(tid >> 6), w4 = w & 3;
  const bool mw = w < 4;
  const int b = blockIdx.x, xcd = b & 7, ii = b >> 3;
  const int t1 = 15 - (4 * (xcd >> 1) + (ii >> 3)), p1 = 8 * (xcd & 1) + (ii & 7);
  const int t3 = 2 * xcd + (ii >> 4), hf = (ii >> 3) & 1, cg = ii & 7;
  const int t4 = 8 * (xcd >> 2) + (ii >> 2), j4 = 4 * (xcd & 3) + (ii & 3);

  const int rot1 = (p1 + 2 * t1) & 7;
  const int rot4 = ii >> 2, rot5 = 7 * (ii >> 3);
  const int r32 = lane & 31, hh = lane >> 5, blk = (lane >> 4) & 1, q4 = (lane & 15) >> 2, p4 = lane & 3, l3 = lane >> 3, l7 = lane & 7;
  const __amdgpu_buffer_rsrc_t rs = __builtin_amdgcn_make_buffer_rsrc(ws, 0, WS_BYTES, 0x00020000);
  STAMP(0);

  u32 tag = 0;
  if (tid < 12) *(LAS u32*)(L + L_DONE + tid * 4) = 0u;
  BAR();
  const int orow = t3 * 128 + hf * 64 + 8 * w + l3;
  STAMP(1);
  u32x4 X[24];
  const __amdgpu_buffer_rsrc_t rx = __builtin_amdgcn_make_buffer_rsrc((void*)x, 0, 2048 * 1024, 0x00020000);
  const u32 xo = l3 * 1024u + l7 * 16u;
  u32x4 xr;
  if (mw) {
    const u32x4 srd = make_srd(wqkv, 512 * 3072);
    const int mg = (15 - t1) * 16 + p1;
    u32x4 M[16], cv[2];
    if (t1 >= 13) {
      const GAS char* mb = (const GAS char*)wm + mg * 128 + (128 * w + 16 * l3) * 6144 + l7 * 16;
      const GAS u32x4* cp = (const GAS u32x4*)((const GAS char*)cvec + (128 * w + 16 * l3) * 2);
      cv[0] = cp[0]; cv[1] = cp[1];
#pragma unroll
      for (int n = 0; n < 16; ++n) M[n] = *(const GAS u32x4*)(mb + n * 6144);
    }
    const u32 ep = __hip_atomic_load((const GAS u32*)(ws + WS_EPOCH), RLX_AGENT);
#pragma unroll
    for (int c = 0; c < 8; ++c) X[c] = __builtin_amdgcn_raw_buffer_load_b128(rx, xo, (t1 * 128 + 32 * w4 + 24) * 1024 + ((c + rot1) & 7) * 128, 0);
    xr = *(const GAS u32x4*)((const GAS char*)x + orow * 1024 + cg * 128 + l7 * 16);
    SB();
    if (t1 >= 13) {
      f32x2 a[4] = {};
#pragma unroll
      for (int n = 0; n < 16; ++n) {
        const float cf = (n & 1) ? bf_hi(cv[n >> 3][(n >> 1) & 3]) : bf_lo(cv[n >> 3][(n >> 1) & 3]);
        const float sl = rb(cf * __builtin_amdgcn_rcpf(1.0f + __builtin_amdgcn_exp2f(-1.4426950408889634f * cf)));
#pragma unroll
        for (int e = 0; e < 4; ++e) a[e] += sl * (f32x2){bf_lo(M[n][e]), bf_hi(M[n][e])};
      }
      float v[8] = {a[0][0], a[0][1], a[1][0], a[1][1], a[2][0], a[2][1], a[3][0], a[3][1]};
#pragma unroll
      for (int e = 0; e < 8; ++e) {
        v[e] += dpp<0x128>(v[e]);
        auto r1_ = __builtin_amdgcn_permlane16_swap(__float_as_uint(v[e]), __float_as_uint(v[e]), false, false);
        v[e] = __uint_as_float(r1_[0]) + __uint_as_float(r1_[1]);
        auto r2_ = __builtin_amdgcn_permlane32_swap(__float_as_uint(v[e]), __float_as_uint(v[e]), false, false);
        v[e] = __uint_as_float(r2_[0]) + __uint_as_float(r2_[1]);
      }
      if (lane < 8) {
        *(LAS f32x4*)(L + L_RED + w * 256 + lane * 32) = (f32x4){v[0], v[1], v[2], v[3]};
        *(LAS f32x4*)(L + L_RED + w * 256 + lane * 32 + 16) = (f32x4){v[4], v[5], v[6], v[7]};
      }
      lds_post(lbase + L_MODF + w * 4, 1);
      if (w == 2) {
        lds_wait(L, L_MODF, 1);
        float s = 0.f;
#pragma unroll
        for (int k = 0; k < 4; ++k) s += *(LAS float*)(L + L_RED + k * 256 + lane * 4);
        const float o = dpp<0xB1>(s);
        const u32 tg = __builtin_amdgcn_readfirstlane(ep) + 1u;
        if (!(lane & 1)) __hip_atomic_store((GAS u64*)(ws + WS_MODG) + 32 * mg + (lane >> 1), ((u64)tg << 32) | pk(s, o), RLX_AGENT);
        STAMP(28);
      }
    }
    if (w == 1) {
      constexpr u32 PG = V_TLB, NWS = (WS_DBG / PG + 31) / 32;
#pragma unroll
      for (u32 k = 0; k < (NWS + 63) / 64; ++k) touch(make_srd(ws, WS_DBG), k * 64 + lane < NWS ? (ii * NWS + k * 64 + lane) * PG : 0x7fffffffu, lbase + L_JUNK);
      const int bi = ii & 7;
      const void* bp = bi == 0 ? (const void*)wo : bi == 1 ? (const void*)wg : bi == 2 ? (const void*)wu : bi == 3 ? (const void*)wd : bi == 4 ? (const void*)out : bi == 5 ? (const void*)wqkv : bi == 6 ? (const void*)x : (const void*)wm;
      const u32 bs = bi == 0 ? 524288u : bi < 4 ? 1835008u : bi == 5 ? 1572864u : bi == 7 ? 3145728u : 2097152u;
      constexpr u32 NB = 3145728 / PG / 4;
#pragma unroll
      for (u32 k = 0; k < (NB + 63) / 64; ++k) touch(make_srd(bp, bs), k * 64 + lane < NB ? ((ii >> 3) * NB + k * 64 + lane) * PG : 0x7fffffffu, lbase + L_JUNK);
    }
    if (w == 3) {
      u64 pc;
      int len;
      asm volatile("s_getpc_b64 %0\n.Ladaln_pf:\n\ts_mov_b32 %1, .Ladaln_end-.Ladaln_pf" : "=s"(pc), "=s"(len));
      touch(make_srd((const void*)pc, len > 0 ? (u32)len : 0u), (ii * CODE_LINES + (lane < CODE_LINES ? lane : 0)) * 128u, lbase + L_JUNK);
      if (lane < 32) lds_st128(L, L_ONES + (lane & 15) * 16 + (lane >> 4) * 1024, (u32x4){0x3f803f80u, 0x3f803f80u, 0x3f803f80u, 0x3f803f80u});
    }
    tag = __builtin_amdgcn_readfirstlane(ep) + 1u;
    if (w == 3 && lane == 0) *(LAS u32*)(L + L_TAG) = tag;
    BAR();
    STAMP(2);
    float rr, mr;
    {
      float sm = 0.f, sq = 0.f, sm2 = 0.f, sq2 = 0.f;
#pragma unroll
      for (int c = 0; c < 8; ++c) {
        sm = dot2(X[c][0], 0x3f803f80u, sm); sq = dot2(X[c][0], X[c][0], sq); sm2 = dot2(X[c][1], 0x3f803f80u, sm2); sq2 = dot2(X[c][1], X[c][1], sq2);
        sm = dot2(X[c][2], 0x3f803f80u, sm); sq = dot2(X[c][2], X[c][2], sq); sm2 = dot2(X[c][3], 0x3f803f80u, sm2); sq2 = dot2(X[c][3], X[c][3], sq2);
      }
      const float me = sum8(sm + sm2) * (1.f / 512.f);
      float mb, sd;
      ln_scal(me, sum8(sq + sq2) * (1.f / 512.f) - me * me, mb, sd);
      rr = __builtin_amdgcn_rcpf(sd); mr = mb * rr;
      PIN(rr); PIN(mr);
    }
    {
      const u32 vo = (lane >> 2) * 3072u + (lane & 3) * 16u;
      u32 go[4];
#pragma unroll
      for (int e = 0; e < 4; ++e) { const int q = 4 * w + e; go[e] = (q / 3) * 16 * 3072 + (q % 3) * 1024 + 64 * p1; }
#pragma unroll
      for (int c = 0; c < 8; ++c) {
        if (w < 3) {
#pragma unroll
          for (int e = 0; e < 4; ++e) glds(srd, vo, go[e] + ((c + rot1) & 7) * 196608, lbase + wq_base(c) + (4 * w + e) * 1024);
        }
        PIN(X[c]); X[c] = znorm8(X[c], rr, mr); PIN(X[c]);
      }
    }
    if (w == 3) {
      if (lane == 0) __hip_atomic_store((GAS u32*)(ws + WS_ARR) + b, tag, RLX_AGENT);
      u32x4 v[4];
#pragma nounroll
      for (int it = 0; it < SPIN_MAX; ++it) {
        bool ok = true;
#pragma unroll
        for (int k = 0; k < 4; ++k) { v[k] = __builtin_amdgcn_raw_buffer_load_b128(rs, lane * 16, WS_MODG + k * 1024, 16); ok &= v[k][1] == tag && v[k][3] == tag; }
        if (__all(ok)) break;
        __builtin_amdgcn_s_sleep(1);
      }
#pragma unroll
      for (int k = 0; k < 2; ++k) {
        const u32 o = L_C1 + ((lane + 64 * k) >> 1) * 32 + (lane & 1) * 8;
        *(LAS u32x2*)(L + o + 16) = (u32x2){pkh(bf_lo(v[k][0]), bf_hi(v[k][0])), pkh(bf_lo(v[k][2]), bf_hi(v[k][2]))};
        *(LAS u32x2*)(L + o) = (u32x2){pkh(rb(1.f + bf_lo(v[2 + k][0])), rb(1.f + bf_hi(v[2 + k][0]))), pkh(rb(1.f + bf_lo(v[2 + k][2])), rb(1.f + bf_hi(v[2 + k][2])))};
      }
    }
    STAMP(3);
  } else {
#pragma unroll
    for (int c = 0; c < 8; ++c)
#pragma unroll
      for (int g = 0; g < 3; ++g) X[3 * c + g] = __builtin_amdgcn_raw_buffer_load_b128(rx, xo, (t1 * 128 + 32 * w4 + 8 * g) * 1024 + ((c + rot1) & 7) * 128, 0);
    xr = *(const GAS u32x4*)((const GAS char*)x + orow * 1024 + cg * 128 + l7 * 16);
    SB();
    BAR();
    STAMP(2);
    float sm[3] = {}, sq[3] = {};
#pragma unroll
    for (int c = 0; c < 8; ++c) {
      SB();
#pragma unroll
      for (int e = 0; e < 4; ++e)
#pragma unroll
        for (int g = 0; g < 3; ++g) { sm[g] = dot2(X[3 * c + g][e], 0x3f803f80u, sm[g]); sq[g] = dot2(X[3 * c + g][e], X[3 * c + g][e], sq[g]); }
    }
    tag = __builtin_amdgcn_readfirstlane(*(LAS u32*)(L + L_TAG));
    float rr[3], mr[3];
#pragma unroll
    for (int g = 0; g < 3; ++g) {
      const float me = sum8(sm[g]) * (1.f / 512.f);
      float mb, sd;
      ln_scal(me, sum8(sq[g]) * (1.f / 512.f) - me * me, mb, sd);
      rr[g] = __builtin_amdgcn_rcpf(sd); mr[g] = mb * rr[g];
    }
#pragma unroll
    for (int n = 0; n < 24; ++n) { X[n] = znorm8(X[n], rr[n % 3], mr[n % 3]); PIN(X[n]); }
    STAMP(3);
  }
  BAR();
  STAMP(4);
  STAMPC(29);

  u32 cso = L_MISC + l7 * 32;
  PIN(cso);
  const u32x4 srd_g = make_srd(wg, 512 * 3584), srd_u = make_srd(wu, 512 * 3584), srd_d = make_srd(wd, 1792 * 1024), srd_o = make_srd(wo, 512 * 1024);

  u32 guo[7];
  const u32x4 srd_3 = w4 < 2 ? srd_g : srd_u;
  auto load_gu = [&](int c, u32 slot) {
    int r_ = rot4; PINS(r_);
    const u32 so = j4 * 224 + ((c + r_) & 7) * (64 * 3584), ld = lbase + slot + w4 * 1024;
#pragma unroll
    for (int n = 0; n < 7; ++n) glds(n < 3 ? srd_g : n == 3 ? srd_3 : srd_u, guo[n], so, ld + n * 4096);
  };
  const u32 w8o = l3 * 1024u + ((l7 ^ (((lane >> 4) & 1) << 2)) << 4);
  const u32 awE = L_AR1 + (32 * w4 + l3) * 128 + ((l7 ^ l3) << 4), awO = awE ^ 16u;
  u32x4 ca[2], sa[2];
#define CSLOAD(c) do { const u32 o_ = cso + (((c) + rot1) & 7) * 256; ca[(c) & 1] = lds_ld128(L, o_); sa[(c) & 1] = lds_ld128(L, o_ + 16); } while (0)
  if (mw) {
    u32x4 f0, f1, f2, f3, f4, f5;
    f32x16 aq = {}, ak = {}, av = {};
    u32 ard[4];
    {
      const int row = 32 * w4 + r32, f = (row & 7) ^ ((row >> 3) & 1);
#pragma unroll
      for (int s = 0; s < 4; ++s) { ard[s] = L_AR1 + row * 128 + ((((2 * s) | hh) ^ f) << 4); PIN(ard[s]); }
    }
    const u32 wl = (8 * hh + q4) * 64 + 32 * blk + 8 * p4;
    u32 wlh = wl + 60 * KB;
    PIN(wlh);
    bf16x8 hf_[3], qf_[3], kf_[3], vf_[3];
#define P1O(n) (((n) < 20 ? wl + wq_base((n) >> 2) : wlh + (wq_base((n) >> 2) - 60 * KB)) + ((n) & 3) * 3072)
#define P1H(n) hf_[(n) % 3] = asbf(lds_ld128(L, ard[(n) & 3] + ar_off((n) >> 2)))
    CSLOAD(0); CSLOAD(1);
    lds_st128(L, awO + 3072 + ar_off(0), xform8(X[0], ca[0], sa[0]));
    u32x4 hm;
    WAITV_BAR(28);
#pragma unroll
    for (int n = 0; n < 2; ++n) {
      P1H(n); qf_[n] = lds_tr(L, P1O(n), P1O(n) + 256); kf_[n] = lds_tr(L, P1O(n) + 1024, P1O(n) + 1280); vf_[n] = lds_tr(L, P1O(n) + 2048, P1O(n) + 2304);
    }
#pragma unroll
    for (int n = 0; n < 32; ++n) {
      const int m = n + 2;
      const bool bnd = (m & 3) == 0;
      SB();
      ak = MFMA(kf_[n % 3], hf_[n % 3], ak);
      if (m < 32 && !bnd) { P1H(m); kf_[m % 3] = lds_tr(L, P1O(m) + 1024, P1O(m) + 1280); }
      if ((n & 3) < 2 && n < 28) {
        const int cc = n / 4 + 1, hs = n & 1;
        if (!hs) PIN(X[cc & 7]);
        hm[2 * hs] = xform2(X[cc & 7][2 * hs], ca[cc & 1][2 * hs], sa[cc & 1][2 * hs]);
        hm[2 * hs + 1] = xform2(X[cc & 7][2 * hs + 1], ca[cc & 1][2 * hs + 1], sa[cc & 1][2 * hs + 1]);
      }
      SB();
      av = MFMA(hf_[n % 3], vf_[n % 3], av);
      if (m < 32 && bnd) {
        PIN(ak); PIN(av);
        STAMP1(20 + m / 4);
        WAITV_BAR(28 - m);
        P1H(m); kf_[m % 3] = lds_tr(L, P1O(m) + 1024, P1O(m) + 1280);
      }
      if (m < 32) vf_[m % 3] = lds_tr(L, P1O(m) + 2048, P1O(m) + 2304);
      SB();
      aq = MFMA(qf_[n % 3], hf_[n % 3], aq);
      if (m < 32) qf_[m % 3] = lds_tr(L, P1O(m), P1O(m) + 256);
      if ((n & 3) == 1 && n < 28) lds_st128(L, awO + 3072 + ar_off(n / 4 + 1), hm);
      if ((n & 3) == 2 && n < 24) CSLOAD((n + 2) / 4 + 1);
      PIN(ak); PIN(av); PIN(aq);
    }
    STAMP(5);
    STAMPC(30);
    f0 = pack8<0>(ak); f1 = pack8<8>(ak); f2 = pack8<0>(av); f3 = pack8<8>(av);
    const u32 o = WS_KV + ((t1 * 16 + p1) * 4 + w) * 4096 + lane * 16;
    __builtin_amdgcn_raw_buffer_store_b128(f0, rs, o, 0, 16);
    __builtin_amdgcn_raw_buffer_store_b128(f1, rs, o + 1024, 0, 16);
    __builtin_amdgcn_raw_buffer_store_b128(f2, rs, o + 2048, 0, 16);
    __builtin_amdgcn_raw_buffer_store_b128(f3, rs, o + 3072, 0, 16);
    aq *= QSCALE;
    f4 = pack8<0>(aq); f5 = pack8<8>(aq);
    BAR();
    const u32 ok = L_KV + (8 + w) * 4096 + lane * 16, oq = L_Q + w * 2048 + lane * 16;
    lds_st128(L, ok, f0); lds_st128(L, ok + 1024, f1); lds_st128(L, ok + 2048, f2); lds_st128(L, ok + 3072, f3);
    lds_st128(L, oq, f4); lds_st128(L, oq + 1024, f5);
    BAR();
    WAITV(0);
    if (lane == 0) __hip_atomic_store((GAS u32*)(ws + WS_FKV) + (t1 * 16 + p1) * 16 + w, tag, RLX_AGENT);
  } else {
    __builtin_amdgcn_s_setprio(2);
    CSLOAD(0);
#pragma unroll
    for (int c = 0; c < 8; ++c) {
      if (c < 7) CSLOAD(c + 1);
      SB();
#pragma unroll
      for (int g = 0; g < 3; ++g) {
        PIN(X[3 * c + g]);
        lds_st128(L, ((g & 1) ? awO : awE) + g * 1024 + ar_off(c), xform8(X[3 * c + g], ca[c & 1], sa[c & 1]));
      }
      if (c >= 1) STAMP1(19 + c);
      if (c >= 1) asm volatile("s_waitcnt lgkmcnt(3)\n\ts_barrier" ::: "memory");
    }
    BAR();
    __builtin_amdgcn_s_setprio(0);
#pragma unroll
    for (int n = 0; n < 7; ++n) {
      const int pc = w4 + 4 * n, up = pc >= 14, e = (pc - 14 * up) * 64 + lane, rho = e / 14, cc = e - 14 * rho;
      guo[n] = (u32)((up ? (rho ^ 4) : rho) * 3584 + cc * 16);
    }
    STAMP(5);
    if (w4 < 3) {
      const u32x4 srd_a = w4 == 0 ? srd_o : w4 == 1 ? srd_g : srd_u;
      const int e1 = 64 + lane;
      const u32 ta0 = w4 ? (16 * ii + lane / 7) * 3584 + (xcd & 3) * 896 + (lane % 7) * 128 : ii * 16384 + lane * 128;
      const u32 ta1 = w4 ? (lane < 48 ? (16 * ii + e1 / 7) * 3584 + (xcd & 3) * 896 + (e1 % 7) * 128 : 0x7fffffffu) : ta0 + 8192;
      touch(srd_a, ta0, lbase + L_JUNK);
      touch(srd_a, ta1, lbase + L_JUNK);
    }
    BAR();
    BAR();
  }
  STAMP(6);

  {
    const int qb = mw ? w : 7 - w, hd = w >> 2;
    const bf16x8 qf = asbf(lds_ld128(L, L_Q + qb * 2048 + hd * 1024 + lane * 16));
    const u32 ka = L_KV + hd * 1024 + lane * 16;
    const bool vdat = (r32 >> 4) == hd;
    const u32 va = vdat ? L_KV + 2048 + lane * 16 : L_ONES + (lane & 15) * 16;
    const u32 vs = vdat ? 4096u : 0u;
    f32x16 cm;
#pragma unroll
    for (int g = 0; g < 16; ++g) cm[g] = (g & 3) + 8 * (g >> 2) + 4 * hh <= r32 ? 0.f : NEGBIG;
    const f32x16 zero = {};
    f32x16 O = {};
    float m = NEGBIG;
    auto rowmax = [&](const f32x16& s, float mm) {
#pragma unroll
      for (int g = 0; g < 16; g += 2) mm = fmaxf(fmaxf(mm, s[g]), s[g + 1]);
      return mm;
    };
    auto ldk = [&](int lb) { return asbf(lds_ld128(L, ka + lb * 4096)); };
#pragma nounroll
    for (int st = 0; st < 2; ++st) {
      int lm = 8 + qb, lo = 8, hi = 8 + qb;
      if (st) {
        STAMP(7);
        if (!mw) {
          for (int n = w4 * 16; n < w4 * 16 + 16; ++n) glds(srd_o, w8o, cg * 128 + n * 8192, lbase + L_WO + n * 1024);
          load_gu(0, gu_slot(0));
        }
        if (t1 == 0) break;
        if (w < 2) {
          const int tp = t1 - 2 + w;
          u32x4 nb[16] = {};
          if (tp >= 0) {
            wait_flags((const GAS u32*)(ws + WS_FKV) + (tp * 16 + p1) * 16 + (lane & 3), tag);
            const u32 o = WS_KV + (tp * 16 + p1) * 16384 + lane * 16;
#pragma unroll
            for (int n = 0; n < 16; ++n) nb[n] = __builtin_amdgcn_raw_buffer_load_b128(rs, o + n * 1024, 0, 16);
          }
#pragma unroll
          for (int n = 0; n < 16; ++n) lds_st128(L, L_KV + w * 16384 + n * 1024 + lane * 16, nb[n]);
        }
#pragma unroll
        for (int g = 0; g < 16; ++g) { float u = t1 == 1 ? NEGBIG : NEGBIG - cm[g]; asm("" : "+v"(u)); cm[g] = u; }
        lm = qb; lo = t1 == 1 ? 4 : qb + 1; hi = 8;
        BAR();
        STAMP(8);
      }
      bf16x8 kA = ldk(lo), kB = ldk(lo + 1);
      f32x16 sA = MFMA(ldk(lm), qf, cm), sB, sC;
      const bf16x8 k0 = kA;
      float mn = m;
      for (int lb = lo; lb < hi; lb += 2) {
        sB = MFMA(kA, qf, zero);
        sC = MFMA(kB, qf, zero);
        kA = ldk(lb + 2); kB = ldk(lb + 3);
        SB();
        mn = rowmax(sB, mn);
        if (lb + 1 < hi) mn = rowmax(sC, mn);
      }
      mn = rowmax(sA, mn);
      {
        auto r_ = __builtin_amdgcn_permlane32_swap(__float_as_uint(mn), __float_as_uint(mn), false, false);
        mn = fmaxf(__uint_as_float(r_[0]), __uint_as_float(r_[1]));
      }
      u32 vv = va + lm * vs;
      bf16x8 v0 = asbf(lds_ld128(L, vv)), v1 = asbf(lds_ld128(L, vv + 1024));
      f32x16 negm;
#pragma unroll
      for (int g = 0; g < 16; ++g) negm[g] = -mn;
#pragma unroll
      for (int g = 0; g < 16; ++g) { float u = sA[g] - mn; asm("" : "+v"(u)); sA[g] = u; }
      if (st) {
        const float al = __builtin_amdgcn_exp2f(m - mn);
#pragma unroll
        for (int g = 0; g < 16; ++g) { float o = O[g] * al; asm("" : "+v"(o)); O[g] = o; }
      }
      m = mn;
      kA = k0;
      vv = va + lo * vs;
#define P2STEP(CUR, NXT)                                                                      \
  {                                                                                           \
    NXT = MFMA(kA, qf, negm);                                                                 \
    PIN(NXT);                                                                                 \
    kA = ldk(lb + 1);                                                                         \
    const bf16x8 n0_ = asbf(lds_ld128(L, vv)), n1_ = asbf(lds_ld128(L, vv + 1024));           \
    vv += vs;                                                                                 \
    SB();                                                                                     \
    _Pragma("unroll") for (int g = 0; g < 8; ++g) CUR[g] = __builtin_amdgcn_exp2f(CUR[g]);    \
    O = MFMA(v0, asbf(pack8<0>(CUR)), O);                                                     \
    _Pragma("unroll") for (int g = 8; g < 16; ++g) CUR[g] = __builtin_amdgcn_exp2f(CUR[g]);   \
    O = MFMA(v1, asbf(pack8<8>(CUR)), O);                                                     \
    SB();                                                                                     \
    v0 = n0_; v1 = n1_;                                                                       \
  }
      for (int lb = lo;; ++lb) {
        P2STEP(sA, sB);
        if (lb >= hi) break;
        ++lb;
        P2STEP(sB, sA);
        if (lb >= hi) break;
      }
    }
    u32x4 of;
    {
      const float inv = __builtin_amdgcn_rcpf(hd ? O[0] : O[8]);
      float o[8];
#pragma unroll
      for (int g = 0; g < 8; ++g) { o[g] = (hd ? O[8 + g] : O[g]) * inv; asm("" : "+v"(o[g])); }
      of = (u32x4){pk(o[0], o[1]), pk(o[2], o[3]), pk(o[4], o[5]), pk(o[6], o[7])};
    }
    __builtin_amdgcn_raw_buffer_store_b128(of, rs, WS_O + ((t1 * 4 + qb) * 32 + 2 * p1 + hd) * 1024 + lane * 16, 0, 16);
    WAITV(0);
    if (lane == 0) __hip_atomic_store((GAS u32*)(ws + WS_FO) + (t1 * 4 + qb) * 32 + 2 * p1 + hd, tag, RLX_AGENT);
  }
  STAMP(9);
  {
    if (t1 == 0 && p1 == 0 && w == 7) {
#pragma nounroll
      for (int it = 0; it < SPIN_MAX; ++it) {
        bool ok = true;
#pragma unroll
        for (int k = 0; k < 4; ++k) ok &= __hip_atomic_load((const GAS u32*)(ws + WS_ARR) + lane + 64 * k, RLX_AGENT) == tag;
        if (__all(ok)) break;
        __builtin_amdgcn_s_sleep(1);
      }
      if (lane == 0) __hip_atomic_store((GAS u32*)(ws + WS_EPOCH), tag, RLX_AGENT);
    }
  }
  BAR();

  const u32 wt0 = (4 * hh + q4) * 128 + ((32 * blk + 8 * p4) ^ ((q4 >> 1) << 6)), wt1 = wt0 ^ 64u;
  auto red_write = [&](u32 base, u32 S8, const f32x16& a0, const f32x16& a1) {
    u32 ln = lane; PIN(ln);
    const u32 o = base + ((ln & 31) >> 3) * S8 + (ln & 7) * 256, sw = (ln & 7) ^ (ln >> 5);
#pragma unroll
    for (int g = 0; g < 4; ++g) {
      *(LAS f32x4*)(L + o + (((2 * g) ^ sw) << 4)) = (f32x4){a0[4 * g], a0[4 * g + 1], a0[4 * g + 2], a0[4 * g + 3]};
      *(LAS f32x4*)(L + o + (((2 * g) ^ sw) << 4) + 128) = (f32x4){a1[4 * g], a1[4 * g + 1], a1[4 * g + 2], a1[4 * g + 3]};
    }
  };
  auto red_read = [&](u32 base, u32 S8, u32 SK, f32x4& lo, f32x4& hi) {
    u32 ln = lane; PIN(ln);
    const u32 o0 = base + w * S8 + (ln >> 3) * 256 + (((2 * (ln & 7)) ^ (ln >> 3)) << 4), o1 = o0 ^ 16u;
    lo = lds_ldf4(L, o0); hi = lds_ldf4(L, o1);
#pragma unroll
    for (int k = 1; k < 4; ++k) { lo += lds_ldf4(L, o0 + k * SK); hi += lds_ldf4(L, o1 + k * SK); }
  };
  u32x4 x1p;
  {
    if (mw) {
      bf16x8 wa[8], wb[8];
#pragma unroll
      for (int i = 0; i < 8; ++i) {
        const u32 o = L_WO + w * 16384 + i * 2048;
        wa[i] = lds_tr(L, o + wt0, o + wt0 + 1024); wb[i] = lds_tr(L, o + wt1, o + wt1 + 1024);
      }
      wait_flags((const GAS u32*)(ws + WS_FO) + (t3 * 4 + 2 * hf + (l3 & 1)) * 32 + 8 * w + l7, tag);
      STAMP(10);
      u32x4 of0[8], of1[8];
      const u32 oo = WS_O + ((t3 * 4 + 2 * hf) * 32 + 8 * w) * 1024 + lane * 16;
#pragma unroll
      for (int i = 0; i < 8; ++i) {
        of0[i] = __builtin_amdgcn_raw_buffer_load_b128(rs, oo + i * 1024, 0, 16);
        of1[i] = __builtin_amdgcn_raw_buffer_load_b128(rs, oo + 32768 + i * 1024, 0, 16);
      }
      SB();
      f32x16 a00 = {}, a01 = {}, a10 = {}, a11 = {};
#pragma unroll
      for (int i = 0; i < 8; ++i) {
        a00 = MFMA(wa[i], asbf(of0[i]), a00); a01 = MFMA(wb[i], asbf(of0[i]), a01);
        a10 = MFMA(wa[i], asbf(of1[i]), a10); a11 = MFMA(wb[i], asbf(of1[i]), a11);
      }
      STAMP(11);
      red_write(L_WO + w * 16384, 2048, a00, a01);
      red_write(L_WO + w * 16384 + 8192, 2048, a10, a11);
    } else {
      u32x4 q[4];
      {
        u32 v[3];
        const GAS u64* g = (const GAS u64*)(ws + WS_MODG);
        const u32 qo = (((r32 & 1) ? 768 : 1024) + rot4 * 32 + (t4 >> 3) * 16 + 4 * hh) * 8;
#pragma nounroll
        for (int it = 0; it < SPIN_MAX; ++it) {
          const u64 x1_ = __hip_atomic_load(g + 1024 + (tid - 256), RLX_AGENT);
          const u64 x2 = __hip_atomic_load(g + (lane < 32 ? 512 : 1248) + cg * 32 + lane, RLX_AGENT);
          bool ok = (u32)(x1_ >> 32) == tag && (u32)(x2 >> 32) == tag;
#pragma unroll
          for (int k = 0; k < 4; ++k) { q[k] = __builtin_amdgcn_raw_buffer_load_b128(rs, qo + (k >> 1) * 64 + (k & 1) * 16, WS_MODG, 16); ok &= q[k][1] == tag && q[k][3] == tag; }
          v[1] = (u32)x1_; v[2] = (u32)x2;
          if (__all(ok)) break;
          __builtin_amdgcn_s_sleep(1);
        }
        *(LAS f32x2*)(L + L_C1B + (tid - 256) * 8) = (f32x2){rb(1.f + bf_lo(v[1])), rb(1.f + bf_hi(v[1]))};
        if (w == 4) *(LAS f32x2*)(L + L_GA + lane * 8) = (f32x2){bf_lo(v[2]), bf_hi(v[2])};
      }
      STAMP(10);
      {
        const u32 gl = blk * 14336 + (8 * hh + q4) * 224 + 8 * p4 + w4 * 32 + (t4 >> 3) * 7168;
        const u32 g0 = gl + (blk ? 896u : 0u), g1 = gl + (blk ? 0u : 896u);
        const float one = (r32 & 1) ? 0.f : 1.f;
        f32x16 a0 = {}, a1 = {};
#pragma unroll
        for (int s = 0; s < 2; ++s) {
          const u32x4 bv = {pk(one + bf_lo(q[2 * s][0]), one + bf_hi(q[2 * s][0])), pk(one + bf_lo(q[2 * s][2]), one + bf_hi(q[2 * s][2])),
                            pk(one + bf_lo(q[2 * s + 1][0]), one + bf_hi(q[2 * s + 1][0])), pk(one + bf_lo(q[2 * s + 1][2]), one + bf_hi(q[2 * s + 1][2]))};
          a0 = MFMA(lds_tr(L, g0 + s * 3584, g1 + s * 3584), asbf(bv), a0);
          a1 = MFMA(lds_tr(L, g0 + s * 3584 + 128, g1 + s * 3584 + 128), asbf(bv), a1);
        }
        if (r32 < 2) {
          const u32 o = WS_UV + (j4 * 16 + t4) * 2048 + w4 * 256 + r32 * 128 + hh * 16;
#pragma unroll
          for (int g = 0; g < 4; ++g) {
            __builtin_amdgcn_raw_buffer_store_b128(__builtin_bit_cast(u32x4, (f32x4){a0[4 * g], a0[4 * g + 1], a0[4 * g + 2], a0[4 * g + 3]}), rs, o + g * 32, 0, 16);
            if (w4 < 3) __builtin_amdgcn_raw_buffer_store_b128(__builtin_bit_cast(u32x4, (f32x4){a1[4 * g], a1[4 * g + 1], a1[4 * g + 2], a1[4 * g + 3]}), rs, o + 1024 + g * 32, 0, 16);
          }
        }
      }
      WAITV(0);
      if (lane == 0) __hip_atomic_store((GAS u32*)(ws + WS_FUV) + j4 * 64 + t4 * 4 + w4, tag, RLX_AGENT);
      load_gu(1, gu_slot(1));
      load_gu(2, gu_slot(2));
      STAMP(11);
    }
    BAR();
    f32x4 lo, hi;
    red_read(L_WO, 2048, 16384, lo, hi);
    const f32x4 ga0 = lds_ldf4(L, cso + M_GA), ga1 = lds_ldf4(L, cso + M_GA + 16);
    float v[8];
    v[0] = rb(bf_lo(xr[0]) + rb(ga0[0] * rb(lo[0]))); v[1] = rb(bf_hi(xr[0]) + rb(ga0[1] * rb(lo[1])));
    v[2] = rb(bf_lo(xr[1]) + rb(ga0[2] * rb(lo[2]))); v[3] = rb(bf_hi(xr[1]) + rb(ga0[3] * rb(lo[3])));
    v[4] = rb(bf_lo(xr[2]) + rb(ga1[0] * rb(hi[0]))); v[5] = rb(bf_hi(xr[2]) + rb(ga1[1] * rb(hi[1])));
    v[6] = rb(bf_lo(xr[3]) + rb(ga1[2] * rb(hi[2]))); v[7] = rb(bf_hi(xr[3]) + rb(ga1[3] * rb(hi[3])));
    x1p = (u32x4){pk(v[0], v[1]), pk(v[2], v[3]), pk(v[4], v[5]), pk(v[6], v[7])};
    {
      const f32x4 c0 = lds_ldf4(L, cso + M_C1B + cg * 256), c1 = lds_ldf4(L, cso + M_C1B + cg * 256 + 16);
      const u32x4 y = {pk(v[0] * c0[0], v[1] * c0[1]), pk(v[2] * c0[2], v[3] * c0[3]), pk(v[4] * c1[0], v[5] * c1[1]), pk(v[6] * c1[2], v[7] * c1[3])};
      __builtin_amdgcn_raw_buffer_store_b128(y, rs, WS_X1 + ((t3 * 4 + 2 * hf + (w >> 2)) * 32 + 4 * cg + (l7 >> 1)) * 1024 + ((l7 & 1) * 32 + 8 * w4 + l3) * 16, 0, 16);
    }
    const float sm = sum8(((v[0] + v[1]) + (v[2] + v[3])) + ((v[4] + v[5]) + (v[6] + v[7])));
    const float mu = sm * (1.f / 64.f);
    float m2 = 0.f;
#pragma unroll
    for (int k = 0; k < 8; ++k) m2 = fmaf(v[k] - mu, v[k] - mu, m2);
    m2 = sum8(m2);
    if (l7 == 0) __hip_atomic_store((GAS u64*)(ws + WS_ST) + orow * 8 + cg, ((u64)__float_as_uint(m2) << 32) | __float_as_uint(sm), RLX_AGENT);
    WAITV(0);
    if (lane == 0) __hip_atomic_store((GAS u32*)(ws + WS_FX) + (t3 * 4 + 2 * hf + (w >> 2)) * 32 + cg * 4 + w4, tag, RLX_AGENT);
  }
  STAMP(12);
  BAR();

  if (mw) {
    u32 ln = lane; PIN(ln);
    const u32 bk = (ln >> 4) & 1, gl = bk * 14336 + (8 * (ln >> 5) + ((ln & 15) >> 2)) * 224 + 8 * (ln & 3);
    const u32 g0 = gl + (bk ? 896u : 0u), g1 = gl + (bk ? 0u : 896u);
    u32 g0h = g0 + 92 * KB, g1h = g1 + 92 * KB;
    PIN(g0h); PIN(g1h);
    f32x16 ag[7] = {};
    bf16x8 wf_[7];
    u32x4 yf[32];
#define P4HI(n) (gu_slot(((n) >> 2) & 3) >= 92 * KB)
#define P4O(n) (gu_slot(((n) >> 2) & 3) - (P4HI(n) ? 92 * KB : 0) + ((n) & 3) * 3584)
#define P4W(n, f) wf_[f] = lds_tr(L, (P4HI(n) ? g0h : g0) + P4O(n) + (f) * 32, (P4HI(n) ? g1h : g1) + P4O(n) + (f) * 32)
#pragma unroll
    for (int f = 0; f < 7; ++f) P4W(0, f);
    wait_flags((const GAS u32*)(ws + WS_FX) + (t4 * 4 + w) * 32 + (ln & 31), tag);
    STAMP(13);
    auto ld_y = [&](int n) { yf[n] = __builtin_amdgcn_raw_buffer_load_b128(rs, ln * 16, WS_X1 + ((t4 * 4 + w) * 32 + ((n + 4 * rot4) & 31)) * 1024, 16); };
#pragma unroll
    for (int n = 0; n < 5; ++n) ld_y(n);
    const u32 a_done = lbase + L_DONE + w * 4;
    u32x4 land;
    static_for<0, 32>([&](auto nc) {
      constexpr int n = nc.value, m = n + 1;
      if constexpr (n + 2 >= 23 && n + 2 < 32) yf[n + 2] = lds_ld128(L, L_YL + (w * 9 + (n + 2 - 23)) * 1024 + ln * 16);
#pragma unroll
      for (int f = 0; f < 7; ++f) {
        SB();
        ag[f] = MFMA(wf_[f], asbf(yf[n]), ag[f]);
        if ((m & 3) == 0 && f == 0) {
          STAMP4(19 + m / 4);
          lds_post(a_done, m / 4);
          if (m >= 12) { PIN(ag[0]); if (!all_ge(land, m / 4)) lds_wait(L, L_LAND, m / 4); }
        }
        if ((m & 3) == 3 && m >= 11 && f == 0) land = *(volatile LAS u32x4*)(L + L_LAND);
        if (m < 32) P4W(m, f);
        if constexpr (n < 6) { if (f & 1) ld_y(5 + 3 * n + (f >> 1)); }
      }
#pragma unroll
      for (int f = 0; f < 7; ++f) PIN(ag[f]);
    });
    const u32x4 bst = lds_ld128(L, L_BST + (32 * w + (ln & 31)) * 16);
    const u32x4 bfr = {ln < 32 ? bst[0] : 0u, ln < 32 ? bst[1] : 0u, 0u, 0u};
#pragma unroll
    for (int f = 0; f < 7; ++f) {
      const u32x2 uv = *(LAS u32x2*)(L + L_UVT + (f * 32 + (ln & 31)) * 8);
      ag[f] = MFMA(asbf((u32x4){uv[0], uv[1], 0u, 0u}), asbf(bfr), ag[f]);
    }
    const float rs_ = __uint_as_float(bst[2]), sd_ = bf_lo(bst[1]);
    const float k1 = -2.0f * 0.7978845608028654f * 1.4426950408889634f * rs_, k3 = k1 * 0.044715f * rs_ * rs_, i2 = sd_ * sd_;
    STAMP(15);
    PIN(ln);
    const u32 ao = WS_ACT + ((t4 * 4 + w) * 112 + 7 * j4) * 1024 + ln * 16;
#pragma unroll
    for (int f = 0; f < 7; ++f) {
      u32x4 o;
#pragma unroll
      for (int k = 0; k < 4; ++k) { const f32x2 a = geglu2((f32x2){ag[f][2 * k], ag[f][2 * k + 1]}, (f32x2){ag[f][8 + 2 * k], ag[f][9 + 2 * k]}, k1, k3, i2); o[k] = pk(a[0], a[1]); }
      __builtin_amdgcn_raw_buffer_store_b128(o, rs, ao + f * 1024, 0, 16);
    }
    WAITV(0);
    if (ln == 0) __hip_atomic_store((GAS u32*)(ws + WS_FA) + (t4 * 4 + w) * 32 + j4, tag, RLX_AGENT);
    STAMP(16);
  } else {
    auto load_wd = [&](int i, int li) {
      int ks = rot5; PINS(ks);
      ks += i; ks -= ks >= 28 ? 28 : 0;
      const u32 so = cg * 128 + (w4 * 448 + ks * 16) * 1024, ld = lbase + wd_base(li) + w4 * 2048;
      glds(srd_d, w8o, so, ld);
      glds(srd_d, w8o, so + 8192, ld + 1024);
    };
    load_gu(3, gu_slot(3));
    {
      wait_flags((const GAS u32*)(ws + WS_FX) + (t4 * 4 + w4) * 32 + r32, tag);
      lds_post(lbase + L_LAND + w4 * 4, 3);
      STAMP(13);
      const u32 so = (t4 * 128 + 32 * w4 + r32) * 64 + hh * 32;
      const u32x4 s0 = __builtin_amdgcn_raw_buffer_load_b128(rs, so, WS_ST, 16), s1 = __builtin_amdgcn_raw_buffer_load_b128(rs, so + 16, WS_ST, 16);
      u32x4 yl[9];
#pragma unroll
      for (int k = 0; k < 9; ++k) yl[k] = __builtin_amdgcn_raw_buffer_load_b128(rs, lane * 16, WS_X1 + ((t4 * 4 + w4) * 32 + ((23 + k + 4 * rot4) & 31)) * 1024, 16);
      SB();
      auto xsum = [&](float v) {
        auto r_ = __builtin_amdgcn_permlane32_swap(__float_as_uint(v), __float_as_uint(v), false, false);
        return __uint_as_float(r_[0]) + __uint_as_float(r_[1]);
      };
      const float a0 = __uint_as_float(s0[0]), a1 = __uint_as_float(s0[2]), a2 = __uint_as_float(s1[0]), a3 = __uint_as_float(s1[2]);
      const float me = xsum((a0 + a1) + (a2 + a3)) * (1.f / 512.f);
      const float d0 = a0 * (1.f / 64.f) - me, d1 = a1 * (1.f / 64.f) - me, d2 = a2 * (1.f / 64.f) - me, d3 = a3 * (1.f / 64.f) - me;
      const float m2 = (__uint_as_float(s0[1]) + __uint_as_float(s0[3])) + (__uint_as_float(s1[1]) + __uint_as_float(s1[3]));
      float mb, sd;
      ln_scal(me, xsum(m2 + 64.f * ((d0 * d0 + d1 * d1) + (d2 * d2 + d3 * d3))) * (1.f / 512.f), mb, sd);
      if (lane < 32) lds_st128(L, L_BST + (32 * w4 + lane) * 16, (u32x4){pk(-mb, -mb), pk(sd, sd), __float_as_uint(__builtin_amdgcn_rcpf(sd)), 0u});
#pragma unroll
      for (int k = 0; k < 9; ++k) lds_st128(L, L_YL + (w4 * 9 + k) * 1024 + lane * 16, yl[k]);
    }
    {
      const u32 a_land = lbase + L_LAND + w4 * 4;
      STAMP4(20);
      lds_wait(L, L_DONE, 1); load_gu(4, gu_slot(0)); STAMP4(21);
      lds_wait(L, L_DONE, 2); load_gu(5, gu_slot(1)); WAITV(7); lds_post(a_land, 4); STAMP4(22);
      lds_wait(L, L_DONE, 3); load_gu(6, gu_slot(2)); WAITV(7); lds_post(a_land, 5); STAMP4(23);
      lds_wait(L, L_DONE, 4); load_gu(7, gu_slot(3)); WAITV(7); lds_post(a_land, 6); WAITV(0); lds_post(a_land, 7); STAMP4(24);
      wait_flags((const GAS u32*)(ws + WS_FUV) + j4 * 64 + lane, tag);
      {
        u32x4 pt[16];
        const u32 o = lane < 28 ? (w4 * 28 + lane) * 16 : 0x7fffffffu;
#pragma unroll
        for (int k = 0; k < 16; ++k) pt[k] = __builtin_amdgcn_raw_buffer_load_b128(rs, o, WS_UV + (j4 * 16 + k) * 2048, 16);
        SB();
        f32x4 a = {};
#pragma unroll
        for (int k = 0; k < 16; ++k) {
          const f32x4 t_ = __builtin_bit_cast(f32x4, pt[k]);
#pragma unroll
          for (int e = 0; e < 4; ++e) { float y_ = a[e] + t_[e]; asm("" : "+v"(y_)); a[e] = y_; }
        }
        if (lane < 28) {
          const u32 vi = (w4 * 28 + lane) * 4;
#pragma unroll
          for (int e = 0; e < 4; ++e) {
            const float hi = rb(a[e]);
            *(LAS u32*)(L + L_UVT + ((vi >> 6) * 32 + (vi & 31) + e) * 8 + ((vi >> 5) & 1) * 4) = pk(hi, a[e] - hi);
          }
        }
      }
      lds_post(a_land, 8);
      lds_wait(L, L_DONE, 5); load_wd(0, 0); load_wd(1, 1); load_wd(2, 2); STAMP4(25);
      lds_wait(L, L_DONE, 6); load_wd(3, 3); load_wd(4, 4); load_wd(5, 5);
      lds_wait(L, L_DONE, 7); load_wd(6, 6); load_wd(7, 7); load_wd(8, 8); load_wd(9, 9);
      lds_wait(L, L_DONE, 8);
    }
    STAMP(15);
    for (int i = 10; i < 18; ++i) load_wd(i, i);
    WAITV(16);
    bf16x8 wp0[10], wp1[10];
    u32 dA0 = w4 * 2048 + wt0, dA1 = w4 * 2048 + wt1, dB0 = dA0 + 92 * KB, dB1 = dA1 + 92 * KB, dC0 = dA0 + 28 * KB, dC1 = dA1 + 28 * KB;
    PIN(dA0); PIN(dA1); PIN(dB0); PIN(dB1); PIN(dC0); PIN(dC1);
#define WD0(li) ((li) < 3 ? dA0 + (li) * 8192 : (li) < 10 ? dB0 + ((li) - 3) * 8192 : dC0 + ((li) - 10) * 8192)
#define WD1(li) ((li) < 3 ? dA1 + (li) * 8192 : (li) < 10 ? dB1 + ((li) - 3) * 8192 : dC1 + ((li) - 10) * 8192)
#pragma unroll
    for (int i = 0; i < 10; ++i) { wp0[i] = lds_tr(L, WD0(i), WD0(i) + 1024); wp1[i] = lds_tr(L, WD1(i), WD1(i) + 1024); }
    asm volatile("s_waitcnt lgkmcnt(0)" ::: "memory");
    for (int i = 0; i < 10; ++i) load_wd(18 + i, i);
    STAMP(16);
    wait_flags((const GAS u32*)(ws + WS_FA) + (t3 * 4 + 2 * hf + ((lane >> 2) & 1)) * 32 + 4 * w4 + (lane & 3), tag);
    STAMP(17);
    const u32 ao = WS_ACT + ((t3 * 4 + 2 * hf) * 112 + 28 * w4) * 1024 + lane * 16;
    u32x4 af0[28], af1[28];
    auto ld_act = [&](int i) {
      int ks = i + rot5; ks -= ks >= 28 ? 28 : 0;
      af0[i] = __builtin_amdgcn_raw_buffer_load_b128(rs, ao, ks * 1024, 16);
      af1[i] = __builtin_amdgcn_raw_buffer_load_b128(rs, ao + 112 * 1024, ks * 1024, 16);
    };
#pragma unroll
    for (int i = 0; i < 7; ++i) ld_act(i);
    f32x16 a00 = {}, a01 = {}, a10 = {}, a11 = {};
#pragma unroll
    for (int i = 0; i < 28; ++i) {
      SB();
      a00 = MFMA(wp0[i % 10], asbf(af0[i]), a00);
      a01 = MFMA(wp1[i % 10], asbf(af0[i]), a01);
      SB();
      a10 = MFMA(wp0[i % 10], asbf(af1[i]), a10);
      if (i < 5) { ld_act(7 + 2 * i); ld_act(8 + 2 * i); }
      else if (i + 12 < 28) ld_act(i + 12);
      SB();
      a11 = MFMA(wp1[i % 10], asbf(af1[i]), a11);
      if (i >= 7 && i + 3 < 28) {
        const int li = i + 3 < 18 ? i + 3 : i - 15;
        wp0[(i + 3) % 10] = lds_tr(L, WD0(li), WD0(li) + 1024);
        wp1[(i + 3) % 10] = lds_tr(L, WD1(li), WD1(li) + 1024);
      }
      PIN(a00); PIN(a01); PIN(a10); PIN(a11);
    }
    STAMP(18);
    red_write(28 * KB + w4 * 2048, 8192, a00, a01);
    red_write(28 * KB + w4 * 2048 + 32768, 8192, a10, a11);
  }
  BAR();
  {
    f32x4 lo, hi;
    red_read(28 * KB, 8192, 2048, lo, hi);
    u32 ln = lane; PIN(ln);
    const f32x4 gb0 = lds_ldf4(L, L_MISC + M_GB + (ln & 7) * 32), gb1 = lds_ldf4(L, L_MISC + M_GB + (ln & 7) * 32 + 16);
    u32x4 o;
    o[0] = pk(bf_lo(x1p[0]) + rb(gb0[0] * rb(lo[0])), bf_hi(x1p[0]) + rb(gb0[1] * rb(lo[1])));
    o[1] = pk(bf_lo(x1p[1]) + rb(gb0[2] * rb(lo[2])), bf_hi(x1p[1]) + rb(gb0[3] * rb(lo[3])));
    o[2] = pk(bf_lo(x1p[2]) + rb(gb1[0] * rb(hi[0])), bf_hi(x1p[2]) + rb(gb1[1] * rb(hi[1])));
    o[3] = pk(bf_lo(x1p[3]) + rb(gb1[2] * rb(hi[2])), bf_hi(x1p[3]) + rb(gb1[3] * rb(hi[3])));
    *(GAS u32x4*)((GAS char*)out + (t3 * 128 + hf * 64 + 8 * w) * 1024 + cg * 128 + (ln >> 3) * 1024 + (ln & 7) * 16) = o;
  }
  STAMP(19);
  asm volatile(".Ladaln_end:");
}

extern "C" void kernel_launch(void* const* d_in, const int* in_sizes, int n_in, void* d_out, int out_size, void* d_ws, size_t ws_size,
                              hipStream_t stream) {
  adaln_block<<<256, 512, 0, stream>>>((const u16*)d_in[0], (const u16*)d_in[1], (const u16*)d_in[2], (const u16*)d_in[4], (const u16*)d_in[5],
                                       (const u16*)d_in[6], (const u16*)d_in[7], (const u16*)d_in[8], (u16*)d_out, (char*)d_ws);
}
```

```cpp
#include <hip/hip_runtime.h>
#include <stdint.h>

typedef unsigned short u16;
typedef unsigned int u32;
typedef unsigned long long u64;
typedef __attribute__((ext_vector_type(8))) __bf16 bf16x8;
typedef __attribute__((ext_vector_type(2))) __bf16 bf16x2;
typedef __attribute__((ext_vector_type(4))) short s16x4;
typedef __attribute__((ext_vector_type(8))) short s16x8;
typedef __attribute__((ext_vector_type(16))) float f32x16;
typedef __attribute__((ext_vector_type(4))) float f32x4;
typedef __attribute__((ext_vector_type(2))) float f32x2;
typedef __attribute__((ext_vector_type(4))) u32 u32x4;
typedef __attribute__((ext_vector_type(2))) u32 u32x2;
#define LAS __attribute__((address_space(3)))
#define GAS __attribute__((address_space(1)))
#define DI __device__ __forceinline__
#define RLX_AGENT __ATOMIC_RELAXED, __HIP_MEMORY_SCOPE_AGENT

constexpr u32 WS_EPOCH = 0;
constexpr u32 WS_ARR = 1024;
constexpr u32 WS_MODG = 4096;
constexpr u32 WS_FKV = 16384;
constexpr u32 WS_FO = WS_FKV + 256 * 64;
constexpr u32 WS_FX = WS_FO + 64 * 128;
constexpr u32 WS_FA = WS_FX + 64 * 128;
constexpr u32 WS_FUV = WS_FA + 64 * 128;
constexpr u32 WS_ST = 65536;
constexpr u32 WS_UV = 196608;
constexpr u32 WS_KV = 1048576;
constexpr u32 WS_O = WS_KV + 4194304;
constexpr u32 WS_X1 = WS_O + 2097152;
constexpr u32 WS_ACT = WS_X1 + 2097152;
constexpr u32 WS_DBG = WS_ACT + 7340032;
constexpr u32 WS_BYTES = WS_DBG + 1048576;
static_assert(WS_FUV + 16 * 256 <= WS_ST && WS_UV + 256 * 2048 <= WS_KV, "workspace map");

constexpr int SPIN_MAX = 1 << 17;
constexpr u32 KB = 1024;
constexpr u32 L_WO = 0;
constexpr u32 L_KV = 64 * KB;
constexpr u32 L_QX = 144 * KB;
constexpr u32 L_AR1 = 120 * KB;
constexpr u32 L_YL = 0;
constexpr u32 L_MISC = 148 * KB;
constexpr u32 L_C1 = L_MISC, L_SH = L_MISC + 2048, L_C1B = L_MISC + 4096, L_SHB = L_MISC + 6144, M_C1B = 4096;
constexpr u32 L_UVT = L_C1, L_BST = L_SH;
constexpr u32 L_ONES = L_MISC + 8192;
constexpr u32 L_GA = L_ONES + 256, M_GA = 8192 + 256, M_GB = 8192 + 512;
constexpr u32 L_DONE = L_ONES + 784, L_LAND = L_ONES + 800, L_MODF = L_ONES + 816;
constexpr u32 L_TAG = L_ONES + 768, L_JUNK = L_ONES + 1536;
constexpr u32 L_RED = L_MISC + 10240;
constexpr u32 LDS_BYTES = 163840;
DI constexpr u32 gu_slot(int c) { return (c & 3) == 3 ? 36 * KB : 64 * KB + (c & 3) * 28 * KB; }
DI constexpr u32 wd_base(int i) { return i < 14 ? 20 * KB + (i & 1) * 8192u : i < 17 ? 64 * KB + (i - 14) * 8192u : i < 24 ? 92 * KB + (i - 17) * 8192u : i < 27 ? 36 * KB + (i - 24) * 8192u : 0u; }
constexpr u32 CODE_LINES = 16;

constexpr float NEGBIG = -1e30f;
constexpr float QSCALE = 0.25f * 1.4426950408889634f;

template <int I> struct IC { static constexpr int value = I; };
template <int I, int N, class F> DI void static_for(F&& f) { if constexpr (I < N) { f(IC<I>{}); static_for<I + 1, N>(f); } }
DI u32 lane_id() { u32 r; asm volatile("v_mbcnt_lo_u32_b32 %0, -1, 0\n\tv_mbcnt_hi_u32_b32 %0, -1, %0" : "=v"(r)); return r; }
DI float bf_lo(u32 v) { return __uint_as_float(v << 16); }
DI float bf_hi(u32 v) { return __uint_as_float(v & 0xffff0000u); }
DI u32 pk(float a, float b) { f32x2 v = {a, b}; return __builtin_bit_cast(u32, __builtin_convertvector(v, bf16x2)); }
DI float rb(float a) { return bf_lo(pk(a, a)); }
template <int O> DI u32x4 pack8(const f32x16& a) { return (u32x4){pk(a[O], a[O + 1]), pk(a[O + 2], a[O + 3]), pk(a[O + 4], a[O + 5]), pk(a[O + 6], a[O + 7])}; }
DI bf16x8 asbf(u32x4 v) { return __builtin_bit_cast(bf16x8, v); }
#define MFMA(a, b, c) __builtin_amdgcn_mfma_f32_32x32x16_bf16((a), (b), (c), 0, 0, 0)
template <int CTRL, int RM = 0xf> DI float dpp(float v) { return __builtin_bit_cast(float, __builtin_amdgcn_update_dpp(0, __builtin_bit_cast(int, v), CTRL, RM, 0xf, true)); }
DI float sum8(float v) { v += dpp<0xB1>(v); v += dpp<0x4E>(v); v += dpp<0x141>(v); return v; }
DI float dot2(u32 a, u32 b, float c) { return __builtin_amdgcn_fdot2_f32_bf16(__builtin_bit_cast(bf16x2, a), __builtin_bit_cast(bf16x2, b), c, false); }

DI u32x4 lds_ld128(LAS char* L, u32 off) { return *(LAS u32x4*)(L + off); }
DI f32x4 lds_ldf4(LAS char* L, u32 off) { return *(LAS f32x4*)(L + off); }
DI void lds_st128(LAS char* L, u32 off, u32x4 v) { *(LAS u32x4*)(L + off) = v; }
DI bf16x8 lds_tr(LAS char* L, u32 off0, u32 off1) {
  s16x4 a = __builtin_amdgcn_ds_read_tr16_b64_v4i16((LAS s16x4*)(L + off0));
  s16x4 b = __builtin_amdgcn_ds_read_tr16_b64_v4i16((LAS s16x4*)(L + off1));
  return __builtin_bit_cast(bf16x8, __builtin_shufflevector(a, b, 0, 1, 2, 3, 4, 5, 6, 7));
}
DI u32x4 make_srd(const void* p, u32 bytes) { const u64 a = (u64)p; return (u32x4){(u32)a, (u32)(a >> 32), bytes, 0x00020000u}; }
DI void glds(const u32x4& srd, u32 voff, u32 soff, u32 ldsaddr) {
  asm volatile("s_mov_b32 m0, %3\n\ts_nop 0\n\tbuffer_load_dwordx4 %0, %1, %2 offen lds" ::"v"(voff), "s"(srd), "s"(soff), "s"(ldsaddr) : "memory");
}
DI void glds_sc1(const u32x4& srd, u32 voff, u32 soff, u32 ldsaddr) {
  asm volatile("s_mov_b32 m0, %3\n\ts_nop 0\n\tbuffer_load_dwordx4 %0, %1, %2 offen sc1 lds" ::"v"(voff), "s"(srd), "s"(soff), "s"(ldsaddr) : "memory");
}
DI void touch(const u32x4& srd, u32 voff, u32 ldsaddr) {
  asm volatile("s_mov_b32 m0, %2\n\ts_nop 0\n\tbuffer_load_dword %0, %1, 0 offen lds" ::"v"(voff), "s"(srd), "s"(ldsaddr) : "memory");
}
DI void lds_post(u32 addr, u32 val) {
  asm volatile("s_mov_b64 exec, 1\n\tds_write_b32 %0, %1\n\ts_mov_b64 exec, -1" ::"v"(addr), "v"(val) : "memory");
}
DI bool all_ge(u32x4 v, u32 k) { return __builtin_amdgcn_readfirstlane(min(min(v[0], v[1]), min(v[2], v[3]))) >= k; }
DI void lds_wait(LAS char* L, u32 off, u32 k) {
#pragma nounroll
  for (int it = 0; it < (SPIN_MAX << 4); ++it) {
    if (all_ge(*(volatile LAS u32x4*)(L + off), k)) break;
    __builtin_amdgcn_s_sleep(3);
  }
  asm volatile("" ::: "memory");
}
#define BAR() asm volatile("s_waitcnt lgkmcnt(0)\n\ts_barrier" ::: "memory")
#define WAITV_BAR(N) asm volatile("s_waitcnt vmcnt(%0) lgkmcnt(0)\n\ts_barrier" ::"i"(N) : "memory")
#define WAITV(N) asm volatile("s_waitcnt vmcnt(%0)" ::"i"(N) : "memory")
#define SB() __builtin_amdgcn_sched_barrier(0)
#define PIN(x) asm volatile("" : "+v"(x))
#define PINS(x) asm volatile("" : "+s"(x))
#define ORDER() __builtin_amdgcn_fence(__ATOMIC_ACQUIRE, "wavefront")

DI void wait_flags(const GAS u32* p, u32 tag) {
#pragma nounroll
  for (int it = 0; it < SPIN_MAX; ++it) {
    u32 v = __hip_atomic_load(p, RLX_AGENT);
    if (__all(v == tag)) break;
    __builtin_amdgcn_s_sleep(4);
  }
  ORDER();
}
template <int N> DI void gather_gran(const GAS u64* g, u32 tag, u32 (&v)[N], int stride) {
#pragma nounroll
  for (int it = 0; it < SPIN_MAX; ++it) {
    bool ok = true;
#pragma unroll
    for (int k = 0; k < N; ++k) { u64 x = __hip_atomic_load(g + k * stride, RLX_AGENT); v[k] = (u32)x; ok &= (u32)(x >> 32) == tag; }
    if (__all(ok)) break;
    __builtin_amdgcn_s_sleep(1);
  }
  ORDER();
}

#define STAMPC(n) do {} while (0)
#define STAMP(n) do {} while (0)

#define STAMP4(n) do {} while (0)
#define STAMP1(n) do {} while (0)
DI void ln_scal(float mean, float var, float& mb, float& sd) {
  mb = rb(mean);
  sd = rb(__builtin_amdgcn_sqrtf(rb(rb(fmaxf(var, 0.f)) + 1e-5f)));
}
DI float fma1(float a, float b, float c) { float r = fmaf(a, b, c); asm("" : "+v"(r)); return r; }
typedef __attribute__((ext_vector_type(2))) _Float16 f16x2;
DI u32x4 znorm8(u32x4 x, float r, float mr) {
  u32x4 o;
#pragma unroll
  for (int e = 0; e < 4; ++e) {
    const f32x2 v = {fmaf(bf_lo(x[e]), r, -mr), fmaf(bf_hi(x[e]), r, -mr)};
    o[e] = __builtin_bit_cast(u32, __builtin_convertvector(v, f16x2));
  }
  return o;
}
DI u32 xform2(u32 z_, u32 c_, u32 s_) {
  const f16x2 z = __builtin_bit_cast(f16x2, z_), c = __builtin_bit_cast(f16x2, c_), s = __builtin_bit_cast(f16x2, s_);
  return pk(fma1((float)z[0], (float)c[0], (float)s[0]), fma1((float)z[1], (float)c[1], (float)s[1]));
}
DI u32x4 xform8(u32x4 x, u32x4 c, u32x4 s) { return (u32x4){xform2(x[0], c[0], s[0]), xform2(x[1], c[1], s[1]), xform2(x[2], c[2], s[2]), xform2(x[3], c[3], s[3])}; }
DI u32 pkh(float a, float b) { const f32x2 v = {a, b}; return __builtin_bit_cast(u32, __builtin_convertvector(v, f16x2)); }
DI f32x2 geglu2(f32x2 ag, f32x2 au, float k1, float k3, float i2) {
  const f32x2 a = ag * (ag * ag * k3 + k1);
  const f32x2 d = (f32x2){__builtin_amdgcn_exp2f(a[0]), __builtin_amdgcn_exp2f(a[1])} * i2 + i2;
  return ag * au * (f32x2){__builtin_amdgcn_rcpf(d[0]), __builtin_amdgcn_rcpf(d[1])};
}

__global__ __launch_bounds__(512, 2) void adaln_block(const u16* __restrict__ x, const u16* __restrict__ wqkv, const u16* __restrict__ wo,
                                                      const u16* __restrict__ wg, const u16* __restrict__ wu, const u16* __restrict__ wd,
                                                      const u16* __restrict__ cvec, const u16* __restrict__ wm, u16* __restrict__ out,
                                                      char* __restrict__ ws) {
  __shared__ __attribute__((aligned(1024))) char lds[LDS_BYTES];
  LAS char* L = (LAS char*)lds;
  const u32 lbase = (u32)(uintptr_t)L;
  const int tid = threadIdx.x, lane = tid & 63;
  const int w = __builtin_amdgcn_readfirstlane(tid >> 6), w4 = w & 3;
  const bool mw = w < 4;
  const int b = blockIdx.x, xcd = b & 7, ii = b >> 3;
  const int u1 = 31 - (8 * (xcd >> 1) + (ii >> 2)), q1 = 4 * (xcd & 1) + (ii & 3);
  const int t3 = 2 * xcd + (ii >> 4), hf = (ii >> 3) & 1, cg = ii & 7;
  const int t4 = 8 * (xcd >> 2) + (ii >> 2), j4 = 4 * (xcd & 3) + (ii & 3);
  const int rot1 = (u1 + 2 * q1) & 7;
  const int rot4 = ii >> 2, rot5 = 7 * (ii >> 3);
  const int r32 = lane & 31, hh = lane >> 5, blk = (lane >> 4) & 1, q4 = (lane & 15) >> 2, p4 = lane & 3, l3 = lane >> 3, l7 = lane & 7;
  const __amdgpu_buffer_rsrc_t rs = __builtin_amdgcn_make_buffer_rsrc(ws, 0, WS_BYTES, 0x00020000);
  STAMP(0);

  u32 tag = 0;
  const int orow = t3 * 128 + hf * 64 + 8 * w + l3;
  const int mg = (31 - u1) * 8 + q1;
  const u32x4 srd_q = make_srd(wqkv, 512 * 3072);
  const u32 q8o = l3 * 3072u + ((l7 ^ (((lane >> 4) & 1) << 2)) << 4);
  auto load_wq = [&](int c) {
    const u32 so = ((c + rot1) & 7) * 196608 + w * 1024 + q1 * 128;
#pragma unroll
    for (int e = 0; e < 8; ++e) glds(srd_q, q8o, so + e * 24576, lbase + c * 24576 + w * 8192 + e * 1024);
  };
  u32x4 X[16];
  u32x4 xr;
  if (mw) {
    u32x4 M[16];
    u32 cv = 0;
    if (mg < 48) {
      const GAS char* mb = (const GAS char*)wm + mg * 128 + (128 * w + 16 * l3) * 6144 + l7 * 16;
      cv = ((const GAS u32*)cvec)[64 * w + lane];
#pragma unroll
      for (int n = 0; n < 16; ++n) M[n] = *(const GAS u32x4*)(mb + n * 6144);
    }
    const u32 ep = __hip_atomic_load((const GAS u32*)(ws + WS_EPOCH), RLX_AGENT);
    if (lane < 12) *(LAS u32*)(L + L_DONE + lane * 4) = 0u;
    SB();
    BAR();
    STAMP(1);
    if (mg < 48) {
      {
        const float c0 = bf_lo(cv), c1 = bf_hi(cv);
        *(LAS u32*)(L + L_RED + 1024 + w * 256 + lane * 4) = pk(c0 * __builtin_amdgcn_rcpf(1.0f + __builtin_amdgcn_exp2f(-1.4426950408889634f * c0)),
                                                               c1 * __builtin_amdgcn_rcpf(1.0f + __builtin_amdgcn_exp2f(-1.4426950408889634f * c1)));
      }
      const u32x4 s0 = lds_ld128(L, L_RED + 1024 + w * 256 + l3 * 32), s1 = lds_ld128(L, L_RED + 1024 + w * 256 + l3 * 32 + 16);
      f32x2 a[4] = {};
#pragma unroll
      for (int n = 0; n < 8; ++n) {
        const u32 sp = n < 4 ? s0[n & 3] : s1[n & 3];
#pragma unroll
        for (int e = 0; e < 4; ++e) {
          const u32 r0 = M[2 * n][e], r1 = M[2 * n + 1][e];
          a[e][0] = dot2(__builtin_amdgcn_perm(r1, r0, 0x05040100u), sp, a[e][0]);
          a[e][1] = dot2(__builtin_amdgcn_perm(r1, r0, 0x07060302u), sp, a[e][1]);
        }
      }
      float v[8] = {a[0][0], a[0][1], a[1][0], a[1][1], a[2][0], a[2][1], a[3][0], a[3][1]};
#pragma unroll
      for (int e = 0; e < 8; ++e) {
        v[e] += dpp<0x128>(v[e]);
        auto r1_ = __builtin_amdgcn_permlane16_swap(__float_as_uint(v[e]), __float_as_uint(v[e]), false, false);
        v[e] = __uint_as_float(r1_[0]) + __uint_as_float(r1_[1]);
        auto r2_ = __builtin_amdgcn_permlane32_swap(__float_as_uint(v[e]), __float_as_uint(v[e]), false, false);
        v[e] = __uint_as_float(r2_[0]) + __uint_as_float(r2_[1]);
      }
      if (lane < 8) {
        *(LAS f32x4*)(L + L_RED + w * 256 + lane * 32) = (f32x4){v[0], v[1], v[2], v[3]};
        *(LAS f32x4*)(L + L_RED + w * 256 + lane * 32 + 16) = (f32x4){v[4], v[5], v[6], v[7]};
      }
      lds_post(lbase + L_MODF + w * 4, 1);
      if (w == 3) {
        lds_wait(L, L_MODF, 1);
        float s = 0.f;
#pragma unroll
        for (int k = 0; k < 4; ++k) s += *(LAS float*)(L + L_RED + k * 256 + lane * 4);
        const float o = dpp<0xB1>(s);
        const u32 tg = __builtin_amdgcn_readfirstlane(ep) + 1u;
        if (!(lane & 1)) __hip_atomic_store((GAS u64*)(ws + WS_MODG) + 32 * mg + (lane >> 1), ((u64)tg << 32) | pk(s, o), RLX_AGENT);
        STAMP(28);
      }
    }
    xr = *(const GAS u32x4*)((const GAS char*)x + orow * 1024 + cg * 128 + l7 * 16);
    if (w < 3) {
      tag = __builtin_amdgcn_readfirstlane(ep) + 1u;
      load_wq(0); load_wq(1); load_wq(2);
    } else {
      u64 pc;
      int len;
      asm volatile("s_getpc_b64 %0\n.Ladaln_pf:\n\ts_mov_b32 %1, .Ladaln_end-.Ladaln_pf" : "=s"(pc), "=s"(len));
      touch(make_srd((const void*)pc, len > 0 ? (u32)len : 0u), (ii * CODE_LINES + (lane < CODE_LINES ? lane : 0)) * 128u, lbase + L_JUNK);
      if (lane < 32) lds_st128(L, L_ONES + (lane & 15) * 16 + (lane >> 4) * 1024, (u32x4){0x3f803f80u, 0x3f803f80u, 0x3f803f80u, 0x3f803f80u});
      tag = __builtin_amdgcn_readfirstlane(ep) + 1u;
      if (lane == 0) *(LAS u32*)(L + L_TAG) = tag;
      u32x4 v[4];
#pragma nounroll
      for (int it = 0; it < SPIN_MAX; ++it) {
        bool ok = true;
#pragma unroll
        for (int k = 0; k < 4; ++k) { v[k] = __builtin_amdgcn_raw_buffer_load_b128(rs, lane * 16, WS_MODG + k * 1024, 16); ok &= v[k][1] == tag && v[k][3] == tag; }
        if (__all(ok)) break;
        __builtin_amdgcn_s_sleep(2);
      }
#pragma unroll
      for (int k = 0; k < 2; ++k) {
        const u32 o = L_C1 + ((lane + 64 * k) >> 1) * 32 + (lane & 1) * 8;
        *(LAS u32x2*)(L + o + 16) = (u32x2){pkh(bf_lo(v[k][0]), bf_hi(v[k][0])), pkh(bf_lo(v[k][2]), bf_hi(v[k][2]))};
        *(LAS u32x2*)(L + o) = (u32x2){pkh(rb(1.f + bf_lo(v[2 + k][0])), rb(1.f + bf_hi(v[2 + k][0]))), pkh(rb(1.f + bf_lo(v[2 + k][2])), rb(1.f + bf_hi(v[2 + k][2])))};
      }
    }
    STAMP(3);
  } else {
    const __amdgpu_buffer_rsrc_t rx = __builtin_amdgcn_make_buffer_rsrc((void*)x, 0, 2048 * 1024, 0x00020000);
    if (mg < 48) BAR();
#pragma unroll
    for (int c = 0; c < 8; ++c)
#pragma unroll
      for (int g = 0; g < 2; ++g)
        X[2 * c + g] = __builtin_amdgcn_raw_buffer_load_b128(rx, l3 * 1024u + l7 * 16u, (u1 * 64 + 16 * w4 + 8 * g) * 1024 + ((c + rot1) & 7) * 128, 0);
    xr = *(const GAS u32x4*)((const GAS char*)x + orow * 1024 + cg * 128 + l7 * 16);
    SB();
    if (mg >= 48) BAR();
    STAMP(2);
    float sm[2] = {}, sq[2] = {};
#pragma unroll
    for (int c = 0; c < 8; ++c) {
      SB();
#pragma unroll
      for (int e = 0; e < 4; ++e)
#pragma unroll
        for (int g = 0; g < 2; ++g) { sm[g] = dot2(X[2 * c + g][e], 0x3f803f80u, sm[g]); sq[g] = dot2(X[2 * c + g][e], X[2 * c + g][e], sq[g]); }
    }
    float rr[2], mr[2];
#pragma unroll
    for (int g = 0; g < 2; ++g) {
      const float me = sum8(sm[g]) * (1.f / 512.f);
      float mb, sd;
      ln_scal(me, sum8(sq[g]) * (1.f / 512.f) - me * me, mb, sd);
      rr[g] = __builtin_amdgcn_rcpf(sd); mr[g] = mb * rr[g];
    }
#pragma unroll
    for (int n = 0; n < 16; ++n) { X[n] = znorm8(X[n], rr[n & 1], mr[n & 1]); PIN(X[n]); }
    STAMP(3);
  }
  BAR();
  STAMP(4);
  STAMPC(29);

  u32 cso = L_MISC + l7 * 32;
  PIN(cso);
  const u32x4 srd_g = make_srd(wg, 512 * 3584), srd_u = make_srd(wu, 512 * 3584), srd_d = make_srd(wd, 1792 * 1024), srd_o = make_srd(wo, 512 * 1024);

  u32 guo[7];
  const u32x4 srd_3 = w4 < 2 ? srd_g : srd_u;
  auto load_gu = [&](int c, u32 slot) {
    int r_ = rot4; PINS(r_);
    const u32 so = j4 * 224 + ((c + r_) & 7) * (64 * 3584), ld = lbase + slot + w4 * 1024;
#pragma unroll
    for (int n = 0; n < 7; ++n) glds(n < 3 ? srd_g : n == 3 ? srd_3 : srd_u, guo[n], so, ld + n * 4096);
  };
  const u32 w8o = l3 * 1024u + ((l7 ^ (((lane >> 4) & 1) << 2)) << 4);
  const int hp = w4 >> 1, hd = w >> 2, qb = (w ^ hd) & 1;
  bf16x8 qf;
  if (mw) {
    f32x16 aq = {}, ak = {}, av = {};
    u32 ard[4];
    {
      const int row = 32 * qb + r32, f = (row & 7) ^ ((row >> 3) & 1);
#pragma unroll
      for (int s = 0; s < 4; ++s) { ard[s] = L_AR1 + row * 128 + ((((2 * s) | hh) ^ f) << 4); PIN(ard[s]); }
    }
    u32 wl[3];
    wl[0] = (8 * hh + q4) * 128 + ((64 * hp + 32 * blk + 8 * p4) ^ ((q4 >> 1) << 6));
    wl[1] = wl[0] + 48 * KB; wl[2] = wl[0] + 96 * KB;
    PIN(wl[1]); PIN(wl[2]);
    bf16x8 hf_[3], qf_[3], kf_[3], vf_[3];
#define P1O(n) (wl[(((n) >> 2) % 5) >> 1] + ((((n) >> 2) % 5) & 1) * 24576 + ((n) & 3) * 2048)
#define P1H(n) hf_[(n) % 3] = asbf(lds_ld128(L, ard[(n) & 3] + (((n) >> 2) % 3) * 8192))
    if (w < 3) { load_wq(3); load_wq(4); }
    WAITV_BAR(32);
#pragma unroll
    for (int n = 0; n < 2; ++n) {
      P1H(n); kf_[n] = lds_tr(L, P1O(n) + 8192, P1O(n) + 8704); vf_[n] = lds_tr(L, P1O(n) + 16384, P1O(n) + 16896); qf_[n] = lds_tr(L, P1O(n), P1O(n) + 512);
    }
#pragma unroll
    for (int n = 0; n < 32; ++n) {
      const int m = n + 2;
      const bool bnd = (m & 3) == 0;
      SB();
      ak = MFMA(kf_[n % 3], hf_[n % 3], ak);
      if (m < 32 && !bnd) { P1H(m); kf_[m % 3] = lds_tr(L, P1O(m) + 8192, P1O(m) + 8704); }
      SB();
      av = MFMA(hf_[n % 3], vf_[n % 3], av);
      if (m < 32 && bnd) {
        PIN(ak); PIN(av);
        STAMP1(20 + m / 4);
        WAITV_BAR(m <= 16 ? 32 - 2 * m : 0);
        P1H(m); kf_[m % 3] = lds_tr(L, P1O(m) + 8192, P1O(m) + 8704);
      }
      if (m < 32) vf_[m % 3] = lds_tr(L, P1O(m) + 16384, P1O(m) + 16896);
      SB();
      aq = MFMA(qf_[n % 3], hf_[n % 3], aq);
      if (m < 32) qf_[m % 3] = lds_tr(L, P1O(m), P1O(m) + 512);
      PIN(ak); PIN(av); PIN(aq);
    }
    STAMP(5);
    STAMPC(30);
    const u32x4 f0 = pack8<0>(ak), f1 = pack8<8>(ak), f2 = pack8<0>(av), f3 = pack8<8>(av);
    const int kvi = ((u1 >> 1) * 16 + 2 * q1 + hp) * 4 + 2 * (u1 & 1) + qb;
    const u32 o = WS_KV + kvi * 4096 + lane * 16;
    __builtin_amdgcn_raw_buffer_store_b128(f0, rs, o, 0, 16);
    __builtin_amdgcn_raw_buffer_store_b128(f1, rs, o + 1024, 0, 16);
    __builtin_amdgcn_raw_buffer_store_b128(f2, rs, o + 2048, 0, 16);
    __builtin_amdgcn_raw_buffer_store_b128(f3, rs, o + 3072, 0, 16);
    aq *= QSCALE;
    qf = asbf(pack8<0>(aq));
    const u32x4 f5 = pack8<8>(aq);
    BAR();
    const u32 ok = L_KV + hp * 40960 + (8 + qb) * 4096 + lane * 16;
    lds_st128(L, ok, f0); lds_st128(L, ok + 1024, f1); lds_st128(L, ok + 2048, f2); lds_st128(L, ok + 3072, f3);
    lds_st128(L, L_QX + w * 1024 + lane * 16, f5);
    BAR();
    WAITV(0);
    if (lane == 0) __hip_atomic_store((GAS u32*)(ws + WS_FKV) + (kvi >> 2) * 16 + (kvi & 3), tag, RLX_AGENT);
  } else {
    tag = __builtin_amdgcn_readfirstlane(*(LAS u32*)(L + L_TAG));
    if (w == 4 && lane == 0) __hip_atomic_store((GAS u32*)(ws + WS_ARR) + b, tag, RLX_AGENT);
    const u32 awE = L_AR1 + (16 * w4 + l3) * 128 + ((l7 ^ l3) << 4), awO = (awE ^ 16u) + 1024;
    u32x4 ca[2], sa[2];
#define CSLOAD(c) do { const u32 o_ = cso + (((c) + rot1) & 7) * 256; ca[(c) & 1] = lds_ld128(L, o_); sa[(c) & 1] = lds_ld128(L, o_ + 16); } while (0)
    CSLOAD(0);
#pragma unroll
    for (int c = 0; c < 8; ++c) {
      if (c < 7) CSLOAD(c + 1);
      SB();
      PIN(X[2 * c]);
      lds_st128(L, awE + (c % 3) * 8192, xform8(X[2 * c], ca[c & 1], sa[c & 1]));
      PIN(X[2 * c + 1]);
      lds_st128(L, awO + (c % 3) * 8192, xform8(X[2 * c + 1], ca[c & 1], sa[c & 1]));
      if (c >= 3 && c <= 5) {
        const u32 so = ((c + 2 + rot1) & 7) * 196608 + q1 * 128;
#pragma unroll
        for (int e = 0; e < 6; ++e) {
          const int pc = w4 * 6 + e;
          glds(srd_q, q8o, so + (pc >> 3) * 1024 + (pc & 7) * 24576, lbase + (c - 3) * 24576 + pc * 1024);
        }
      }
      if (c == 0) BAR();
      if (c >= 2) STAMP1(19 + c);
      if (c >= 2) asm volatile("s_waitcnt vmcnt(%0) lgkmcnt(2)\n\ts_barrier" ::"i"(c == 6 ? 12 : c == 7 ? 6 : 63) : "memory");
    }
    WAITV_BAR(0);
#pragma unroll
    for (int n = 0; n < 7; ++n) {
      const int pc = w4 + 4 * n, up = pc >= 14, e = (pc - 14 * up) * 64 + lane, rho = e / 14, cc = e - 14 * rho;
      guo[n] = (u32)((up ? (rho ^ 4) : rho) * 3584 + cc * 16);
    }
    STAMP(5);
    BAR();
    BAR();
    qf = asbf(lds_ld128(L, L_QX + (w4 ^ 1) * 1024 + lane * 16));
  }
  STAMP(6);

  {
    const u32 ka = L_KV + hp * 40960 + hd * 1024 + lane * 16;
    const bool vdat = (r32 >> 4) == hd;
    const u32 va = vdat ? L_KV + hp * 40960 + 2048 + lane * 16 : L_ONES + (lane & 15) * 16;
    const u32 vs = vdat ? 4096u : 0u;
    f32x16 cm;
#pragma unroll
    for (int g = 0; g < 16; ++g) cm[g] = (g & 3) + 8 * (g >> 2) + 4 * hh <= r32 ? 0.f : NEGBIG;
    const f32x16 zero = {};
    f32x16 O = {};
    float m = NEGBIG;
    auto rowmax = [&](const f32x16& s, float mm) {
#pragma unroll
      for (int g = 0; g < 16; g += 2) mm = fmaxf(fmaxf(mm, s[g]), s[g + 1]);
      return mm;
    };
    auto ldk = [&](int lb) { return asbf(lds_ld128(L, ka + lb * 4096)); };
#pragma nounroll
    for (int st = 0; st < 2; ++st) {
      int lm = 8 + qb, lo = 8, hi = 8 + qb;
      if (st) {
        STAMP(7);
        if (!mw && qb) {
          for (int n = hp * 32; n < hp * 32 + 32; ++n) glds(srd_o, w8o, cg * 128 + n * 8192, lbase + L_WO + n * 1024);
        }
        if (u1 == 0) break;
        if (!qb) {
          const int g0 = 2 * u1 - 8 + 4 * hd, gl = g0 + (lane & 3);
          wait_flags((const GAS u32*)(ws + WS_FKV) + (gl < 0 ? ((u1 >> 1) * 16 + 2 * q1 + hp) * 16 + 2 * (u1 & 1) : ((gl >> 2) * 16 + 2 * q1 + hp) * 16 + (gl & 3)), tag);
          u32x4 nb[16];
#pragma unroll
          for (int k = 0; k < 4; ++k) {
            const int g = g0 + k;
            const u32 so = g < 0 ? 0x7ff00000u : WS_KV + (((g >> 2) * 16 + 2 * q1 + hp) * 4 + (g & 3)) * 4096;
#pragma unroll
            for (int n = 0; n < 4; ++n) nb[4 * k + n] = __builtin_amdgcn_raw_buffer_load_b128(rs, lane * 16 + n * 1024, so, 16);
          }
#pragma unroll
          for (int n = 0; n < 16; ++n) lds_st128(L, L_KV + hp * 40960 + hd * 16384 + n * 1024 + lane * 16, nb[n]);
        }
        const int lmin = 8 - 2 * u1;
#pragma unroll
        for (int g = 0; g < 16; ++g) { float u = lmin > qb ? NEGBIG : NEGBIG - cm[g]; asm("" : "+v"(u)); cm[g] = u; }
        lm = qb; lo = lmin > qb ? lmin : qb + 1; hi = 8;
        BAR();
        STAMP(8);
      }
      bf16x8 kA = ldk(lo), kB = ldk(lo + 1);
      f32x16 sA = MFMA(ldk(lm), qf, cm), sB, sC;
      const bf16x8 k0 = kA;
      float mn = m;
      for (int lb = lo; lb < hi; lb += 2) {
        sB = MFMA(kA, qf, zero);
        sC = MFMA(kB, qf, zero);
        kA = ldk(lb + 2); kB = ldk(lb + 3);
        SB();
        mn = rowmax(sB, mn);
        if (lb + 1 < hi) mn = rowmax(sC, mn);
      }
      mn = rowmax(sA, mn);
      {
        auto r_ = __builtin_amdgcn_permlane32_swap(__float_as_uint(mn), __float_as_uint(mn), false, false);
        mn = fmaxf(__uint_as_float(r_[0]), __uint_as_float(r_[1]));
      }
      u32 vv = va + lm * vs;
      bf16x8 v0 = asbf(lds_ld128(L, vv)), v1 = asbf(lds_ld128(L, vv + 1024));
      f32x16 negm;
#pragma unroll
      for (int g = 0; g < 16; ++g) negm[g] = -mn;
#pragma unroll
      for (int g = 0; g < 16; ++g) { float u = sA[g] - mn; asm("" : "+v"(u)); sA[g] = u; }
      if (st) {
        const float al = __builtin_amdgcn_exp2f(m - mn);
#pragma unroll
        for (int g = 0; g < 16; ++g) { float o = O[g] * al; asm("" : "+v"(o)); O[g] = o; }
      }
      m = mn;
      kA = k0;
      vv = va + lo * vs;
#define P2STEP(CUR, NXT)                                                                      \
  {                                                                                           \
    NXT = MFMA(kA, qf, negm);                                                                 \
    PIN(NXT);                                                                                 \
    kA = ldk(lb + 1);                                                                         \
    const bf16x8 n0_ = asbf(lds_ld128(L, vv)), n1_ = asbf(lds_ld128(L, vv + 1024));           \
    vv += vs;                                                                                 \
    SB();                                                                                     \
    _Pragma("unroll") for (int g = 0; g < 8; ++g) CUR[g] = __builtin_amdgcn_exp2f(CUR[g]);    \
    O = MFMA(v0, asbf(pack8<0>(CUR)), O);                                                     \
    _Pragma("unroll") for (int g = 8; g < 16; ++g) CUR[g] = __builtin_amdgcn_exp2f(CUR[g]);   \
    O = MFMA(v1, asbf(pack8<8>(CUR)), O);                                                     \
    SB();                                                                                     \
    v0 = n0_; v1 = n1_;                                                                       \
  }
      for (int lb = lo;; ++lb) {
        P2STEP(sA, sB);
        if (lb >= hi) break;
        ++lb;
        P2STEP(sB, sA);
        if (lb >= hi) break;
      }
    }
    u32x4 of;
    {
      const float inv = __builtin_amdgcn_rcpf(hd ? O[0] : O[8]);
      float o[8];
#pragma unroll
      for (int g = 0; g < 8; ++g) { o[g] = (hd ? O[8 + g] : O[g]) * inv; asm("" : "+v"(o[g])); }
      of = (u32x4){pk(o[0], o[1]), pk(o[2], o[3]), pk(o[4], o[5]), pk(o[6], o[7])};
    }
    const int oi = (2 * u1 + qb) * 32 + 4 * q1 + 2 * hp + hd;
    __builtin_amdgcn_raw_buffer_store_b128(of, rs, WS_O + oi * 1024 + lane * 16, 0, 16);
    WAITV(0);
    if (lane == 0) __hip_atomic_store((GAS u32*)(ws + WS_FO) + oi, tag, RLX_AGENT);
  }
  STAMP(9);
  {
    if (u1 == 0 && q1 == 0 && w == 7) {
#pragma nounroll
      for (int it = 0; it < SPIN_MAX; ++it) {
        bool ok = true;
#pragma unroll
        for (int k = 0; k < 4; ++k) ok &= __hip_atomic_load((const GAS u32*)(ws + WS_ARR) + lane + 64 * k, RLX_AGENT) == tag;
        if (__all(ok)) break;
        __builtin_amdgcn_s_sleep(1);
      }
      if (lane == 0) __hip_atomic_store((GAS u32*)(ws + WS_EPOCH), tag, RLX_AGENT);
    }
  }
  BAR();

  const u32 wt0 = (4 * hh + q4) * 128 + ((32 * blk + 8 * p4) ^ ((q4 >> 1) << 6)), wt1 = wt0 ^ 64u;
  auto red_write = [&](u32 base, u32 S8, const f32x16& a0, const f32x16& a1) {
    u32 ln = lane_id();
    const u32 o = base + ((ln & 31) >> 3) * S8 + (ln & 7) * 256, sw = (ln & 7) ^ (ln >> 5);
#pragma unroll
    for (int g = 0; g < 4; ++g) {
      *(LAS f32x4*)(L + o + (((2 * g) ^ sw) << 4)) = (f32x4){a0[4 * g], a0[4 * g + 1], a0[4 * g + 2], a0[4 * g + 3]};
      *(LAS f32x4*)(L + o + (((2 * g) ^ sw) << 4) + 128) = (f32x4){a1[4 * g], a1[4 * g + 1], a1[4 * g + 2], a1[4 * g + 3]};
    }
  };
  auto red_read = [&](u32 base, u32 S8, u32 SK, f32x4& lo, f32x4& hi) {
    u32 ln = lane_id();
    const u32 o0 = base + w * S8 + (ln >> 3) * 256 + (((2 * (ln & 7)) ^ (ln >> 3)) << 4), o1 = o0 ^ 16u;
    lo = lds_ldf4(L, o0); hi = lds_ldf4(L, o1);
#pragma unroll
    for (int k = 1; k < 4; ++k) { lo += lds_ldf4(L, o0 + k * SK); hi += lds_ldf4(L, o1 + k * SK); }
  };
  u32x4 x1p;
  {
    if (mw) {
      bf16x8 wa[8], wb[8];
#pragma unroll
      for (int i = 0; i < 8; ++i) {
        const u32 o = L_WO + w * 16384 + i * 2048;
        wa[i] = lds_tr(L, o + wt0, o + wt0 + 1024); wb[i] = lds_tr(L, o + wt1, o + wt1 + 1024);
      }
      wait_flags((const GAS u32*)(ws + WS_FO) + (t3 * 4 + 2 * hf + (l3 & 1)) * 32 + 8 * w + l7, tag);
      STAMP(10);
      u32x4 of0[8], of1[8];
      const u32 oo = WS_O + ((t3 * 4 + 2 * hf) * 32 + 8 * w) * 1024 + lane * 16;
#pragma unroll
      for (int i = 0; i < 8; ++i) {
        of0[i] = __builtin_amdgcn_raw_buffer_load_b128(rs, oo + i * 1024, 0, 16);
        of1[i] = __builtin_amdgcn_raw_buffer_load_b128(rs, oo + 32768 + i * 1024, 0, 16);
      }
      SB();
      f32x16 a00 = {}, a01 = {}, a10 = {}, a11 = {};
#pragma unroll
      for (int i = 0; i < 8; ++i) {
        a00 = MFMA(wa[i], asbf(of0[i]), a00); a01 = MFMA(wb[i], asbf(of0[i]), a01);
        a10 = MFMA(wa[i], asbf(of1[i]), a10); a11 = MFMA(wb[i], asbf(of1[i]), a11);
      }
      STAMP(11);
      red_write(L_WO + w * 16384, 2048, a00, a01);
      red_write(L_WO + w * 16384 + 8192, 2048, a10, a11);
    } else {
      load_gu(0, gu_slot(0));
      u32x4 bv[2];
      {
        u32x4 q[4];
        u32 v[3];
        const GAS u64* g = (const GAS u64*)(ws + WS_MODG);
        const u32 qo = (((r32 & 1) ? 768 : 1024) + rot4 * 32 + (t4 >> 3) * 16 + 4 * hh) * 8;
#pragma nounroll
        for (int it = 0; it < SPIN_MAX; ++it) {
          const u64 x1_ = __hip_atomic_load(g + 1024 + (tid - 256), RLX_AGENT);
          const u64 x2 = __hip_atomic_load(g + (lane < 32 ? 512 : 1248) + cg * 32 + lane, RLX_AGENT);
          bool ok = (u32)(x1_ >> 32) == tag && (u32)(x2 >> 32) == tag;
#pragma unroll
          for (int k = 0; k < 4; ++k) { q[k] = __builtin_amdgcn_raw_buffer_load_b128(rs, qo + (k >> 1) * 64 + (k & 1) * 16, WS_MODG, 16); ok &= q[k][1] == tag && q[k][3] == tag; }
          v[1] = (u32)x1_; v[2] = (u32)x2;
          if (__all(ok)) break;
          __builtin_amdgcn_s_sleep(1);
        }
        *(LAS f32x2*)(L + L_C1B + (tid - 256) * 8) = (f32x2){rb(1.f + bf_lo(v[1])), rb(1.f + bf_hi(v[1]))};
        if (w == 4) *(LAS f32x2*)(L + L_GA + lane * 8) = (f32x2){bf_lo(v[2]), bf_hi(v[2])};
        const float one = (r32 & 1) ? 0.f : 1.f;
#pragma unroll
        for (int s = 0; s < 2; ++s)
          bv[s] = (u32x4){pk(one + bf_lo(q[2 * s][0]), one + bf_hi(q[2 * s][0])), pk(one + bf_lo(q[2 * s][2]), one + bf_hi(q[2 * s][2])),
                          pk(one + bf_lo(q[2 * s + 1][0]), one + bf_hi(q[2 * s + 1][0])), pk(one + bf_lo(q[2 * s + 1][2]), one + bf_hi(q[2 * s + 1][2]))};
      }
      STAMP(10);
      lds_post(lbase + L_LAND + w4 * 4, 1);
      load_gu(1, gu_slot(1));
      lds_wait(L, L_LAND, 1);
      {
        const u32 gl = blk * 14336 + (8 * hh + q4) * 224 + 8 * p4 + w4 * 32 + (t4 >> 3) * 7168 + gu_slot(0);
        const u32 g0 = gl + (blk ? 896u : 0u), g1 = gl + (blk ? 0u : 896u);
        f32x16 a0 = {}, a1 = {};
#pragma unroll
        for (int s = 0; s < 2; ++s) {
          a0 = MFMA(lds_tr(L, g0 + s * 3584, g1 + s * 3584), asbf(bv[s]), a0);
          a1 = MFMA(lds_tr(L, g0 + s * 3584 + 128, g1 + s * 3584 + 128), asbf(bv[s]), a1);
        }
        if (r32 < 2) {
          const u32 o = WS_UV + (j4 * 16 + t4) * 2048 + w4 * 256 + r32 * 128 + hh * 16;
#pragma unroll
          for (int g = 0; g < 4; ++g) {
            __builtin_amdgcn_raw_buffer_store_b128(__builtin_bit_cast(u32x4, (f32x4){a0[4 * g], a0[4 * g + 1], a0[4 * g + 2], a0[4 * g + 3]}), rs, o + g * 32, 0, 16);
            if (w4 < 3) __builtin_amdgcn_raw_buffer_store_b128(__builtin_bit_cast(u32x4, (f32x4){a1[4 * g], a1[4 * g + 1], a1[4 * g + 2], a1[4 * g + 3]}), rs, o + 1024 + g * 32, 0, 16);
          }
        }
      }
      load_gu(2, gu_slot(2));
      STAMP(11);
    }
    BAR();
    f32x4 lo, hi;
    red_read(L_WO, 2048, 16384, lo, hi);
    const f32x4 ga0 = lds_ldf4(L, cso + M_GA), ga1 = lds_ldf4(L, cso + M_GA + 16);
    float v[8];
    v[0] = rb(bf_lo(xr[0]) + rb(ga0[0] * rb(lo[0]))); v[1] = rb(bf_hi(xr[0]) + rb(ga0[1] * rb(lo[1])));
    v[2] = rb(bf_lo(xr[1]) + rb(ga0[2] * rb(lo[2]))); v[3] = rb(bf_hi(xr[1]) + rb(ga0[3] * rb(lo[3])));
    v[4] = rb(bf_lo(xr[2]) + rb(ga1[0] * rb(hi[0]))); v[5] = rb(bf_hi(xr[2]) + rb(ga1[1] * rb(hi[1])));
    v[6] = rb(bf_lo(xr[3]) + rb(ga1[2] * rb(hi[2]))); v[7] = rb(bf_hi(xr[3]) + rb(ga1[3] * rb(hi[3])));
    x1p = (u32x4){pk(v[0], v[1]), pk(v[2], v[3]), pk(v[4], v[5]), pk(v[6], v[7])};
    PIN(x1p);
    {
      const f32x4 c0 = lds_ldf4(L, cso + M_C1B + cg * 256), c1 = lds_ldf4(L, cso + M_C1B + cg * 256 + 16);
      const u32x4 y = {pk(v[0] * c0[0], v[1] * c0[1]), pk(v[2] * c0[2], v[3] * c0[3]), pk(v[4] * c1[0], v[5] * c1[1]), pk(v[6] * c1[2], v[7] * c1[3])};
      __builtin_amdgcn_raw_buffer_store_b128(y, rs, WS_X1 + ((t3 * 4 + 2 * hf + (w >> 2)) * 32 + 4 * cg + (l7 >> 1)) * 1024 + ((l7 & 1) * 32 + 8 * w4 + l3) * 16, 0, 16);
    }
    const float sm = sum8(((v[0] + v[1]) + (v[2] + v[3])) + ((v[4] + v[5]) + (v[6] + v[7])));
    const float mu = sm * (1.f / 64.f);
    float m2 = 0.f;
#pragma unroll
    for (int k = 0; k < 8; ++k) m2 = fmaf(v[k] - mu, v[k] - mu, m2);
    m2 = sum8(m2);
    if (l7 == 0) __hip_atomic_store((GAS u64*)(ws + WS_ST) + orow * 8 + cg, ((u64)__float_as_uint(m2) << 32) | __float_as_uint(sm), RLX_AGENT);
    WAITV(0);
    if (lane == 0) __hip_atomic_store((GAS u32*)(ws + WS_FX) + (t3 * 4 + 2 * hf + (w >> 2)) * 32 + cg * 4 + w4, tag, RLX_AGENT);
    if (!mw && lane == 1) __hip_atomic_store((GAS u32*)(ws + WS_FUV) + j4 * 64 + t4 * 4 + w4, tag, RLX_AGENT);
  }
  STAMP(12);
  BAR();

  if (mw) {
    u32 ln = lane_id();
    const u32 bk = (ln >> 4) & 1, gl = bk * 14336 + (8 * (ln >> 5) + ((ln & 15) >> 2)) * 224 + 8 * (ln & 3);
    u32 g0 = gl + (bk ? 896u : 0u) + 36 * KB, g1 = gl + (bk ? 0u : 896u) + 36 * KB, g0h = g0 + 56 * KB, g1h = g1 + 56 * KB;
    PIN(g0); PIN(g1); PIN(g0h); PIN(g1h);
    f32x16 ag[7] = {};
    bf16x8 wf_[7];
    u32x4 yf[32];
#define P4S(n) (((n) >> 2) & 3)
#define P4O(n) ((P4S(n) & 1 ? 0 : 28 * KB) + ((n) & 3) * 3584)
#define P4W(n, f) wf_[f] = lds_tr(L, (P4S(n) == 1 || P4S(n) == 2 ? g0h : g0) + P4O(n) + (f) * 32, (P4S(n) == 1 || P4S(n) == 2 ? g1h : g1) + P4O(n) + (f) * 32)
#pragma unroll
    for (int f = 0; f < 7; ++f) P4W(0, f);
    wait_flags((const GAS u32*)(ws + WS_FX) + (t4 * 4 + w) * 32 + (ln & 31), tag);
    STAMP(13);
    auto ld_y = [&](int n) { yf[n] = __builtin_amdgcn_raw_buffer_load_b128(rs, ln * 16, WS_X1 + ((t4 * 4 + w) * 32 + ((n + 4 * rot4) & 31)) * 1024, 16); };
#pragma unroll
    for (int n = 0; n < 5; ++n) ld_y(n);
    const u32 a_done = lbase + L_DONE + w * 4;
    u32x4 land;
    static_for<0, 32>([&](auto nc) {
      constexpr int n = nc.value, m = n + 1;
      if constexpr (n + 2 >= 27 && n + 2 < 32) yf[n + 2] = lds_ld128(L, L_YL + (w * 5 + (n + 2 - 27)) * 1024 + ln * 16);
#pragma unroll
      for (int f = 0; f < 7; ++f) {
        SB();
        ag[f] = MFMA(wf_[f], asbf(yf[n]), ag[f]);
        if ((m & 3) == 0 && f == 0) {
          STAMP4(19 + m / 4);
          lds_post(a_done, m / 4);
          if (m >= 12) { PIN(ag[0]); if (!all_ge(land, m / 4)) lds_wait(L, L_LAND, m / 4); }
        }
        if ((m & 3) == 3 && m >= 11 && f == 0) land = *(volatile LAS u32x4*)(L + L_LAND);
        if (m < 32) P4W(m, f);
        if constexpr (n < 11) { if (f == 1 || f == 4) ld_y(5 + 2 * n + (f >> 2)); }
      }
#pragma unroll
      for (int f = 0; f < 7; ++f) PIN(ag[f]);
    });
    const u32x4 bst = lds_ld128(L, L_BST + (32 * w + (ln & 31)) * 16);
    const u32x4 bfr = {ln < 32 ? bst[0] : 0u, ln < 32 ? bst[1] : 0u, 0u, 0u};
#pragma unroll
    for (int f = 0; f < 7; ++f) {
      const u32x2 uv = *(LAS u32x2*)(L + L_UVT + (f * 32 + (ln & 31)) * 8);
      ag[f] = MFMA(asbf((u32x4){uv[0], uv[1], 0u, 0u}), asbf(bfr), ag[f]);
    }
    const float rs_ = __uint_as_float(bst[2]), sd_ = bf_lo(bst[1]);
    const float k1 = -2.0f * 0.7978845608028654f * 1.4426950408889634f * rs_, k3 = k1 * 0.044715f * rs_ * rs_, i2 = sd_ * sd_;
    STAMP(15);
    ln = lane_id();
    const u32 ao = WS_ACT + ((t4 * 4 + w) * 112 + 7 * j4) * 1024 + ln * 16;
#pragma unroll
    for (int f = 0; f < 7; ++f) {
      u32x4 o;
#pragma unroll
      for (int k = 0; k < 4; ++k) { const f32x2 a = geglu2((f32x2){ag[f][2 * k], ag[f][2 * k + 1]}, (f32x2){ag[f][8 + 2 * k], ag[f][9 + 2 * k]}, k1, k3, i2); o[k] = pk(a[0], a[1]); }
      __builtin_amdgcn_raw_buffer_store_b128(o, rs, ao + f * 1024, 0, 16);
    }
    WAITV(0);
    if (ln == 0) __hip_atomic_store((GAS u32*)(ws + WS_FA) + (t4 * 4 + w) * 32 + j4, tag, RLX_AGENT);
    STAMP(16);
  } else {
    const u32 lane = lane_id(), r32 = lane & 31, hh = lane >> 5;
    const u32 w8o = (lane >> 3) * 1024u + (((lane & 7) ^ (((lane >> 4) & 1) << 2)) << 4);
    const u32 wt0 = (4 * hh + ((lane & 15) >> 2)) * 128 + ((32 * ((lane >> 4) & 1) + 8 * (lane & 3)) ^ (((lane & 15) >> 3) << 6)), wt1 = wt0 ^ 64u;
    auto load_wd = [&](int i) {
      int ks = rot5; PINS(ks);
      ks += i; ks -= ks >= 28 ? 28 : 0;
      const u32 so = cg * 128 + (w4 * 448 + ks * 16) * 1024, ld = lbase + wd_base(i) + w4 * 2048;
      glds(srd_d, w8o, so, ld);
      glds(srd_d, w8o, so + 8192, ld + 1024);
    };
    const u32 a_land = lbase + L_LAND + w4 * 4;
    bf16x8 wp0[12], wp1[12];
    u32 dC0 = w4 * 2048 + wt0, dC1 = w4 * 2048 + wt1, dB0 = dC0 + 92 * KB, dB1 = dC1 + 92 * KB, dA0 = dC0 + 64 * KB, dA1 = dC1 + 64 * KB;
    PIN(dA0); PIN(dA1); PIN(dB0); PIN(dB1); PIN(dC0); PIN(dC1);
#define WD0(i) (wd_base(i) < 64 * KB ? dC0 + wd_base(i) : wd_base(i) < 92 * KB ? dA0 + (wd_base(i) - 64 * KB) : dB0 + (wd_base(i) - 92 * KB))
#define WD1(i) (wd_base(i) < 64 * KB ? dC1 + wd_base(i) : wd_base(i) < 92 * KB ? dA1 + (wd_base(i) - 64 * KB) : dB1 + (wd_base(i) - 92 * KB))
#define WDTURN(i)                                                                                                                   \
  do {                                                                                                                              \
    _Pragma("unroll") for (int k = (i); k < (i) + 2; ++k) { wp0[k] = lds_tr(L, WD0(k), WD0(k) + 1024); wp1[k] = lds_tr(L, WD1(k), WD1(k) + 1024); } \
    _Pragma("unroll") for (int k = (i); k < (i) + 2; ++k) { PIN(wp0[k]); PIN(wp1[k]); }                                             \
    load_wd((i) + 2); load_wd((i) + 3);                                                                                             \
  } while (0)
    load_gu(3, gu_slot(3));
    load_wd(0); load_wd(1);
    WAITV(4); lds_post(a_land, 3);
    lds_wait(L, L_DONE, 1);
    STAMP(13);
    {
      load_gu(4, gu_slot(4));
      const u32x4 srd_w = make_srd(ws, WS_BYTES);
#pragma unroll
      for (int k = 0; k < 5; ++k) glds_sc1(srd_w, lane * 16, WS_X1 + ((t4 * 4 + w4) * 32 + ((27 + k + 4 * rot4) & 31)) * 1024, lbase + L_YL + (w4 * 5 + k) * 1024);
      WAITV(12); WDTURN(0); STAMP4(21);
      lds_wait(L, L_DONE, 2); load_gu(5, gu_slot(5)); WAITV(16); lds_post(a_land, 4); WAITV(7); WDTURN(2); STAMP4(22);
      lds_wait(L, L_DONE, 3); load_gu(6, gu_slot(6)); WAITV(11); lds_post(a_land, 5); WAITV(7); WDTURN(4); STAMP4(23);
      lds_wait(L, L_DONE, 4); load_gu(7, gu_slot(7)); WAITV(11); lds_post(a_land, 6); WAITV(7); WDTURN(6); STAMP4(24);
        wait_flags((const GAS u32*)(ws + WS_FUV) + j4 * 64 + lane, tag);
        lds_post(a_land, 7);
        {
          u32x4 pt[16];
          const u32 o = lane < 28 ? (w4 * 28 + lane) * 16 : 0x7fffffffu;
#pragma unroll
          for (int k = 0; k < 16; ++k) pt[k] = __builtin_amdgcn_raw_buffer_load_b128(rs, o, WS_UV + (j4 * 16 + k) * 2048, 16);
          SB();
          f32x4 a = {};
#pragma unroll
          for (int k = 0; k < 16; ++k) {
            const f32x4 t_ = __builtin_bit_cast(f32x4, pt[k]);
#pragma unroll
            for (int e = 0; e < 4; ++e) { float y_ = a[e] + t_[e]; asm("" : "+v"(y_)); a[e] = y_; }
          }
          if (lane < 28) {
            const u32 vi = (w4 * 28 + lane) * 4;
#pragma unroll
            for (int e = 0; e < 4; ++e) {
              const float hi = rb(a[e]);
              *(LAS u32*)(L + L_UVT + ((vi >> 6) * 32 + (vi & 31) + e) * 8 + ((vi >> 5) & 1) * 4) = pk(hi, a[e] - hi);
            }
          }
        }
      WDTURN(8);
      {
        const u32 so = (t4 * 128 + 32 * w4 + r32) * 64 + hh * 32;
        const u32x4 s0 = __builtin_amdgcn_raw_buffer_load_b128(rs, so, WS_ST, 16), s1 = __builtin_amdgcn_raw_buffer_load_b128(rs, so + 16, WS_ST, 16);
        auto xsum = [&](float v) {
          auto r_ = __builtin_amdgcn_permlane32_swap(__float_as_uint(v), __float_as_uint(v), false, false);
          return __uint_as_float(r_[0]) + __uint_as_float(r_[1]);
        };
        const float a0 = __uint_as_float(s0[0]), a1 = __uint_as_float(s0[2]), a2 = __uint_as_float(s1[0]), a3 = __uint_as_float(s1[2]);
        const float me = xsum((a0 + a1) + (a2 + a3)) * (1.f / 512.f);
        const float d0 = a0 * (1.f / 64.f) - me, d1 = a1 * (1.f / 64.f) - me, d2 = a2 * (1.f / 64.f) - me, d3 = a3 * (1.f / 64.f) - me;
        const float m2 = (__uint_as_float(s0[1]) + __uint_as_float(s0[3])) + (__uint_as_float(s1[1]) + __uint_as_float(s1[3]));
        float mb, sd;
        ln_scal(me, xsum(m2 + 64.f * ((d0 * d0 + d1 * d1) + (d2 * d2 + d3 * d3))) * (1.f / 512.f), mb, sd);
        if (lane < 32) lds_st128(L, L_BST + (32 * w4 + lane) * 16, (u32x4){pk(-mb, -mb), pk(sd, sd), __float_as_uint(__builtin_amdgcn_rcpf(sd)), 0u});
      }
      lds_post(a_land, 8);
      STAMP4(25);
      lds_wait(L, L_DONE, 5); load_wd(14); load_wd(15); load_wd(16); WAITV(6); WDTURN(10);
      lds_wait(L, L_DONE, 6); load_wd(17); load_wd(18); load_wd(19);
      lds_wait(L, L_DONE, 7); load_wd(20); load_wd(21); load_wd(22); load_wd(23);
      lds_wait(L, L_DONE, 8);
    }
    STAMP(15);
    load_wd(24); load_wd(25); load_wd(26); load_wd(27);
    STAMP(16);
    wait_flags((const GAS u32*)(ws + WS_FA) + (t3 * 4 + 2 * hf + ((lane >> 2) & 1)) * 32 + 4 * w4 + (lane & 3), tag);
    STAMP(17);
    const u32 ao = WS_ACT + ((t3 * 4 + 2 * hf) * 112 + 28 * w4) * 1024 + lane * 16;
    u32x4 af0[28], af1[28];
    auto ld_act = [&](int i) {
      int ks = i + rot5; ks -= ks >= 28 ? 28 : 0;
      af0[i] = __builtin_amdgcn_raw_buffer_load_b128(rs, ao, ks * 1024, 16);
      af1[i] = __builtin_amdgcn_raw_buffer_load_b128(rs, ao + 112 * 1024, ks * 1024, 16);
    };
#pragma unroll
    for (int i = 0; i < 4; ++i) ld_act(i);
    f32x16 a00 = {}, a01 = {}, a10 = {}, a11 = {};
    static_for<0, 28>([&](auto ic) {
      constexpr int i = ic.value;
      SB();
      a00 = MFMA(wp0[i % 12], asbf(af0[i]), a00);
      a01 = MFMA(wp1[i % 12], asbf(af0[i]), a01);
      SB();
      a10 = MFMA(wp0[i % 12], asbf(af1[i]), a10);
      if constexpr (i < 7) { ld_act(4 + 2 * i); ld_act(5 + 2 * i); }
      else if constexpr (i + 11 < 28) ld_act(i + 11);
      SB();
      a11 = MFMA(wp1[i % 12], asbf(af1[i]), a11);
      if constexpr (i >= 9 && i + 3 < 28) {
        wp0[(i + 3) % 12] = lds_tr(L, WD0(i + 3), WD0(i + 3) + 1024);
        wp1[(i + 3) % 12] = lds_tr(L, WD1(i + 3), WD1(i + 3) + 1024);
      }
      PIN(a00); PIN(a01); PIN(a10); PIN(a11);
    });
    STAMP(18);
    red_write(20 * KB + w4 * 2048, 8192, a00, a01);
    red_write(92 * KB + w4 * 2048, 8192, a10, a11);
  }
  BAR();
  {
    f32x4 lo, hi;
    red_read(mw ? 20 * KB : 60 * KB, 8192, 2048, lo, hi);
    u32 ln = lane_id();
    const f32x4 gb0 = lds_ldf4(L, L_MISC + M_GB + (ln & 7) * 32), gb1 = lds_ldf4(L, L_MISC + M_GB + (ln & 7) * 32 + 16);
    u32x4 o;
    o[0] = pk(bf_lo(x1p[0]) + rb(gb0[0] * rb(lo[0])), bf_hi(x1p[0]) + rb(gb0[1] * rb(lo[1])));
    o[1] = pk(bf_lo(x1p[1]) + rb(gb0[2] * rb(lo[2])), bf_hi(x1p[1]) + rb(gb0[3] * rb(lo[3])));
    o[2] = pk(bf_lo(x1p[2]) + rb(gb1[0] * rb(hi[0])), bf_hi(x1p[2]) + rb(gb1[1] * rb(hi[1])));
    o[3] = pk(bf_lo(x1p[3]) + rb(gb1[2] * rb(hi[2])), bf_hi(x1p[3]) + rb(gb1[3] * rb(hi[3])));
    __builtin_amdgcn_raw_buffer_store_b128(o, __builtin_amdgcn_make_buffer_rsrc(out, 0, 2097152, 0x00020000), (ln >> 3) * 1024 + (ln & 7) * 16, (t3 * 128 + hf * 64 + 8 * w) * 1024 + cg * 128, 16);
  }
  STAMP(19);
  asm volatile(".Ladaln_end:");
}

extern "C" void kernel_launch(void* const* d_in, const int* in_sizes, int n_in, void* d_out, int out_size, void* d_ws, size_t ws_size,
                              hipStream_t stream) {
  adaln_block<<<256, 512, 0, stream>>>((const u16*)d_in[0], (const u16*)d_in[1], (const u16*)d_in[2], (const u16*)d_in[4], (const u16*)d_in[5],
                                       (const u16*)d_in[6], (const u16*)d_in[7], (const u16*)d_in[8], (u16*)d_out, (char*)d_ws);
}
```

```cpp
#include <hip/hip_runtime.h>
#include <stdint.h>

typedef unsigned short u16;
typedef unsigned int u32;
typedef unsigned long long u64;
typedef __attribute__((ext_vector_type(8))) __bf16 bf16x8;
typedef __attribute__((ext_vector_type(2))) __bf16 bf16x2;
typedef __attribute__((ext_vector_type(4))) short s16x4;
typedef __attribute__((ext_vector_type(8))) short s16x8;
typedef __attribute__((ext_vector_type(16))) float f32x16;
typedef __attribute__((ext_vector_type(4))) float f32x4;
typedef __attribute__((ext_vector_type(2))) float f32x2;
typedef __attribute__((ext_vector_type(4))) u32 u32x4;
typedef __attribute__((ext_vector_type(2))) u32 u32x2;
#define LAS __attribute__((address_space(3)))
#define GAS __attribute__((address_space(1)))
#define DI __device__ __forceinline__
#define RLX_AGENT __ATOMIC_RELAXED, __HIP_MEMORY_SCOPE_AGENT

constexpr u32 WS_EPOCH = 0;
constexpr u32 WS_ARR = 1024;
constexpr u32 WS_MODG = 4096;
constexpr u32 WS_ST = 65536;
constexpr u32 WS_UV = 196608;
constexpr u32 WS_KV = 1048576;
constexpr u32 WS_O = WS_KV + 4194304;
constexpr u32 WS_X1 = WS_O + 2097152;
constexpr u32 WS_ACT = WS_X1 + 2097152;
#define WO_AT 0
#define FS 16
#define MREP 8
constexpr u32 WS_FKV = WS_ACT + 7340032;
constexpr u32 WS_FO = WS_FKV + 1024 * 4 * FS;
constexpr u32 WS_FX = WS_FO + 2048 * 4 * FS;
constexpr u32 WS_FA = WS_FX + 2048 * 4 * FS;
constexpr u32 WS_FUV = WS_FA + 1024 * 4 * FS;
constexpr u32 WS_MOD1 = WS_FUV + 1024 * 4 * FS;
constexpr u32 WS_DBG = 20971520;
constexpr u32 WS_BYTES = WS_DBG + 1048576;
static_assert(WS_MOD1 + 8 * 4096 <= WS_DBG && WS_UV + 256 * 2048 <= WS_KV, "workspace map");
#define FLAG(base, i) ((GAS u32*)(ws + (base)) + (i) * FS)

constexpr int SPIN_MAX = 1 << 17;
constexpr u32 KB = 1024;
constexpr u32 L_WO = 0;
constexpr u32 L_KV = 64 * KB;
constexpr u32 L_QX = 144 * KB;
constexpr u32 L_AR1 = 120 * KB;
constexpr u32 L_YL = 0;
constexpr u32 L_MISC = 148 * KB;
constexpr u32 L_C1 = L_MISC, L_SH = L_MISC + 2048, L_C1B = L_MISC + 4096, L_SHB = L_MISC + 6144, M_C1B = 4096;
constexpr u32 L_UVT = L_C1, L_BST = L_SH;
constexpr u32 L_ONES = L_MISC + 8192;
constexpr u32 L_GA = L_ONES + 256, M_GA = 8192 + 256, M_GB = 8192 + 512;
constexpr u32 L_DONE = L_ONES + 784, L_LAND = L_ONES + 800, L_MODF = L_ONES + 816;
constexpr u32 L_TAG = L_ONES + 768, L_JUNK = L_ONES + 1536;
constexpr u32 L_RED = L_MISC + 10240;
constexpr u32 LDS_BYTES = 163840;
DI constexpr u32 gu_slot(int c) { return (c & 3) == 3 ? 36 * KB : 64 * KB + (c & 3) * 28 * KB; }
DI constexpr u32 wd_base(int i) {
  return i < 8 ? 20 * KB + (i & 1) * 8192u : i < 11 ? 64 * KB + (i - 8) * 8192u : i < 18 ? 92 * KB + (i - 11) * 8192u : i < 21 ? 36 * KB + (i - 18) * 8192u
       : i < 23 ? (i - 21) * 8192u : i < 25 ? 20 * KB + (i - 23) * 8192u : 64 * KB + (i - 25) * 8192u;
}
constexpr u32 CODE_LINES = 16;

constexpr float NEGBIG = -1e30f;
constexpr float QSCALE = 0.25f * 1.4426950408889634f;

template <int I> struct IC { static constexpr int value = I; };
template <int I, int N, class F> DI void static_for(F&& f) { if constexpr (I < N) { f(IC<I>{}); static_for<I + 1, N>(f); } }
DI u32 lane_id() { u32 r; asm volatile("v_mbcnt_lo_u32_b32 %0, -1, 0\n\tv_mbcnt_hi_u32_b32 %0, -1, %0" : "=v"(r)); return r; }
DI float bf_lo(u32 v) { return __uint_as_float(v << 16); }
DI float bf_hi(u32 v) { return __uint_as_float(v & 0xffff0000u); }
DI u32 pk(float a, float b) { f32x2 v = {a, b}; return __builtin_bit_cast(u32, __builtin_convertvector(v, bf16x2)); }
DI float rb(float a) { return bf_lo(pk(a, a)); }
template <int O> DI u32x4 pack8(const f32x16& a) { return (u32x4){pk(a[O], a[O + 1]), pk(a[O + 2], a[O + 3]), pk(a[O + 4], a[O + 5]), pk(a[O + 6], a[O + 7])}; }
DI bf16x8 asbf(u32x4 v) { return __builtin_bit_cast(bf16x8, v); }
#define MFMA(a, b, c) __builtin_amdgcn_mfma_f32_32x32x16_bf16((a), (b), (c), 0, 0, 0)
template <int CTRL, int RM = 0xf> DI float dpp(float v) { return __builtin_bit_cast(float, __builtin_amdgcn_update_dpp(0, __builtin_bit_cast(int, v), CTRL, RM, 0xf, true)); }
DI float sum8(float v) { v += dpp<0xB1>(v); v += dpp<0x4E>(v); v += dpp<0x141>(v); return v; }
DI float dot2(u32 a, u32 b, float c) { return __builtin_amdgcn_fdot2_f32_bf16(__builtin_bit_cast(bf16x2, a), __builtin_bit_cast(bf16x2, b), c, false); }

DI u32x4 lds_ld128(LAS char* L, u32 off) { return *(LAS u32x4*)(L + off); }
DI f32x4 lds_ldf4(LAS char* L, u32 off) { return *(LAS f32x4*)(L + off); }
DI void lds_st128(LAS char* L, u32 off, u32x4 v) { *(LAS u32x4*)(L + off) = v; }
DI bf16x8 lds_tr(LAS char* L, u32 off0, u32 off1) {
  s16x4 a = __builtin_amdgcn_ds_read_tr16_b64_v4i16((LAS s16x4*)(L + off0));
  s16x4 b = __builtin_amdgcn_ds_read_tr16_b64_v4i16((LAS s16x4*)(L + off1));
  return __builtin_bit_cast(bf16x8, __builtin_shufflevector(a, b, 0, 1, 2, 3, 4, 5, 6, 7));
}
DI u32x4 make_srd(const void* p, u32 bytes) { const u64 a = (u64)p; return (u32x4){(u32)a, (u32)(a >> 32), bytes, 0x00020000u}; }
DI void glds(const u32x4& srd, u32 voff, u32 soff, u32 ldsaddr) {
  asm volatile("s_mov_b32 m0, %3\n\ts_nop 0\n\tbuffer_load_dwordx4 %0, %1, %2 offen lds" ::"v"(voff), "s"(srd), "s"(soff), "s"(ldsaddr) : "memory");
}
DI void glds_sc1(const u32x4& srd, u32 voff, u32 soff, u32 ldsaddr) {
  asm volatile("s_mov_b32 m0, %3\n\ts_nop 0\n\tbuffer_load_dwordx4 %0, %1, %2 offen sc1 lds" ::"v"(voff), "s"(srd), "s"(soff), "s"(ldsaddr) : "memory");
}
DI void touch(const u32x4& srd, u32 voff, u32 ldsaddr) {
  asm volatile("s_mov_b32 m0, %2\n\ts_nop 0\n\tbuffer_load_dword %0, %1, 0 offen lds" ::"v"(voff), "s"(srd), "s"(ldsaddr) : "memory");
}
DI void lds_post(u32 addr, u32 val) {
  asm volatile("s_mov_b64 exec, 1\n\tds_write_b32 %0, %1\n\ts_mov_b64 exec, -1" ::"v"(addr), "v"(val) : "memory");
}
DI bool all_ge(u32x4 v, u32 k) { return __builtin_amdgcn_readfirstlane(min(min(v[0], v[1]), min(v[2], v[3]))) >= k; }
DI void lds_wait(LAS char* L, u32 off, u32 k) {
#pragma nounroll
  for (int it = 0; it < (SPIN_MAX << 4); ++it) {
    if (all_ge(*(volatile LAS u32x4*)(L + off), k)) break;
    __builtin_amdgcn_s_sleep(3);
  }
  asm volatile("" ::: "memory");
}
#define BAR() asm volatile("s_waitcnt lgkmcnt(0)\n\ts_barrier" ::: "memory")
#define WAITV_BAR(N) asm volatile("s_waitcnt vmcnt(%0) lgkmcnt(0)\n\ts_barrier" ::"i"(N) : "memory")
#define WAITV(N) asm volatile("s_waitcnt vmcnt(%0)" ::"i"(N) : "memory")
#define SB() __builtin_amdgcn_sched_barrier(0)
#define PIN(x) asm volatile("" : "+v"(x))
#define PINS(x) asm volatile("" : "+s"(x))
#define ORDER() __builtin_amdgcn_fence(__ATOMIC_ACQUIRE, "wavefront")

DI void wait_flags(const GAS u32* p, u32 tag) {
  u32 v = tag + 1u;
#pragma nounroll
  for (int it = 0; it < SPIN_MAX; ++it) {
    if (v != tag) v = __hip_atomic_load(p, RLX_AGENT);
    if (__all(v == tag)) break;
    __builtin_amdgcn_s_sleep(4);
  }
  ORDER();
}
template <int N> DI void gather_gran(const GAS u64* g, u32 tag, u32 (&v)[N], int stride) {
#pragma nounroll
  for (int it = 0; it < SPIN_MAX; ++it) {
    bool ok = true;
#pragma unroll
    for (int k = 0; k < N; ++k) { u64 x = __hip_atomic_load(g + k * stride, RLX_AGENT); v[k] = (u32)x; ok &= (u32)(x >> 32) == tag; }
    if (__all(ok)) break;
    __builtin_amdgcn_s_sleep(1);
  }
  ORDER();
}

#define STAMPC(n) do {} while (0)
#define STAMP(n) do {} while (0)

#define STAMP4(n) do {} while (0)
#define STAMP1(n) do {} while (0)
DI void ln_scal(float mean, float var, float& mb, float& sd) {
  mb = rb(mean);
  sd = rb(__builtin_amdgcn_sqrtf(rb(rb(fmaxf(var, 0.f)) + 1e-5f)));
}
DI float fma1(float a, float b, float c) { float r = fmaf(a, b, c); asm("" : "+v"(r)); return r; }
typedef __attribute__((ext_vector_type(2))) _Float16 f16x2;
DI u32x4 znorm8(u32x4 x, float r, float mr) {
  u32x4 o;
#pragma unroll
  for (int e = 0; e < 4; ++e) {
    const f32x2 v = {fmaf(bf_lo(x[e]), r, -mr), fmaf(bf_hi(x[e]), r, -mr)};
    o[e] = __builtin_bit_cast(u32, __builtin_convertvector(v, f16x2));
  }
  return o;
}
DI u32 xform2(u32 z_, u32 c_, u32 s_) {
  const f16x2 z = __builtin_bit_cast(f16x2, z_), c = __builtin_bit_cast(f16x2, c_), s = __builtin_bit_cast(f16x2, s_);
  return pk(fma1((float)z[0], (float)c[0], (float)s[0]), fma1((float)z[1], (float)c[1], (float)s[1]));
}
DI u32x4 xform8(u32x4 x, u32x4 c, u32x4 s) { return (u32x4){xform2(x[0], c[0], s[0]), xform2(x[1], c[1], s[1]), xform2(x[2], c[2], s[2]), xform2(x[3], c[3], s[3])}; }
DI u32 pkh(float a, float b) { const f32x2 v = {a, b}; return __builtin_bit_cast(u32, __builtin_convertvector(v, f16x2)); }
DI f32x2 geglu2(f32x2 ag, f32x2 au, float k1, float k3, float i2) {
  const f32x2 a = ag * (ag * ag * k3 + k1);
  const f32x2 d = (f32x2){__builtin_amdgcn_exp2f(a[0]), __builtin_amdgcn_exp2f(a[1])} * i2 + i2;
  return ag * au * (f32x2){__builtin_amdgcn_rcpf(d[0]), __builtin_amdgcn_rcpf(d[1])};
}

__global__ __launch_bounds__(512, 2) void adaln_block(const u16* __restrict__ x, const u16* __restrict__ wqkv, const u16* __restrict__ wo,
                                                      const u16* __restrict__ wg, const u16* __restrict__ wu, const u16* __restrict__ wd,
                                                      const u16* __restrict__ cvec, const u16* __restrict__ wm, u16* __restrict__ out,
                                                      char* __restrict__ ws) {
  __shared__ __attribute__((aligned(1024))) char lds[LDS_BYTES];
  LAS char* L = (LAS char*)lds;
  const u32 lbase = (u32)(uintptr_t)L;
  const int tid = threadIdx.x, lane = tid & 63;
  const int w = __builtin_amdgcn_readfirstlane(tid >> 6), w4 = w & 3;
  const bool mw = w < 4;
  const int b = blockIdx.x, xcd = b & 7, ii = b >> 3;
  const int u1 = 31 - (8 * (xcd >> 1) + (ii >> 2)), q1 = 4 * (xcd & 1) + (ii & 3);
  const int t3 = 2 * xcd + (ii >> 4), hf = (ii >> 3) & 1, cg = ii & 7;
  const int t4 = 8 * (xcd >> 2) + (ii >> 2), j4 = 4 * (xcd & 3) + (ii & 3);
  const int rot1 = (u1 + 2 * q1) & 7;
  const int rot4 = ii >> 2, rot5 = 7 * (ii >> 3);
  const int r32 = lane & 31, hh = lane >> 5, blk = (lane >> 4) & 1, q4 = (lane & 15) >> 2, p4 = lane & 3, l3 = lane >> 3, l7 = lane & 7;
  const __amdgpu_buffer_rsrc_t rs = __builtin_amdgcn_make_buffer_rsrc(ws, 0, WS_BYTES, 0x00020000);
  STAMP(0);

  u32 tag = 0;
  const int orow = t3 * 128 + hf * 64 + 8 * w + l3;
  const int mg = (31 - u1) * 8 + q1;
  const u32x4 srd_q = make_srd(wqkv, 512 * 3072);
  const u32 q8o = l3 * 3072u + ((l7 ^ (((lane >> 4) & 1) << 2)) << 4);
  auto load_wq = [&](int c) {
    const u32 so = ((c + rot1) & 7) * 196608 + w * 1024 + q1 * 128;
#pragma unroll
    for (int e = 0; e < 8; ++e) glds(srd_q, q8o, so + e * 24576, lbase + c * 24576 + w * 8192 + e * 1024);
  };
  u32x4 X[16];
  u32x4 xr;
  if (mw) {
    u32x4 M[16];
    u32 cv = 0;
    if (mg < 48) {
      const GAS char* mb = (const GAS char*)wm + mg * 128 + (128 * w + 16 * l3) * 6144 + l7 * 16;
      cv = ((const GAS u32*)cvec)[64 * w + lane];
#pragma unroll
      for (int n = 0; n < 16; ++n) M[n] = *(const GAS u32x4*)(mb + n * 6144);
    }
    const u32 ep = __hip_atomic_load((const GAS u32*)(ws + WS_EPOCH), RLX_AGENT);
    if (lane < 12) *(LAS u32*)(L + L_DONE + lane * 4) = 0u;
    SB();
    BAR();
    STAMP(1);
    if (mg < 48) {
      {
        const float c0 = bf_lo(cv), c1 = bf_hi(cv);
        *(LAS u32*)(L + L_RED + 1024 + w * 256 + lane * 4) = pk(c0 * __builtin_amdgcn_rcpf(1.0f + __builtin_amdgcn_exp2f(-1.4426950408889634f * c0)),
                                                               c1 * __builtin_amdgcn_rcpf(1.0f + __builtin_amdgcn_exp2f(-1.4426950408889634f * c1)));
      }
      const u32x4 s0 = lds_ld128(L, L_RED + 1024 + w * 256 + l3 * 32), s1 = lds_ld128(L, L_RED + 1024 + w * 256 + l3 * 32 + 16);
      f32x2 a[4] = {};
#pragma unroll
      for (int n = 0; n < 8; ++n) {
        const u32 sp = n < 4 ? s0[n & 3] : s1[n & 3];
#pragma unroll
        for (int e = 0; e < 4; ++e) {
          const u32 r0 = M[2 * n][e], r1 = M[2 * n + 1][e];
          a[e][0] = dot2(__builtin_amdgcn_perm(r1, r0, 0x05040100u), sp, a[e][0]);
          a[e][1] = dot2(__builtin_amdgcn_perm(r1, r0, 0x07060302u), sp, a[e][1]);
        }
      }
      float v[8] = {a[0][0], a[0][1], a[1][0], a[1][1], a[2][0], a[2][1], a[3][0], a[3][1]};
#pragma unroll
      for (int e = 0; e < 8; ++e) {
        v[e] += dpp<0x128>(v[e]);
        auto r1_ = __builtin_amdgcn_permlane16_swap(__float_as_uint(v[e]), __float_as_uint(v[e]), false, false);
        v[e] = __uint_as_float(r1_[0]) + __uint_as_float(r1_[1]);
        auto r2_ = __builtin_amdgcn_permlane32_swap(__float_as_uint(v[e]), __float_as_uint(v[e]), false, false);
        v[e] = __uint_as_float(r2_[0]) + __uint_as_float(r2_[1]);
      }
      if (lane < 8) {
        *(LAS f32x4*)(L + L_RED + w * 256 + lane * 32) = (f32x4){v[0], v[1], v[2], v[3]};
        *(LAS f32x4*)(L + L_RED + w * 256 + lane * 32 + 16) = (f32x4){v[4], v[5], v[6], v[7]};
      }
      lds_post(lbase + L_MODF + w * 4, 1);
      if (w == 3) {
        lds_wait(L, L_MODF, 1);
        float s = 0.f;
#pragma unroll
        for (int k = 0; k < 4; ++k) s += *(LAS float*)(L + L_RED + k * 256 + lane * 4);
        const float o = dpp<0xB1>(s);
        const u32 tg = __builtin_amdgcn_readfirstlane(ep) + 1u;
        const u64 gr = ((u64)tg << 32) | ((lane & 1) ? pk(o, s) : pk(s, o));
        if (mg < 16) {
#pragma unroll
          for (int r = 0; r < MREP / 2 + (MREP & 1); ++r)
            if (2 * r + (lane & 1) < MREP) __hip_atomic_store((GAS u64*)(ws + WS_MOD1) + (2 * r + (lane & 1)) * 512 + 32 * mg + (lane >> 1), gr, RLX_AGENT);
        } else if (!(lane & 1)) __hip_atomic_store((GAS u64*)(ws + WS_MODG) + 32 * mg + (lane >> 1), gr, RLX_AGENT);
        STAMP(28);
      }
    }
    xr = *(const GAS u32x4*)((const GAS char*)x + orow * 1024 + cg * 128 + l7 * 16);
    if (w < 3) {
      tag = __builtin_amdgcn_readfirstlane(ep) + 1u;
      load_wq(0); load_wq(1); load_wq(2);
    } else {
      u64 pc;
      int len;
      asm volatile("s_getpc_b64 %0\n.Ladaln_pf:\n\ts_mov_b32 %1, .Ladaln_end-.Ladaln_pf" : "=s"(pc), "=s"(len));
      touch(make_srd((const void*)pc, len > 0 ? (u32)len : 0u), (ii * CODE_LINES + (lane < CODE_LINES ? lane : 0)) * 128u, lbase + L_JUNK);
      if (lane < 32) lds_st128(L, L_ONES + (lane & 15) * 16 + (lane >> 4) * 1024, (u32x4){0x3f803f80u, 0x3f803f80u, 0x3f803f80u, 0x3f803f80u});
      tag = __builtin_amdgcn_readfirstlane(ep) + 1u;
      if (lane == 0) *(LAS u32*)(L + L_TAG) = tag;
      u32x4 v[4];
#pragma nounroll
      for (int it = 0; it < SPIN_MAX; ++it) {
        bool ok = true;
#pragma unroll
        for (int k = 0; k < 4; ++k) { v[k] = __builtin_amdgcn_raw_buffer_load_b128(rs, lane * 16, WS_MOD1 + (xcd % MREP) * 4096 + k * 1024, 16); ok &= v[k][1] == tag && v[k][3] == tag; }
        if (__all(ok)) break;
        __builtin_amdgcn_s_sleep(2);
      }
#pragma unroll
      for (int k = 0; k < 2; ++k) {
        const u32 o = L_C1 + ((lane + 64 * k) >> 1) * 32 + (lane & 1) * 8;
        *(LAS u32x2*)(L + o + 16) = (u32x2){pkh(bf_lo(v[k][0]), bf_hi(v[k][0])), pkh(bf_lo(v[k][2]), bf_hi(v[k][2]))};
        *(LAS u32x2*)(L + o) = (u32x2){pkh(rb(1.f + bf_lo(v[2 + k][0])), rb(1.f + bf_hi(v[2 + k][0]))), pkh(rb(1.f + bf_lo(v[2 + k][2])), rb(1.f + bf_hi(v[2 + k][2])))};
      }
    }
    STAMP(3);
  } else {
    const __amdgpu_buffer_rsrc_t rx = __builtin_amdgcn_make_buffer_rsrc((void*)x, 0, 2048 * 1024, 0x00020000);
    if (mg < 48) BAR();
#pragma unroll
    for (int c = 0; c < 8; ++c)
#pragma unroll
      for (int g = 0; g < 2; ++g)
        X[2 * c + g] = __builtin_amdgcn_raw_buffer_load_b128(rx, l3 * 1024u + l7 * 16u, (u1 * 64 + 16 * w4 + 8 * g) * 1024 + ((c + rot1) & 7) * 128, 0);
    xr = *(const GAS u32x4*)((const GAS char*)x + orow * 1024 + cg * 128 + l7 * 16);
    SB();
    if (mg >= 48) BAR();
    STAMP(2);
    float sm[2] = {}, sq[2] = {};
#pragma unroll
    for (int c = 0; c < 8; ++c) {
      SB();
#pragma unroll
      for (int e = 0; e < 4; ++e)
#pragma unroll
        for (int g = 0; g < 2; ++g) { sm[g] = dot2(X[2 * c + g][e], 0x3f803f80u, sm[g]); sq[g] = dot2(X[2 * c + g][e], X[2 * c + g][e], sq[g]); }
    }
    float rr[2], mr[2];
#pragma unroll
    for (int g = 0; g < 2; ++g) {
      const float me = sum8(sm[g]) * (1.f / 512.f);
      float mb, sd;
      ln_scal(me, sum8(sq[g]) * (1.f / 512.f) - me * me, mb, sd);
      rr[g] = __builtin_amdgcn_rcpf(sd); mr[g] = mb * rr[g];
    }
#pragma unroll
    for (int n = 0; n < 16; ++n) { X[n] = znorm8(X[n], rr[n & 1], mr[n & 1]); PIN(X[n]); }
    STAMP(3);
  }
  BAR();
  STAMP(4);
  STAMPC(29);

  u32 cso = L_MISC + l7 * 32;
  PIN(cso);
  const u32x4 srd_g = make_srd(wg, 512 * 3584), srd_u = make_srd(wu, 512 * 3584), srd_d = make_srd(wd, 1792 * 1024), srd_o = make_srd(wo, 512 * 1024);

  u32 guo[7];
  const u32x4 srd_3 = w4 < 2 ? srd_g : srd_u;
  auto load_gu = [&](int c, u32 slot) {
    int r_ = rot4; PINS(r_);
    const u32 so = j4 * 224 + ((c + r_) & 7) * (64 * 3584), ld = lbase + slot + w4 * 1024;
#pragma unroll
    for (int n = 0; n < 7; ++n) glds(n < 3 ? srd_g : n == 3 ? srd_3 : srd_u, guo[n], so, ld + n * 4096);
  };
  const u32 w8o = l3 * 1024u + ((l7 ^ (((lane >> 4) & 1) << 2)) << 4);
  const int hp = w4 >> 1, hd = w >> 2, qb = (w ^ hd) & 1;
  bf16x8 qf;
  if (mw) {
    f32x16 aq = {}, ak = {}, av = {};
    u32 ard[4];
    {
      const int row = 32 * qb + r32, f = (row & 7) ^ ((row >> 3) & 1);
#pragma unroll
      for (int s = 0; s < 4; ++s) { ard[s] = L_AR1 + row * 128 + ((((2 * s) | hh) ^ f) << 4); PIN(ard[s]); }
    }
    u32 wl[3];
    wl[0] = (8 * hh + q4) * 128 + ((64 * hp + 32 * blk + 8 * p4) ^ ((q4 >> 1) << 6));
    wl[1] = wl[0] + 48 * KB; wl[2] = wl[0] + 96 * KB;
    PIN(wl[1]); PIN(wl[2]);
    bf16x8 hf_[3], qf_[3], kf_[3], vf_[3];
#define P1O(n) (wl[(((n) >> 2) % 5) >> 1] + ((((n) >> 2) % 5) & 1) * 24576 + ((n) & 3) * 2048)
#define P1H(n) hf_[(n) % 3] = asbf(lds_ld128(L, ard[(n) & 3] + (((n) >> 2) % 3) * 8192))
    if (w < 3) { load_wq(3); load_wq(4); }
    WAITV_BAR(32);
#pragma unroll
    for (int n = 0; n < 2; ++n) {
      P1H(n); kf_[n] = lds_tr(L, P1O(n) + 8192, P1O(n) + 8704); vf_[n] = lds_tr(L, P1O(n) + 16384, P1O(n) + 16896); qf_[n] = lds_tr(L, P1O(n), P1O(n) + 512);
    }
#pragma unroll
    for (int n = 0; n < 32; ++n) {
      const int m = n + 2;
      const bool bnd = (m & 3) == 0;
      SB();
      ak = MFMA(kf_[n % 3], hf_[n % 3], ak);
      if (m < 32 && !bnd) { P1H(m); kf_[m % 3] = lds_tr(L, P1O(m) + 8192, P1O(m) + 8704); }
      SB();
      av = MFMA(hf_[n % 3], vf_[n % 3], av);
      if (m < 32 && bnd) {
        PIN(ak); PIN(av);
        STAMP1(20 + m / 4);
        WAITV_BAR(m <= 16 ? 32 - 2 * m : 0);
        P1H(m); kf_[m % 3] = lds_tr(L, P1O(m) + 8192, P1O(m) + 8704);
      }
      if (m < 32) vf_[m % 3] = lds_tr(L, P1O(m) + 16384, P1O(m) + 16896);
      SB();
      aq = MFMA(qf_[n % 3], hf_[n % 3], aq);
      if (m < 32) qf_[m % 3] = lds_tr(L, P1O(m), P1O(m) + 512);
      PIN(ak); PIN(av); PIN(aq);
    }
    STAMP(5);
    STAMPC(30);
    const u32x4 f0 = pack8<0>(ak), f1 = pack8<8>(ak), f2 = pack8<0>(av), f3 = pack8<8>(av);
    const int kvi = ((u1 >> 1) * 16 + 2 * q1 + hp) * 4 + 2 * (u1 & 1) + qb;
    const u32 o = WS_KV + kvi * 4096 + lane * 16;
    __builtin_amdgcn_raw_buffer_store_b128(f0, rs, o, 0, 16);
    __builtin_amdgcn_raw_buffer_store_b128(f1, rs, o + 1024, 0, 16);
    __builtin_amdgcn_raw_buffer_store_b128(f2, rs, o + 2048, 0, 16);
    __builtin_amdgcn_raw_buffer_store_b128(f3, rs, o + 3072, 0, 16);
    aq *= QSCALE;
    qf = asbf(pack8<0>(aq));
    const u32x4 f5 = pack8<8>(aq);
    BAR();
    const u32 ok = L_KV + hp * 40960 + (8 + qb) * 4096 + lane * 16;
    lds_st128(L, ok, f0); lds_st128(L, ok + 1024, f1); lds_st128(L, ok + 2048, f2); lds_st128(L, ok + 3072, f3);
    lds_st128(L, L_QX + w * 1024 + lane * 16, f5);
    BAR();
    WAITV(0);
    if (lane == 0) __hip_atomic_store(FLAG(WS_FKV, kvi), tag, RLX_AGENT);
  } else {
    tag = __builtin_amdgcn_readfirstlane(*(LAS u32*)(L + L_TAG));
    if (w == 4 && lane == 0) __hip_atomic_store((GAS u32*)(ws + WS_ARR) + b, tag, RLX_AGENT);
    const u32 awE = L_AR1 + (16 * w4 + l3) * 128 + ((l7 ^ l3) << 4), awO = (awE ^ 16u) + 1024;
    u32x4 ca[2], sa[2];
#define CSLOAD(c) do { const u32 o_ = cso + (((c) + rot1) & 7) * 256; ca[(c) & 1] = lds_ld128(L, o_); sa[(c) & 1] = lds_ld128(L, o_ + 16); } while (0)
    CSLOAD(0);
#pragma unroll
    for (int c = 0; c < 8; ++c) {
      if (c < 7) CSLOAD(c + 1);
      SB();
      PIN(X[2 * c]);
      lds_st128(L, awE + (c % 3) * 8192, xform8(X[2 * c], ca[c & 1], sa[c & 1]));
      PIN(X[2 * c + 1]);
      lds_st128(L, awO + (c % 3) * 8192, xform8(X[2 * c + 1], ca[c & 1], sa[c & 1]));
      if (c >= 3 && c <= 5) {
        const u32 so = ((c + 2 + rot1) & 7) * 196608 + q1 * 128;
#pragma unroll
        for (int e = 0; e < 6; ++e) {
          const int pc = w4 * 6 + e;
          glds(srd_q, q8o, so + (pc >> 3) * 1024 + (pc & 7) * 24576, lbase + (c - 3) * 24576 + pc * 1024);
        }
      }
      if (c == 0) BAR();
      if (c >= 2) STAMP1(19 + c);
      if (c >= 2) asm volatile("s_waitcnt vmcnt(%0) lgkmcnt(2)\n\ts_barrier" ::"i"(c == 6 ? 12 : c == 7 ? 6 : 63) : "memory");
    }
    WAITV_BAR(0);
#pragma unroll
    for (int n = 0; n < 7; ++n) {
      const int pc = w4 + 4 * n, up = pc >= 14, e = (pc - 14 * up) * 64 + lane, rho = e / 14, cc = e - 14 * rho;
      guo[n] = (u32)((up ? (rho ^ 4) : rho) * 3584 + cc * 16);
    }
    STAMP(5);
    BAR();
    BAR();
    qf = asbf(lds_ld128(L, L_QX + (w4 ^ 1) * 1024 + lane * 16));

  }
  PIN(xr);
  STAMP(6);
#define LOAD_WO() do { if (!mw && qb) { for (int n = hp * 32; n < hp * 32 + 32; ++n) glds(srd_o, w8o, cg * 128 + n * 8192, lbase + L_WO + n * 1024); } } while (0)
  LOAD_WO();

  {
    const u32 ka = L_KV + hp * 40960 + hd * 1024 + lane * 16;
    const bool vdat = (r32 >> 4) == hd;
    const u32 va = vdat ? L_KV + hp * 40960 + 2048 + lane * 16 : L_ONES + (lane & 15) * 16;
    const u32 vs = vdat ? 4096u : 0u;
    f32x16 cm;
#pragma unroll
    for (int g = 0; g < 16; ++g) cm[g] = (g & 3) + 8 * (g >> 2) + 4 * hh <= r32 ? 0.f : NEGBIG;
    const f32x16 zero = {};
    f32x16 O = {};
    float m = NEGBIG;
    auto rowmax = [&](const f32x16& s, float mm) {
#pragma unroll
      for (int g = 0; g < 16; g += 2) mm = fmaxf(fmaxf(mm, s[g]), s[g + 1]);
      return mm;
    };
    auto ldk = [&](int lb) { return asbf(lds_ld128(L, ka + lb * 4096)); };
#pragma nounroll
    for (int st = 0; st < 2; ++st) {
      int lm = 8 + qb, lo = 8, hi = 8 + qb;
      if (st) {
        STAMP(7);
        if (u1 == 0) break;
        if (!qb) {
          const int g0 = 2 * u1 - 8 + 4 * hd, gl = g0 + (lane & 3);
          wait_flags(FLAG(WS_FKV, gl < 0 ? ((u1 >> 1) * 16 + 2 * q1 + hp) * 4 + 2 * (u1 & 1) : ((gl >> 2) * 16 + 2 * q1 + hp) * 4 + (gl & 3)), tag);
          const u32x4 srd_w = make_srd(ws, WS_BYTES);
#pragma unroll
          for (int k = 0; k < 4; ++k) {
            const int g = g0 + k;
            const u32 so = g < 0 ? 0x7ff00000u : WS_KV + (((g >> 2) * 16 + 2 * q1 + hp) * 4 + (g & 3)) * 4096;
#pragma unroll
            for (int n = 0; n < 4; ++n) glds_sc1(srd_w, lane * 16, so + n * 1024, lbase + L_KV + hp * 40960 + hd * 16384 + (4 * k + n) * 1024);
          }
          WAITV(0);
        }
        const int lmin = 8 - 2 * u1;
#pragma unroll
        for (int g = 0; g < 16; ++g) { float u = lmin > qb ? NEGBIG : NEGBIG - cm[g]; asm("" : "+v"(u)); cm[g] = u; }
        lm = qb; lo = lmin > qb ? lmin : qb + 1; hi = 8;
        BAR();
        STAMP(8);
      }
      bf16x8 kA = ldk(lo), kB = ldk(lo + 1);
      f32x16 sA = MFMA(ldk(lm), qf, cm), sB, sC;
      const bf16x8 k0 = kA;
      float mn = m;
      for (int lb = lo; lb < hi; lb += 2) {
        sB = MFMA(kA, qf, zero);
        sC = MFMA(kB, qf, zero);
        kA = ldk(lb + 2); kB = ldk(lb + 3);
        SB();
        mn = rowmax(sB, mn);
        if (lb + 1 < hi) mn = rowmax(sC, mn);
      }
      mn = rowmax(sA, mn);
      {
        auto r_ = __builtin_amdgcn_permlane32_swap(__float_as_uint(mn), __float_as_uint(mn), false, false);
        mn = fmaxf(__uint_as_float(r_[0]), __uint_as_float(r_[1]));
      }
      u32 vv = va + lm * vs;
      bf16x8 v0 = asbf(lds_ld128(L, vv)), v1 = asbf(lds_ld128(L, vv + 1024));
      f32x16 negm;
#pragma unroll
      for (int g = 0; g < 16; ++g) negm[g] = -mn;
#pragma unroll
      for (int g = 0; g < 16; ++g) { float u = sA[g] - mn; asm("" : "+v"(u)); sA[g] = u; }
      if (st) {
        const float al = __builtin_amdgcn_exp2f(m - mn);
#pragma unroll
        for (int g = 0; g < 16; ++g) { float o = O[g] * al; asm("" : "+v"(o)); O[g] = o; }
      }
      m = mn;
      kA = k0;
      vv = va + lo * vs;
#define P2STEP(CUR, NXT)                                                                      \
  {                                                                                           \
    NXT = MFMA(kA, qf, negm);                                                                 \
    PIN(NXT);                                                                                 \
    kA = ldk(lb + 1);                                                                         \
    const bf16x8 n0_ = asbf(lds_ld128(L, vv)), n1_ = asbf(lds_ld128(L, vv + 1024));           \
    vv += vs;                                                                                 \
    SB();                                                                                     \
    _Pragma("unroll") for (int g = 0; g < 8; ++g) CUR[g] = __builtin_amdgcn_exp2f(CUR[g]);    \
    O = MFMA(v0, asbf(pack8<0>(CUR)), O);                                                     \
    _Pragma("unroll") for (int g = 8; g < 16; ++g) CUR[g] = __builtin_amdgcn_exp2f(CUR[g]);   \
    O = MFMA(v1, asbf(pack8<8>(CUR)), O);                                                     \
    SB();                                                                                     \
    v0 = n0_; v1 = n1_;                                                                       \
  }
      for (int lb = lo;; ++lb) {
        P2STEP(sA, sB);
        if (lb >= hi) break;
        ++lb;
        P2STEP(sB, sA);
        if (lb >= hi) break;
      }
    }
    u32x4 of;
    {
      const float inv = __builtin_amdgcn_rcpf(hd ? O[0] : O[8]);
      float o[8];
#pragma unroll
      for (int g = 0; g < 8; ++g) { o[g] = (hd ? O[8 + g] : O[g]) * inv; asm("" : "+v"(o[g])); }
      of = (u32x4){pk(o[0], o[1]), pk(o[2], o[3]), pk(o[4], o[5]), pk(o[6], o[7])};
    }
    const int oi = (2 * u1 + qb) * 32 + 4 * q1 + 2 * hp + hd;
    __builtin_amdgcn_raw_buffer_store_b128(of, rs, WS_O + oi * 1024 + lane * 16, 0, 16);
    WAITV(0);
    if (lane == 0) __hip_atomic_store(FLAG(WS_FO, oi), tag, RLX_AGENT);
  }
  STAMP(9);
  {
    if (u1 == 0 && q1 == 0 && w == 7) {
#pragma nounroll
      for (int it = 0; it < SPIN_MAX; ++it) {
        bool ok = true;
#pragma unroll
        for (int k = 0; k < 4; ++k) ok &= __hip_atomic_load((const GAS u32*)(ws + WS_ARR) + lane + 64 * k, RLX_AGENT) == tag;
        if (__all(ok)) break;
        __builtin_amdgcn_s_sleep(1);
      }
      if (lane == 0) __hip_atomic_store((GAS u32*)(ws + WS_EPOCH), tag, RLX_AGENT);
    }
  }
  BAR();

  const u32 wt0 = (4 * hh + q4) * 128 + ((32 * blk + 8 * p4) ^ ((q4 >> 1) << 6)), wt1 = wt0 ^ 64u;
  auto red_write = [&](u32 base, u32 S8, const f32x16& a0, const f32x16& a1) {
    u32 ln = lane_id();
    const u32 o = base + ((ln & 31) >> 3) * S8 + (ln & 7) * 256, sw = (ln & 7) ^ (ln >> 5);
#pragma unroll
    for (int g = 0; g < 4; ++g) {
      *(LAS f32x4*)(L + o + (((2 * g) ^ sw) << 4)) = (f32x4){a0[4 * g], a0[4 * g + 1], a0[4 * g + 2], a0[4 * g + 3]};
      *(LAS f32x4*)(L + o + (((2 * g) ^ sw) << 4) + 128) = (f32x4){a1[4 * g], a1[4 * g + 1], a1[4 * g + 2], a1[4 * g + 3]};
    }
  };
  auto red_read = [&](u32 base, u32 S8, u32 SK, f32x4& lo, f32x4& hi) {
    u32 ln = lane_id();
    const u32 o0 = base + w * S8 + (ln >> 3) * 256 + (((2 * (ln & 7)) ^ (ln >> 3)) << 4), o1 = o0 ^ 16u;
    lo = lds_ldf4(L, o0); hi = lds_ldf4(L, o1);
#pragma unroll
    for (int k = 1; k < 4; ++k) { lo += lds_ldf4(L, o0 + k * SK); hi += lds_ldf4(L, o1 + k * SK); }
  };
  u32x4 x1p;
  {
    if (mw) {
      bf16x8 wa[8], wb[8];
#pragma unroll
      for (int i = 0; i < 8; ++i) {
        const u32 o = L_WO + w * 16384 + i * 2048;
        wa[i] = lds_tr(L, o + wt0, o + wt0 + 1024); wb[i] = lds_tr(L, o + wt1, o + wt1 + 1024);
      }
      wait_flags(FLAG(WS_FO, (t3 * 4 + 2 * hf + (l3 & 1)) * 32 + 8 * w + l7), tag);
      STAMP(10);
      u32x4 of0[8], of1[8];
      const u32 oo = WS_O + ((t3 * 4 + 2 * hf) * 32 + 8 * w) * 1024 + lane * 16;
#pragma unroll
      for (int i = 0; i < 8; ++i) {
        of0[i] = __builtin_amdgcn_raw_buffer_load_b128(rs, oo + i * 1024, 0, 16);
        of1[i] = __builtin_amdgcn_raw_buffer_load_b128(rs, oo + 32768 + i * 1024, 0, 16);
      }
      SB();
      f32x16 a00 = {}, a01 = {}, a10 = {}, a11 = {};
#pragma unroll
      for (int i = 0; i < 8; ++i) {
        a00 = MFMA(wa[i], asbf(of0[i]), a00); a01 = MFMA(wb[i], asbf(of0[i]), a01);
        a10 = MFMA(wa[i], asbf(of1[i]), a10); a11 = MFMA(wb[i], asbf(of1[i]), a11);
      }
      STAMP(11);
      red_write(L_WO + w * 16384, 2048, a00, a01);
      red_write(L_WO + w * 16384 + 8192, 2048, a10, a11);
    } else {
      load_gu(0, gu_slot(0));
      u32x4 bv[2];
      {
        u32x4 q[4];
        u32 v[3];
        const GAS u64* g = (const GAS u64*)(ws + WS_MODG);
        const u32 qo = (((r32 & 1) ? 768 : 1024) + rot4 * 32 + (t4 >> 3) * 16 + 4 * hh) * 8;
#pragma nounroll
        for (int it = 0; it < SPIN_MAX; ++it) {
          const u64 x1_ = __hip_atomic_load(g + 1024 + (tid - 256), RLX_AGENT);
          const u64 x2 = __hip_atomic_load(g + (lane < 32 ? 512 : 1248) + cg * 32 + lane, RLX_AGENT);
          bool ok = (u32)(x1_ >> 32) == tag && (u32)(x2 >> 32) == tag;
#pragma unroll
          for (int k = 0; k < 4; ++k) { q[k] = __builtin_amdgcn_raw_buffer_load_b128(rs, qo + (k >> 1) * 64 + (k & 1) * 16, WS_MODG, 16); ok &= q[k][1] == tag && q[k][3] == tag; }
          v[1] = (u32)x1_; v[2] = (u32)x2;
          if (__all(ok)) break;
          __builtin_amdgcn_s_sleep(1);
        }
        *(LAS f32x2*)(L + L_C1B + (tid - 256) * 8) = (f32x2){rb(1.f + bf_lo(v[1])), rb(1.f + bf_hi(v[1]))};
        if (w == 4) *(LAS f32x2*)(L + L_GA + lane * 8) = (f32x2){bf_lo(v[2]), bf_hi(v[2])};
        const float one = (r32 & 1) ? 0.f : 1.f;
#pragma unroll
        for (int s = 0; s < 2; ++s)
          bv[s] = (u32x4){pk(one + bf_lo(q[2 * s][0]), one + bf_hi(q[2 * s][0])), pk(one + bf_lo(q[2 * s][2]), one + bf_hi(q[2 * s][2])),
                          pk(one + bf_lo(q[2 * s + 1][0]), one + bf_hi(q[2 * s + 1][0])), pk(one + bf_lo(q[2 * s + 1][2]), one + bf_hi(q[2 * s + 1][2]))};
      }
      STAMP(10);
      lds_post(lbase + L_LAND + w4 * 4, 1);
      load_gu(1, gu_slot(1));
      lds_wait(L, L_LAND, 1);
      {
        const u32 gl = blk * 14336 + (8 * hh + q4) * 224 + 8 * p4 + w4 * 32 + (t4 >> 3) * 7168 + gu_slot(0);
        const u32 g0 = gl + (blk ? 896u : 0u), g1 = gl + (blk ? 0u : 896u);
        f32x16 a0 = {}, a1 = {};
#pragma unroll
        for (int s = 0; s < 2; ++s) {
          a0 = MFMA(lds_tr(L, g0 + s * 3584, g1 + s * 3584), asbf(bv[s]), a0);
          a1 = MFMA(lds_tr(L, g0 + s * 3584 + 128, g1 + s * 3584 + 128), asbf(bv[s]), a1);
        }
        if (r32 < 2) {
          const u32 o = WS_UV + (j4 * 16 + t4) * 2048 + w4 * 256 + r32 * 128 + hh * 16;
#pragma unroll
          for (int g = 0; g < 4; ++g) {
            __builtin_amdgcn_raw_buffer_store_b128(__builtin_bit_cast(u32x4, (f32x4){a0[4 * g], a0[4 * g + 1], a0[4 * g + 2], a0[4 * g + 3]}), rs, o + g * 32, 0, 16);
            if (w4 < 3) __builtin_amdgcn_raw_buffer_store_b128(__builtin_bit_cast(u32x4, (f32x4){a1[4 * g], a1[4 * g + 1], a1[4 * g + 2], a1[4 * g + 3]}), rs, o + 1024 + g * 32, 0, 16);
          }
        }
      }
      load_gu(2, gu_slot(2));
      STAMP(11);
    }
    BAR();
    f32x4 lo, hi;
    red_read(L_WO, 2048, 16384, lo, hi);
    const f32x4 ga0 = lds_ldf4(L, cso + M_GA), ga1 = lds_ldf4(L, cso + M_GA + 16);
    float v[8];
    v[0] = rb(bf_lo(xr[0]) + rb(ga0[0] * rb(lo[0]))); v[1] = rb(bf_hi(xr[0]) + rb(ga0[1] * rb(lo[1])));
    v[2] = rb(bf_lo(xr[1]) + rb(ga0[2] * rb(lo[2]))); v[3] = rb(bf_hi(xr[1]) + rb(ga0[3] * rb(lo[3])));
    v[4] = rb(bf_lo(xr[2]) + rb(ga1[0] * rb(hi[0]))); v[5] = rb(bf_hi(xr[2]) + rb(ga1[1] * rb(hi[1])));
    v[6] = rb(bf_lo(xr[3]) + rb(ga1[2] * rb(hi[2]))); v[7] = rb(bf_hi(xr[3]) + rb(ga1[3] * rb(hi[3])));
    x1p = (u32x4){pk(v[0], v[1]), pk(v[2], v[3]), pk(v[4], v[5]), pk(v[6], v[7])};
    PIN(x1p);
    {
      const f32x4 c0 = lds_ldf4(L, cso + M_C1B + cg * 256), c1 = lds_ldf4(L, cso + M_C1B + cg * 256 + 16);
      const u32x4 y = {pk(v[0] * c0[0], v[1] * c0[1]), pk(v[2] * c0[2], v[3] * c0[3]), pk(v[4] * c1[0], v[5] * c1[1]), pk(v[6] * c1[2], v[7] * c1[3])};
      __builtin_amdgcn_raw_buffer_store_b128(y, rs, WS_X1 + ((t3 * 4 + 2 * hf + (w >> 2)) * 32 + 4 * cg + (l7 >> 1)) * 1024 + ((l7 & 1) * 32 + 8 * w4 + l3) * 16, 0, 16);
    }
    const float sm = sum8(((v[0] + v[1]) + (v[2] + v[3])) + ((v[4] + v[5]) + (v[6] + v[7])));
    const float mu = sm * (1.f / 64.f);
    float m2 = 0.f;
#pragma unroll
    for (int k = 0; k < 8; ++k) m2 = fmaf(v[k] - mu, v[k] - mu, m2);
    m2 = sum8(m2);
    if (l7 == 0) __hip_atomic_store((GAS u64*)(ws + WS_ST) + orow * 8 + cg, ((u64)__float_as_uint(m2) << 32) | __float_as_uint(sm), RLX_AGENT);
    WAITV(0);
    if (lane == 0) __hip_atomic_store(FLAG(WS_FX, (t3 * 4 + 2 * hf + (w >> 2)) * 32 + cg * 4 + w4), tag, RLX_AGENT);
    if (!mw && lane == 1) __hip_atomic_store(FLAG(WS_FUV, j4 * 64 + t4 * 4 + w4), tag, RLX_AGENT);
  }
  STAMP(12);
  BAR();

  if (mw) {
    u32 ln = lane_id();
    const u32 bk = (ln >> 4) & 1, gl = bk * 14336 + (8 * (ln >> 5) + ((ln & 15) >> 2)) * 224 + 8 * (ln & 3);
    u32 g0 = gl + (bk ? 896u : 0u) + 36 * KB, g1 = gl + (bk ? 0u : 896u) + 36 * KB, g0h = g0 + 56 * KB, g1h = g1 + 56 * KB;
    PIN(g0); PIN(g1); PIN(g0h); PIN(g1h);
    f32x16 ag[7] = {};
    bf16x8 wf_[7];
    u32x4 yf[32];
#define P4S(n) (((n) >> 2) & 3)
#define P4O(n) ((P4S(n) & 1 ? 0 : 28 * KB) + ((n) & 3) * 3584)
#define P4W(n, f) wf_[f] = lds_tr(L, (P4S(n) == 1 || P4S(n) == 2 ? g0h : g0) + P4O(n) + (f) * 32, (P4S(n) == 1 || P4S(n) == 2 ? g1h : g1) + P4O(n) + (f) * 32)
#pragma unroll
    for (int f = 0; f < 7; ++f) P4W(0, f);

    wait_flags(FLAG(WS_FX, (t4 * 4 + w) * 32 + (ln & 31)), tag);
    STAMP(13);
    auto ld_y = [&](int n) { yf[n] = __builtin_amdgcn_raw_buffer_load_b128(rs, ln * 16, WS_X1 + ((t4 * 4 + w) * 32 + ((n + 4 * rot4) & 31)) * 1024, 16); };
#pragma unroll
    for (int n = 0; n < 5; ++n) ld_y(n);
    const u32 a_done = lbase + L_DONE + w * 4;
    u32x4 land;
    static_for<0, 32>([&](auto nc) {
      constexpr int n = nc.value, m = n + 1;
      if constexpr (n + 2 >= 27 && n + 2 < 32) yf[n + 2] = lds_ld128(L, L_YL + (w * 5 + (n + 2 - 27)) * 1024 + ln * 16);
#pragma unroll
      for (int f = 0; f < 7; ++f) {
        SB();
        ag[f] = MFMA(wf_[f], asbf(yf[n]), ag[f]);
        if ((m & 3) == 0 && f == 0) {
          STAMP4(19 + m / 4);
          lds_post(a_done, m / 4);
          if (m >= 12) { PIN(ag[0]); if (!all_ge(land, m / 4)) lds_wait(L, L_LAND, m / 4); }
        }
        if ((m & 3) == 3 && m >= 11 && f == 0) land = *(volatile LAS u32x4*)(L + L_LAND);
        if (m < 32) P4W(m, f);
        if constexpr (n < 11) { if (f == 1 || f == 4) ld_y(5 + 2 * n + (f >> 2)); }
      }
#pragma unroll
      for (int f = 0; f < 7; ++f) PIN(ag[f]);
    });
    const u32x4 bst = lds_ld128(L, L_BST + (32 * w + (ln & 31)) * 16);
    const u32x4 bfr = {ln < 32 ? bst[0] : 0u, ln < 32 ? bst[1] : 0u, 0u, 0u};
#pragma unroll
    for (int f = 0; f < 7; ++f) {
      const u32x2 uv = *(LAS u32x2*)(L + L_UVT + (f * 32 + (ln & 31)) * 8);
      ag[f] = MFMA(asbf((u32x4){uv[0], uv[1], 0u, 0u}), asbf(bfr), ag[f]);
    }
    const float rs_ = __uint_as_float(bst[2]), sd_ = bf_lo(bst[1]);
    const float k1 = -2.0f * 0.7978845608028654f * 1.4426950408889634f * rs_, k3 = k1 * 0.044715f * rs_ * rs_, i2 = sd_ * sd_;
    STAMP(15);
    ln = lane_id();
    const u32 ao = WS_ACT + ((t4 * 4 + w) * 112 + 7 * j4) * 1024 + ln * 16;
#pragma unroll
    for (int f = 0; f < 7; ++f) {
      u32x4 o;
#pragma unroll
      for (int k = 0; k < 4; ++k) { const f32x2 a = geglu2((f32x2){ag[f][2 * k], ag[f][2 * k + 1]}, (f32x2){ag[f][8 + 2 * k], ag[f][9 + 2 * k]}, k1, k3, i2); o[k] = pk(a[0], a[1]); }
      __builtin_amdgcn_raw_buffer_store_b128(o, rs, ao + f * 1024, 0, 16);
    }
    WAITV(0);
    if (ln == 0) __hip_atomic_store(FLAG(WS_FA, (t4 * 4 + w) * 16 + j4), tag, RLX_AGENT);
    STAMP(16);
  } else {
    const u32 lane = lane_id(), r32 = lane & 31, hh = lane >> 5;
    const u32 w8o = (lane >> 3) * 1024u + (((lane & 7) ^ (((lane >> 4) & 1) << 2)) << 4);
    const u32 wt0 = (4 * hh + ((lane & 15) >> 2)) * 128 + ((32 * ((lane >> 4) & 1) + 8 * (lane & 3)) ^ (((lane & 15) >> 3) << 6)), wt1 = wt0 ^ 64u;
    auto load_wd = [&](int i) {
      int ks = rot5; PINS(ks);
      ks += i; ks -= ks >= 28 ? 28 : 0;
      const u32 so = cg * 128 + (w4 * 448 + ks * 16) * 1024, ld = lbase + wd_base(i) + w4 * 2048;
      glds(srd_d, w8o, so, ld);
      glds(srd_d, w8o, so + 8192, ld + 1024);
    };
    const u32 a_land = lbase + L_LAND + w4 * 4;
    bf16x8 wp0[11], wp1[11];
    u32 dC0 = w4 * 2048 + wt0, dC1 = w4 * 2048 + wt1, dB0 = dC0 + 92 * KB, dB1 = dC1 + 92 * KB, dA0 = dC0 + 64 * KB, dA1 = dC1 + 64 * KB;
    PIN(dA0); PIN(dA1); PIN(dB0); PIN(dB1); PIN(dC0); PIN(dC1);
#define WD0(i) (wd_base(i) < 64 * KB ? dC0 + wd_base(i) : wd_base(i) < 92 * KB ? dA0 + (wd_base(i) - 64 * KB) : dB0 + (wd_base(i) - 92 * KB))
#define WD1(i) (wd_base(i) < 64 * KB ? dC1 + wd_base(i) : wd_base(i) < 92 * KB ? dA1 + (wd_base(i) - 64 * KB) : dB1 + (wd_base(i) - 92 * KB))
#define WDTURN(i)                                                                                                                   \
  do {                                                                                                                              \
    _Pragma("unroll") for (int k = (i); k < (i) + 2; ++k) { wp0[k] = lds_tr(L, WD0(k), WD0(k) + 1024); wp1[k] = lds_tr(L, WD1(k), WD1(k) + 1024); } \
    _Pragma("unroll") for (int k = (i); k < (i) + 2; ++k) { PIN(wp0[k]); PIN(wp1[k]); }                                             \
    load_wd((i) + 2); load_wd((i) + 3);                                                                                             \
  } while (0)
    load_gu(3, gu_slot(3));
    load_wd(0); load_wd(1);
    WAITV(4); lds_post(a_land, 3);
    lds_wait(L, L_DONE, 1);
    STAMP(13);
    {
      load_gu(4, gu_slot(4));
      const u32x4 srd_w = make_srd(ws, WS_BYTES);
#pragma unroll
      for (int k = 0; k < 5; ++k) glds_sc1(srd_w, lane * 16, WS_X1 + ((t4 * 4 + w4) * 32 + ((27 + k + 4 * rot4) & 31)) * 1024, lbase + L_YL + (w4 * 5 + k) * 1024);
      STAMP4(21);
      lds_wait(L, L_DONE, 2); load_gu(5, gu_slot(5)); WAITV(12); lds_post(a_land, 4); WDTURN(0); STAMP4(22);
      lds_wait(L, L_DONE, 3); load_gu(6, gu_slot(6)); WAITV(11); lds_post(a_land, 5); STAMP4(23);
      lds_wait(L, L_DONE, 4); load_gu(7, gu_slot(7)); WAITV(7); lds_post(a_land, 6); WDTURN(2); WAITV(4); lds_post(a_land, 7); STAMP4(24);
        wait_flags(FLAG(WS_FUV, j4 * 64 + lane), tag);
        {
          u32x4 pt[16];
          const u32 o = lane < 28 ? (w4 * 28 + lane) * 16 : 0x7fffffffu;
#pragma unroll
          for (int k = 0; k < 16; ++k) pt[k] = __builtin_amdgcn_raw_buffer_load_b128(rs, o, WS_UV + (j4 * 16 + k) * 2048, 16);
          SB();
          f32x4 a = {};
#pragma unroll
          for (int k = 0; k < 16; ++k) {
            const f32x4 t_ = __builtin_bit_cast(f32x4, pt[k]);
#pragma unroll
            for (int e = 0; e < 4; ++e) { float y_ = a[e] + t_[e]; asm("" : "+v"(y_)); a[e] = y_; }
          }
          if (lane < 28) {
            const u32 vi = (w4 * 28 + lane) * 4;
#pragma unroll
            for (int e = 0; e < 4; ++e) {
              const float hi = rb(a[e]);
              *(LAS u32*)(L + L_UVT + ((vi >> 6) * 32 + (vi & 31) + e) * 8 + ((vi >> 5) & 1) * 4) = pk(hi, a[e] - hi);
            }
          }
        }
      WDTURN(4);
      {
        const u32 so = (t4 * 128 + 32 * w4 + r32) * 64 + hh * 32;
        const u32x4 s0 = __builtin_amdgcn_raw_buffer_load_b128(rs, so, WS_ST, 16), s1 = __builtin_amdgcn_raw_buffer_load_b128(rs, so + 16, WS_ST, 16);
        auto xsum = [&](float v) {
          auto r_ = __builtin_amdgcn_permlane32_swap(__float_as_uint(v), __float_as_uint(v), false, false);
          return __uint_as_float(r_[0]) + __uint_as_float(r_[1]);
        };
        const float a0 = __uint_as_float(s0[0]), a1 = __uint_as_float(s0[2]), a2 = __uint_as_float(s1[0]), a3 = __uint_as_float(s1[2]);
        const float me = xsum((a0 + a1) + (a2 + a3)) * (1.f / 512.f);
        const float d0 = a0 * (1.f / 64.f) - me, d1 = a1 * (1.f / 64.f) - me, d2 = a2 * (1.f / 64.f) - me, d3 = a3 * (1.f / 64.f) - me;
        const float m2 = (__uint_as_float(s0[1]) + __uint_as_float(s0[3])) + (__uint_as_float(s1[1]) + __uint_as_float(s1[3]));
        float mb, sd;
        ln_scal(me, xsum(m2 + 64.f * ((d0 * d0 + d1 * d1) + (d2 * d2 + d3 * d3))) * (1.f / 512.f), mb, sd);
        if (lane < 32) lds_st128(L, L_BST + (32 * w4 + lane) * 16, (u32x4){pk(-mb, -mb), pk(sd, sd), __float_as_uint(__builtin_amdgcn_rcpf(sd)), 0u});
      }
      lds_post(a_land, 8);
      STAMP4(25);
      lds_wait(L, L_DONE, 5); load_wd(8); load_wd(9); load_wd(10);
      lds_wait(L, L_DONE, 6); load_wd(11); load_wd(12); load_wd(13);
      lds_wait(L, L_DONE, 7); load_wd(14); load_wd(15); load_wd(16); load_wd(17);
      lds_wait(L, L_DONE, 8);
    }
    STAMP(15);
#pragma unroll
    for (int i = 18; i < 23; ++i) load_wd(i);
    WAITV(24);
#pragma unroll
    for (int k = 6; k < 11; ++k) { wp0[k] = lds_tr(L, WD0(k), WD0(k) + 1024); wp1[k] = lds_tr(L, WD1(k), WD1(k) + 1024); }
#pragma unroll
    for (int k = 6; k < 11; ++k) { PIN(wp0[k]); PIN(wp1[k]); }
#pragma unroll
    for (int i = 23; i < 28; ++i) load_wd(i);
    STAMP(16);
    wait_flags(FLAG(WS_FA, (t3 * 4 + 2 * hf + ((lane >> 2) & 1)) * 16 + 4 * w4 + (lane & 3)), tag);
    STAMP(17);
    const u32 ao = WS_ACT + ((t3 * 4 + 2 * hf) * 112 + 28 * w4) * 1024 + lane * 16;
    u32x4 af0[28], af1[28];
    auto ld_act = [&](int i) {
      int ks = i + rot5; ks -= ks >= 28 ? 28 : 0;
      af0[i] = __builtin_amdgcn_raw_buffer_load_b128(rs, ao, ks * 1024, 16);
      af1[i] = __builtin_amdgcn_raw_buffer_load_b128(rs, ao + 112 * 1024, ks * 1024, 16);
    };
#pragma unroll
    for (int i = 0; i < 5; ++i) ld_act(i);
    f32x16 a00 = {}, a01 = {}, a10 = {}, a11 = {};
    static_for<0, 28>([&](auto ic) {
      constexpr int i = ic.value;
      SB();
      a00 = MFMA(wp0[i % 11], asbf(af0[i]), a00);
      a01 = MFMA(wp1[i % 11], asbf(af0[i]), a01);
      SB();
      a10 = MFMA(wp0[i % 11], asbf(af1[i]), a10);
      if constexpr (i < 6) { ld_act(5 + 2 * i); ld_act(6 + 2 * i); }
      else if constexpr (i + 11 < 28) ld_act(i + 11);
      SB();
      a11 = MFMA(wp1[i % 11], asbf(af1[i]), a11);
      if constexpr (i >= 8 && i + 3 < 28) {
        wp0[(i + 3) % 11] = lds_tr(L, WD0(i + 3), WD0(i + 3) + 1024);
        wp1[(i + 3) % 11] = lds_tr(L, WD1(i + 3), WD1(i + 3) + 1024);
      }
      PIN(a00); PIN(a01); PIN(a10); PIN(a11);
    });
    STAMP(18);
    red_write(20 * KB + w4 * 2048, 8192, a00, a01);
    red_write(92 * KB + w4 * 2048, 8192, a10, a11);
  }
  BAR();
  {
    f32x4 lo, hi;
    red_read(mw ? 20 * KB : 60 * KB, 8192, 2048, lo, hi);
    u32 ln = lane_id();
    const f32x4 gb0 = lds_ldf4(L, L_MISC + M_GB + (ln & 7) * 32), gb1 = lds_ldf4(L, L_MISC + M_GB + (ln & 7) * 32 + 16);
    u32x4 o;
    o[0] = pk(bf_lo(x1p[0]) + rb(gb0[0] * rb(lo[0])), bf_hi(x1p[0]) + rb(gb0[1] * rb(lo[1])));
    o[1] = pk(bf_lo(x1p[1]) + rb(gb0[2] * rb(lo[2])), bf_hi(x1p[1]) + rb(gb0[3] * rb(lo[3])));
    o[2] = pk(bf_lo(x1p[2]) + rb(gb1[0] * rb(hi[0])), bf_hi(x1p[2]) + rb(gb1[1] * rb(hi[1])));
    o[3] = pk(bf_lo(x1p[3]) + rb(gb1[2] * rb(hi[2])), bf_hi(x1p[3]) + rb(gb1[3] * rb(hi[3])));
    *(GAS u32x4*)((GAS char*)out + (t3 * 128 + hf * 64 + 8 * w) * 1024 + cg * 128 + (ln >> 3) * 1024 + (ln & 7) * 16) = o;
  }
  STAMP(19);
  asm volatile(".Ladaln_end:");
}

extern "C" void kernel_launch(void* const* d_in, const int* in_sizes, int n_in, void* d_out, int out_size, void* d_ws, size_t ws_size,
                              hipStream_t stream) {
  adaln_block<<<256, 512, 0, stream>>>((const u16*)d_in[0], (const u16*)d_in[1], (const u16*)d_in[2], (const u16*)d_in[4], (const u16*)d_in[5],
                                       (const u16*)d_in[6], (const u16*)d_in[7], (const u16*)d_in[8], (u16*)d_out, (char*)d_ws);
}
```

```cpp
#define PRE 1
#include <hip/hip_runtime.h>
#include <stdint.h>

typedef unsigned short u16;
typedef unsigned int u32;
typedef unsigned long long u64;
typedef __attribute__((ext_vector_type(8))) __bf16 bf16x8;
typedef __attribute__((ext_vector_type(2))) __bf16 bf16x2;
typedef __attribute__((ext_vector_type(4))) short s16x4;
typedef __attribute__((ext_vector_type(8))) short s16x8;
typedef __attribute__((ext_vector_type(16))) float f32x16;
typedef __attribute__((ext_vector_type(4))) float f32x4;
typedef __attribute__((ext_vector_type(2))) float f32x2;
typedef __attribute__((ext_vector_type(4))) u32 u32x4;
typedef __attribute__((ext_vector_type(2))) u32 u32x2;
#define LAS __attribute__((address_space(3)))
#define GAS __attribute__((address_space(1)))
#define DI __device__ __forceinline__
#define RLX_AGENT __ATOMIC_RELAXED, __HIP_MEMORY_SCOPE_AGENT

constexpr u32 WS_EPOCH = 0;
constexpr u32 WS_ARR = 1024;
constexpr u32 WS_MODG = 4096;
constexpr u32 WS_ST = 65536;
constexpr u32 WS_UV = 196608;
constexpr u32 WS_KV = 1048576;
constexpr u32 WS_O = WS_KV + 4194304;
constexpr u32 WS_X1 = WS_O + 2097152;
constexpr u32 WS_ACT = WS_X1 + 2097152;
#define WO_AT 0
#define FS 16
#define MREP 8
constexpr u32 WS_FKV = WS_ACT + 7340032;
constexpr u32 WS_FO = WS_FKV + 1024 * 4 * FS;
constexpr u32 WS_FX = WS_FO + 2048 * 4 * FS;
constexpr u32 WS_FA = WS_FX + 2048 * 4 * FS;
constexpr u32 WS_FUV = WS_FA + 1024 * 4 * FS;
constexpr u32 WS_MOD1 = WS_FUV + 1024 * 4 * FS;
constexpr u32 WS_DBG = 20971520;
constexpr u32 WS_BYTES = WS_DBG + 1048576;
static_assert(WS_MOD1 + 8 * 4096 <= WS_DBG && WS_UV + 256 * 2048 <= WS_KV, "workspace map");
#define FLAG(base, i) ((GAS u32*)(ws + (base)) + (i) * FS)

constexpr int SPIN_MAX = 1 << 17;
constexpr u32 KB = 1024;
constexpr u32 L_WO = 0;
constexpr u32 L_KV = 64 * KB;
constexpr u32 L_QX = 144 * KB;
constexpr u32 L_AR1 = 120 * KB;
constexpr u32 L_YL = 0;
constexpr u32 L_MISC = 148 * KB;
constexpr u32 L_C1 = L_MISC, L_SH = L_MISC + 2048, L_C1B = L_MISC + 4096, L_SHB = L_MISC + 6144, M_C1B = 4096;
constexpr u32 L_UVT = L_C1, L_BST = L_SH;
constexpr u32 L_ONES = L_MISC + 8192;
constexpr u32 L_GA = L_ONES + 256, M_GA = 8192 + 256, M_GB = 8192 + 512;
constexpr u32 L_DONE = L_ONES + 784, L_LAND = L_ONES + 800, L_MODF = L_ONES + 816;
constexpr u32 L_TAG = L_ONES + 768, L_JUNK = L_ONES + 1536;
constexpr u32 L_RED = L_MISC + 10240;
constexpr u32 LDS_BYTES = 163840;
DI constexpr u32 gu_slot(int c) { return (c & 3) == 3 ? 36 * KB : 64 * KB + (c & 3) * 28 * KB; }
DI constexpr u32 wd_base(int i) {
  return i < 8 ? 20 * KB + (i & 1) * 8192u : i < 11 ? 64 * KB + (i - 8) * 8192u : i < 18 ? 92 * KB + (i - 11) * 8192u : i < 21 ? 36 * KB + (i - 18) * 8192u
       : i < 23 ? (i - 21) * 8192u : i < 25 ? 20 * KB + (i - 23) * 8192u : 64 * KB + (i - 25) * 8192u;
}
constexpr u32 CODE_LINES = 16;

constexpr float NEGBIG = -1e30f;
constexpr float QSCALE = 0.25f * 1.4426950408889634f;

template <int I> struct IC { static constexpr int value = I; };
template <int I, int N, class F> DI void static_for(F&& f) { if constexpr (I < N) { f(IC<I>{}); static_for<I + 1, N>(f); } }
DI u32 lane_id() { u32 r; asm volatile("v_mbcnt_lo_u32_b32 %0, -1, 0\n\tv_mbcnt_hi_u32_b32 %0, -1, %0" : "=v"(r)); return r; }
DI float bf_lo(u32 v) { return __uint_as_float(v << 16); }
DI float bf_hi(u32 v) { return __uint_as_float(v & 0xffff0000u); }
DI u32 pk(float a, float b) { f32x2 v = {a, b}; return __builtin_bit_cast(u32, __builtin_convertvector(v, bf16x2)); }
DI float rb(float a) { return bf_lo(pk(a, a)); }
template <int O> DI u32x4 pack8(const f32x16& a) { return (u32x4){pk(a[O], a[O + 1]), pk(a[O + 2], a[O + 3]), pk(a[O + 4], a[O + 5]), pk(a[O + 6], a[O + 7])}; }
DI bf16x8 asbf(u32x4 v) { return __builtin_bit_cast(bf16x8, v); }
#define MFMA(a, b, c) __builtin_amdgcn_mfma_f32_32x32x16_bf16((a), (b), (c), 0, 0, 0)
template <int CTRL, int RM = 0xf> DI float dpp(float v) { return __builtin_bit_cast(float, __builtin_amdgcn_update_dpp(0, __builtin_bit_cast(int, v), CTRL, RM, 0xf, true)); }
DI float sum8(float v) { v += dpp<0xB1>(v); v += dpp<0x4E>(v); v += dpp<0x141>(v); return v; }
DI float dot2(u32 a, u32 b, float c) { return __builtin_amdgcn_fdot2_f32_bf16(__builtin_bit_cast(bf16x2, a), __builtin_bit_cast(bf16x2, b), c, false); }

DI u32x4 lds_ld128(LAS char* L, u32 off) { return *(LAS u32x4*)(L + off); }
DI f32x4 lds_ldf4(LAS char* L, u32 off) { return *(LAS f32x4*)(L + off); }
DI void lds_st128(LAS char* L, u32 off, u32x4 v) { *(LAS u32x4*)(L + off) = v; }
DI bf16x8 lds_tr(LAS char* L, u32 off0, u32 off1) {
  s16x4 a = __builtin_amdgcn_ds_read_tr16_b64_v4i16((LAS s16x4*)(L + off0));
  s16x4 b = __builtin_amdgcn_ds_read_tr16_b64_v4i16((LAS s16x4*)(L + off1));
  return __builtin_bit_cast(bf16x8, __builtin_shufflevector(a, b, 0, 1, 2, 3, 4, 5, 6, 7));
}
DI u32x4 make_srd(const void* p, u32 bytes) { const u64 a = (u64)p; return (u32x4){(u32)a, (u32)(a >> 32), bytes, 0x00020000u}; }
DI void glds(const u32x4& srd, u32 voff, u32 soff, u32 ldsaddr) {
  asm volatile("s_mov_b32 m0, %3\n\ts_nop 0\n\tbuffer_load_dwordx4 %0, %1, %2 offen lds" ::"v"(voff), "s"(srd), "s"(soff), "s"(ldsaddr) : "memory");
}
DI void glds_sc1(const u32x4& srd, u32 voff, u32 soff, u32 ldsaddr) {
  asm volatile("s_mov_b32 m0, %3\n\ts_nop 0\n\tbuffer_load_dwordx4 %0, %1, %2 offen sc1 lds" ::"v"(voff), "s"(srd), "s"(soff), "s"(ldsaddr) : "memory");
}
DI void touch(const u32x4& srd, u32 voff, u32 ldsaddr) {
  asm volatile("s_mov_b32 m0, %2\n\ts_nop 0\n\tbuffer_load_dword %0, %1, 0 offen lds" ::"v"(voff), "s"(srd), "s"(ldsaddr) : "memory");
}
DI void lds_post(u32 addr, u32 val) {
  asm volatile("s_mov_b64 exec, 1\n\tds_write_b32 %0, %1\n\ts_mov_b64 exec, -1" ::"v"(addr), "v"(val) : "memory");
}
DI bool all_ge(u32x4 v, u32 k) { return __builtin_amdgcn_readfirstlane(min(min(v[0], v[1]), min(v[2], v[3]))) >= k; }
DI void lds_wait(LAS char* L, u32 off, u32 k) {
#pragma nounroll
  for (int it = 0; it < (SPIN_MAX << 4); ++it) {
    if (all_ge(*(volatile LAS u32x4*)(L + off), k)) break;
    __builtin_amdgcn_s_sleep(3);
  }
  asm volatile("" ::: "memory");
}
#define BAR() asm volatile("s_waitcnt lgkmcnt(0)\n\ts_barrier" ::: "memory")
#define WAITV_BAR(N) asm volatile("s_waitcnt vmcnt(%0) lgkmcnt(0)\n\ts_barrier" ::"i"(N) : "memory")
#define WAITV(N) asm volatile("s_waitcnt vmcnt(%0)" ::"i"(N) : "memory")
#define SB() __builtin_amdgcn_sched_barrier(0)
#define PIN(x) asm volatile("" : "+v"(x))
#define PINS(x) asm volatile("" : "+s"(x))
#define ORDER() __builtin_amdgcn_fence(__ATOMIC_ACQUIRE, "wavefront")

DI void wait_flags(const GAS u32* p, u32 tag) {
  u32 v = tag + 1u;
#pragma nounroll
  for (int it = 0; it < SPIN_MAX; ++it) {
    if (v != tag) v = __hip_atomic_load(p, RLX_AGENT);
    if (__all(v == tag)) break;
    __builtin_amdgcn_s_sleep(4);
  }
  ORDER();
}
template <int N> DI void gather_gran(const GAS u64* g, u32 tag, u32 (&v)[N], int stride) {
#pragma nounroll
  for (int it = 0; it < SPIN_MAX; ++it) {
    bool ok = true;
#pragma unroll
    for (int k = 0; k < N; ++k) { u64 x = __hip_atomic_load(g + k * stride, RLX_AGENT); v[k] = (u32)x; ok &= (u32)(x >> 32) == tag; }
    if (__all(ok)) break;
    __builtin_amdgcn_s_sleep(1);
  }
  ORDER();
}

#define STAMPC(n) do {} while (0)
#define STAMP(n) do {} while (0)

#define STAMP4(n) do {} while (0)
#define STAMP1(n) do {} while (0)
DI void ln_scal(float mean, float var, float& mb, float& sd) {
  mb = rb(mean);
  sd = rb(__builtin_amdgcn_sqrtf(rb(rb(fmaxf(var, 0.f)) + 1e-5f)));
}
DI float fma1(float a, float b, float c) { float r = fmaf(a, b, c); asm("" : "+v"(r)); return r; }
typedef __attribute__((ext_vector_type(2))) _Float16 f16x2;
DI u32x4 znorm8(u32x4 x, float r, float mr) {
  u32x4 o;
#pragma unroll
  for (int e = 0; e < 4; ++e) {
    const f32x2 v = {fmaf(bf_lo(x[e]), r, -mr), fmaf(bf_hi(x[e]), r, -mr)};
    o[e] = __builtin_bit_cast(u32, __builtin_convertvector(v, f16x2));
  }
  return o;
}
DI u32 xform2(u32 z_, u32 c_, u32 s_) {
  const f16x2 z = __builtin_bit_cast(f16x2, z_), c = __builtin_bit_cast(f16x2, c_), s = __builtin_bit_cast(f16x2, s_);
  return pk(fma1((float)z[0], (float)c[0], (float)s[0]), fma1((float)z[1], (float)c[1], (float)s[1]));
}
DI u32x4 xform8(u32x4 x, u32x4 c, u32x4 s) { return (u32x4){xform2(x[0], c[0], s[0]), xform2(x[1], c[1], s[1]), xform2(x[2], c[2], s[2]), xform2(x[3], c[3], s[3])}; }
DI u32 pkh(float a, float b) { const f32x2 v = {a, b}; return __builtin_bit_cast(u32, __builtin_convertvector(v, f16x2)); }
DI f32x2 geglu2(f32x2 ag, f32x2 au, float k1, float k3, float i2) {
  const f32x2 a = ag * (ag * ag * k3 + k1);
  const f32x2 d = (f32x2){__builtin_amdgcn_exp2f(a[0]), __builtin_amdgcn_exp2f(a[1])} * i2 + i2;
  return ag * au * (f32x2){__builtin_amdgcn_rcpf(d[0]), __builtin_amdgcn_rcpf(d[1])};
}

__global__ __launch_bounds__(512, 2) void adaln_block(const u16* __restrict__ x, const u16* __restrict__ wqkv, const u16* __restrict__ wo,
                                                      const u16* __restrict__ wg, const u16* __restrict__ wu, const u16* __restrict__ wd,
                                                      const u16* __restrict__ cvec, const u16* __restrict__ wm, u16* __restrict__ out,
                                                      char* __restrict__ ws) {
  __shared__ __attribute__((aligned(1024))) char lds[LDS_BYTES];
  LAS char* L = (LAS char*)lds;
  const u32 lbase = (u32)(uintptr_t)L;
  const int tid = threadIdx.x, lane = tid & 63;
  const int w = __builtin_amdgcn_readfirstlane(tid >> 6), w4 = w & 3;
  const bool mw = w < 4;
  const int b = blockIdx.x, xcd = b & 7, ii = b >> 3;
  const int u1 = 31 - (8 * (xcd >> 1) + (ii >> 2)), q1 = 4 * (xcd & 1) + (ii & 3);
  const int t3 = 2 * xcd + (ii >> 4), hf = (ii >> 3) & 1, cg = ii & 7;
  const int t4 = 8 * (xcd >> 2) + (ii >> 2), j4 = 4 * (xcd & 3) + (ii & 3);
  const int rot1 = (u1 + 2 * q1) & 7;
  const int rot4 = ii >> 2, rot5 = 7 * (ii >> 3);
  const int r32 = lane & 31, hh = lane >> 5, blk = (lane >> 4) & 1, q4 = (lane & 15) >> 2, p4 = lane & 3, l3 = lane >> 3, l7 = lane & 7;
  const __amdgpu_buffer_rsrc_t rs = __builtin_amdgcn_make_buffer_rsrc(ws, 0, WS_BYTES, 0x00020000);
  STAMP(0);

  { const u64 t0_ = __builtin_amdgcn_s_memrealtime();
    {
      const void* bp = w == 0 ? (const void*)x : w == 1 ? (const void*)wqkv : w == 2 ? (const void*)wo : w == 3 ? (const void*)wg : w == 4 ? (const void*)wu : w == 5 ? (const void*)wd : (const void*)wm;
      const u32 bs = w == 0 ? 2097152u : w == 1 ? 1572864u : w == 2 ? 524288u : w < 6 ? 1835008u : 3145728u;
      const u32x4 sr = make_srd(bp, bs);
      const u32 per = bs / 32;
      if (w < 7) for (u32 o = 0; o < per; o += 8192) touch(sr, o + lane * 128 < per ? ii * per + o + lane * 128 : 0x7fffffffu, lbase + L_JUNK);
      else { u64 pc; asm volatile("s_getpc_b64 %0" : "=s"(pc)); touch(make_srd((const void*)pc, 40960), (ii * 10 + (lane < 10 ? lane : 0)) * 128, lbase + L_JUNK);
      }
      WAITV(0);
    }
    while (__builtin_amdgcn_s_memrealtime() - t0_ < 1500) __builtin_amdgcn_s_sleep(4); }
  u32 tag = 0;
  const int orow = t3 * 128 + hf * 64 + 8 * w + l3;
  const int mg = (31 - u1) * 8 + q1;
  const u32x4 srd_q = make_srd(wqkv, 512 * 3072);
  const u32 q8o = l3 * 3072u + ((l7 ^ (((lane >> 4) & 1) << 2)) << 4);
  auto load_wq = [&](int c) {
    const u32 so = ((c + rot1) & 7) * 196608 + w * 1024 + q1 * 128;
#pragma unroll
    for (int e = 0; e < 8; ++e) glds(srd_q, q8o, so + e * 24576, lbase + c * 24576 + w * 8192 + e * 1024);
  };
  u32x4 X[16];
  u32x4 xr;
  if (mw) {
    u32x4 M[16];
    u32 cv = 0;
    if (mg < 48) {
      const GAS char* mb = (const GAS char*)wm + mg * 128 + (128 * w + 16 * l3) * 6144 + l7 * 16;
      cv = ((const GAS u32*)cvec)[64 * w + lane];
#pragma unroll
      for (int n = 0; n < 16; ++n) M[n] = *(const GAS u32x4*)(mb + n * 6144);
    }
    const u32 ep = __hip_atomic_load((const GAS u32*)(ws + WS_EPOCH), RLX_AGENT);
    if (lane < 12) *(LAS u32*)(L + L_DONE + lane * 4) = 0u;
    SB();
    BAR();
    STAMP(1);
    if (mg < 48) {
      {
        const float c0 = bf_lo(cv), c1 = bf_hi(cv);
        *(LAS u32*)(L + L_RED + 1024 + w * 256 + lane * 4) = pk(c0 * __builtin_amdgcn_rcpf(1.0f + __builtin_amdgcn_exp2f(-1.4426950408889634f * c0)),
                                                               c1 * __builtin_amdgcn_rcpf(1.0f + __builtin_amdgcn_exp2f(-1.4426950408889634f * c1)));
      }
      const u32x4 s0 = lds_ld128(L, L_RED + 1024 + w * 256 + l3 * 32), s1 = lds_ld128(L, L_RED + 1024 + w * 256 + l3 * 32 + 16);
      f32x2 a[4] = {};
#pragma unroll
      for (int n = 0; n < 8; ++n) {
        const u32 sp = n < 4 ? s0[n & 3] : s1[n & 3];
#pragma unroll
        for (int e = 0; e < 4; ++e) {
          const u32 r0 = M[2 * n][e], r1 = M[2 * n + 1][e];
          a[e][0] = dot2(__builtin_amdgcn_perm(r1, r0, 0x05040100u), sp, a[e][0]);
          a[e][1] = dot2(__builtin_amdgcn_perm(r1, r0, 0x07060302u), sp, a[e][1]);
        }
      }
      float v[8] = {a[0][0], a[0][1], a[1][0], a[1][1], a[2][0], a[2][1], a[3][0], a[3][1]};
#pragma unroll
      for (int e = 0; e < 8; ++e) {
        v[e] += dpp<0x128>(v[e]);
        auto r1_ = __builtin_amdgcn_permlane16_swap(__float_as_uint(v[e]), __float_as_uint(v[e]), false, false);
        v[e] = __uint_as_float(r1_[0]) + __uint_as_float(r1_[1]);
        auto r2_ = __builtin_amdgcn_permlane32_swap(__float_as_uint(v[e]), __float_as_uint(v[e]), false, false);
        v[e] = __uint_as_float(r2_[0]) + __uint_as_float(r2_[1]);
      }
      if (lane < 8) {
        *(LAS f32x4*)(L + L_RED + w * 256 + lane * 32) = (f32x4){v[0], v[1], v[2], v[3]};
        *(LAS f32x4*)(L + L_RED + w * 256 + lane * 32 + 16) = (f32x4){v[4], v[5], v[6], v[7]};
      }
      lds_post(lbase + L_MODF + w * 4, 1);
      if (w == 3) {
        lds_wait(L, L_MODF, 1);
        float s = 0.f;
#pragma unroll
        for (int k = 0; k < 4; ++k) s += *(LAS float*)(L + L_RED + k * 256 + lane * 4);
        const float o = dpp<0xB1>(s);
        const u32 tg = __builtin_amdgcn_readfirstlane(ep) + 1u;
        const u64 gr = ((u64)tg << 32) | ((lane & 1) ? pk(o, s) : pk(s, o));
        if (mg < 16) {
#pragma unroll
          for (int r = 0; r < MREP / 2 + (MREP & 1); ++r)
            if (2 * r + (lane & 1) < MREP) __hip_atomic_store((GAS u64*)(ws + WS_MOD1) + (2 * r + (lane & 1)) * 512 + 32 * mg + (lane >> 1), gr, RLX_AGENT);
        } else if (!(lane & 1)) __hip_atomic_store((GAS u64*)(ws + WS_MODG) + 32 * mg + (lane >> 1), gr, RLX_AGENT);
        STAMP(28);
      }
    }
    xr = *(const GAS u32x4*)((const GAS char*)x + orow * 1024 + cg * 128 + l7 * 16);
    if (w < 3) {
      tag = __builtin_amdgcn_readfirstlane(ep) + 1u;
      load_wq(0); load_wq(1); load_wq(2);
    } else {
      u64 pc;
      int len;
      asm volatile("s_getpc_b64 %0\n.Ladaln_pf:\n\ts_mov_b32 %1, .Ladaln_end-.Ladaln_pf" : "=s"(pc), "=s"(len));
      touch(make_srd((const void*)pc, len > 0 ? (u32)len : 0u), (ii * CODE_LINES + (lane < CODE_LINES ? lane : 0)) * 128u, lbase + L_JUNK);
      if (lane < 32) lds_st128(L, L_ONES + (lane & 15) * 16 + (lane >> 4) * 1024, (u32x4){0x3f803f80u, 0x3f803f80u, 0x3f803f80u, 0x3f803f80u});
      tag = __builtin_amdgcn_readfirstlane(ep) + 1u;
      if (lane == 0) *(LAS u32*)(L + L_TAG) = tag;
      u32x4 v[4];
#pragma nounroll
      for (int it = 0; it < SPIN_MAX; ++it) {
        bool ok = true;
#pragma unroll
        for (int k = 0; k < 4; ++k) { v[k] = __builtin_amdgcn_raw_buffer_load_b128(rs, lane * 16, WS_MOD1 + (xcd % MREP) * 4096 + k * 1024, 16); ok &= v[k][1] == tag && v[k][3] == tag; }
        if (__all(ok)) break;
        __builtin_amdgcn_s_sleep(2);
      }
#pragma unroll
      for (int k = 0; k < 2; ++k) {
        const u32 o = L_C1 + ((lane + 64 * k) >> 1) * 32 + (lane & 1) * 8;
        *(LAS u32x2*)(L + o + 16) = (u32x2){pkh(bf_lo(v[k][0]), bf_hi(v[k][0])), pkh(bf_lo(v[k][2]), bf_hi(v[k][2]))};
        *(LAS u32x2*)(L + o) = (u32x2){pkh(rb(1.f + bf_lo(v[2 + k][0])), rb(1.f + bf_hi(v[2 + k][0]))), pkh(rb(1.f + bf_lo(v[2 + k][2])), rb(1.f + bf_hi(v[2 + k][2])))};
      }
    }
    STAMP(3);
  } else {
    const __amdgpu_buffer_rsrc_t rx = __builtin_amdgcn_make_buffer_rsrc((void*)x, 0, 2048 * 1024, 0x00020000);
    if (mg < 48) BAR();
#pragma unroll
    for (int c = 0; c < 8; ++c)
#pragma unroll
      for (int g = 0; g < 2; ++g)
        X[2 * c + g] = __builtin_amdgcn_raw_buffer_load_b128(rx, l3 * 1024u + l7 * 16u, (u1 * 64 + 16 * w4 + 8 * g) * 1024 + ((c + rot1) & 7) * 128, 0);
    xr = *(const GAS u32x4*)((const GAS char*)x + orow * 1024 + cg * 128 + l7 * 16);
    SB();
    if (mg >= 48) BAR();
    STAMP(2);
    float sm[2] = {}, sq[2] = {};
#pragma unroll
    for (int c = 0; c < 8; ++c) {
      SB();
#pragma unroll
      for (int e = 0; e < 4; ++e)
#pragma unroll
        for (int g = 0; g < 2; ++g) { sm[g] = dot2(X[2 * c + g][e], 0x3f803f80u, sm[g]); sq[g] = dot2(X[2 * c + g][e], X[2 * c + g][e], sq[g]); }
    }
    float rr[2], mr[2];
#pragma unroll
    for (int g = 0; g < 2; ++g) {
      const float me = sum8(sm[g]) * (1.f / 512.f);
      float mb, sd;
      ln_scal(me, sum8(sq[g]) * (1.f / 512.f) - me * me, mb, sd);
      rr[g] = __builtin_amdgcn_rcpf(sd); mr[g] = mb * rr[g];
    }
#pragma unroll
    for (int n = 0; n < 16; ++n) { X[n] = znorm8(X[n], rr[n & 1], mr[n & 1]); PIN(X[n]); }
    STAMP(3);
  }
  BAR();
  STAMP(4);
  STAMPC(29);

  u32 cso = L_MISC + l7 * 32;
  PIN(cso);
  const u32x4 srd_g = make_srd(wg, 512 * 3584), srd_u = make_srd(wu, 512 * 3584), srd_d = make_srd(wd, 1792 * 1024), srd_o = make_srd(wo, 512 * 1024);

  u32 guo[7];
  const u32x4 srd_3 = w4 < 2 ? srd_g : srd_u;
  auto load_gu = [&](int c, u32 slot) {
    int r_ = rot4; PINS(r_);
    const u32 so = j4 * 224 + ((c + r_) & 7) * (64 * 3584), ld = lbase + slot + w4 * 1024;
#pragma unroll
    for (int n = 0; n < 7; ++n) glds(n < 3 ? srd_g : n == 3 ? srd_3 : srd_u, guo[n], so, ld + n * 4096);
  };
  const u32 w8o = l3 * 1024u + ((l7 ^ (((lane >> 4) & 1) << 2)) << 4);
  const int hp = w4 >> 1, hd = w >> 2, qb = (w ^ hd) & 1;
  bf16x8 qf;
  if (mw) {
    f32x16 aq = {}, ak = {}, av = {};
    u32 ard[4];
    {
      const int row = 32 * qb + r32, f = (row & 7) ^ ((row >> 3) & 1);
#pragma unroll
      for (int s = 0; s < 4; ++s) { ard[s] = L_AR1 + row * 128 + ((((2 * s) | hh) ^ f) << 4); PIN(ard[s]); }
    }
    u32 wl[3];
    wl[0] = (8 * hh + q4) * 128 + ((64 * hp + 32 * blk + 8 * p4) ^ ((q4 >> 1) << 6));
    wl[1] = wl[0] + 48 * KB; wl[2] = wl[0] + 96 * KB;
    PIN(wl[1]); PIN(wl[2]);
    bf16x8 hf_[3], qf_[3], kf_[3], vf_[3];
#define P1O(n) (wl[(((n) >> 2) % 5) >> 1] + ((((n) >> 2) % 5) & 1) * 24576 + ((n) & 3) * 2048)
#define P1H(n) hf_[(n) % 3] = asbf(lds_ld128(L, ard[(n) & 3] + (((n) >> 2) % 3) * 8192))
    if (w < 3) { load_wq(3); load_wq(4); }
    WAITV_BAR(32);
#pragma unroll
    for (int n = 0; n < 2; ++n) {
      P1H(n); kf_[n] = lds_tr(L, P1O(n) + 8192, P1O(n) + 8704); vf_[n] = lds_tr(L, P1O(n) + 16384, P1O(n) + 16896); qf_[n] = lds_tr(L, P1O(n), P1O(n) + 512);
    }
#pragma unroll
    for (int n = 0; n < 32; ++n) {
      const int m = n + 2;
      const bool bnd = (m & 3) == 0;
      SB();
      ak = MFMA(kf_[n % 3], hf_[n % 3], ak);
      if (m < 32 && !bnd) { P1H(m); kf_[m % 3] = lds_tr(L, P1O(m) + 8192, P1O(m) + 8704); }
      SB();
      av = MFMA(hf_[n % 3], vf_[n % 3], av);
      if (m < 32 && bnd) {
        PIN(ak); PIN(av);
        STAMP1(20 + m / 4);
        WAITV_BAR(m <= 16 ? 32 - 2 * m : 0);
        P1H(m); kf_[m % 3] = lds_tr(L, P1O(m) + 8192, P1O(m) + 8704);
      }
      if (m < 32) vf_[m % 3] = lds_tr(L, P1O(m) + 16384, P1O(m) + 16896);
      SB();
      aq = MFMA(qf_[n % 3], hf_[n % 3], aq);
      if (m < 32) qf_[m % 3] = lds_tr(L, P1O(m), P1O(m) + 512);
      PIN(ak); PIN(av); PIN(aq);
    }
    STAMP(5);
    STAMPC(30);
    const u32x4 f0 = pack8<0>(ak), f1 = pack8<8>(ak), f2 = pack8<0>(av), f3 = pack8<8>(av);
    const int kvi = ((u1 >> 1) * 16 + 2 * q1 + hp) * 4 + 2 * (u1 & 1) + qb;
    const u32 o = WS_KV + kvi * 4096 + lane * 16;
    __builtin_amdgcn_raw_buffer_store_b128(f0, rs, o, 0, 16);
    __builtin_amdgcn_raw_buffer_store_b128(f1, rs, o + 1024, 0, 16);
    __builtin_amdgcn_raw_buffer_store_b128(f2, rs, o + 2048, 0, 16);
    __builtin_amdgcn_raw_buffer_store_b128(f3, rs, o + 3072, 0, 16);
    aq *= QSCALE;
    qf = asbf(pack8<0>(aq));
    const u32x4 f5 = pack8<8>(aq);
    BAR();
    const u32 ok = L_KV + hp * 40960 + (8 + qb) * 4096 + lane * 16;
    lds_st128(L, ok, f0); lds_st128(L, ok + 1024, f1); lds_st128(L, ok + 2048, f2); lds_st128(L, ok + 3072, f3);
    lds_st128(L, L_QX + w * 1024 + lane * 16, f5);
    BAR();
    WAITV(0);
    if (lane == 0) __hip_atomic_store(FLAG(WS_FKV, kvi), tag, RLX_AGENT);
  } else {
    tag = __builtin_amdgcn_readfirstlane(*(LAS u32*)(L + L_TAG));
    if (w == 4 && lane == 0) __hip_atomic_store((GAS u32*)(ws + WS_ARR) + b, tag, RLX_AGENT);
    const u32 awE = L_AR1 + (16 * w4 + l3) * 128 + ((l7 ^ l3) << 4), awO = (awE ^ 16u) + 1024;
    u32x4 ca[2], sa[2];
#define CSLOAD(c) do { const u32 o_ = cso + (((c) + rot1) & 7) * 256; ca[(c) & 1] = lds_ld128(L, o_); sa[(c) & 1] = lds_ld128(L, o_ + 16); } while (0)
    CSLOAD(0);
#pragma unroll
    for (int c = 0; c < 8; ++c) {
      if (c < 7) CSLOAD(c + 1);
      SB();
      PIN(X[2 * c]);
      lds_st128(L, awE + (c % 3) * 8192, xform8(X[2 * c], ca[c & 1], sa[c & 1]));
      PIN(X[2 * c + 1]);
      lds_st128(L, awO + (c % 3) * 8192, xform8(X[2 * c + 1], ca[c & 1], sa[c & 1]));
      if (c >= 3 && c <= 5) {
        const u32 so = ((c + 2 + rot1) & 7) * 196608 + q1 * 128;
#pragma unroll
        for (int e = 0; e < 6; ++e) {
          const int pc = w4 * 6 + e;
          glds(srd_q, q8o, so + (pc >> 3) * 1024 + (pc & 7) * 24576, lbase + (c - 3) * 24576 + pc * 1024);
        }
      }
      if (c == 0) BAR();
      if (c >= 2) STAMP1(19 + c);
      if (c >= 2) asm volatile("s_waitcnt vmcnt(%0) lgkmcnt(2)\n\ts_barrier" ::"i"(c == 6 ? 12 : c == 7 ? 6 : 63) : "memory");
    }
    WAITV_BAR(0);
#pragma unroll
    for (int n = 0; n < 7; ++n) {
      const int pc = w4 + 4 * n, up = pc >= 14, e = (pc - 14 * up) * 64 + lane, rho = e / 14, cc = e - 14 * rho;
      guo[n] = (u32)((up ? (rho ^ 4) : rho) * 3584 + cc * 16);
    }
    STAMP(5);
    BAR();
    BAR();
    qf = asbf(lds_ld128(L, L_QX + (w4 ^ 1) * 1024 + lane * 16));

  }
  PIN(xr);
  STAMP(6);
#define LOAD_WO() do { if (!mw && qb) { for (int n = hp * 32; n < hp * 32 + 32; ++n) glds(srd_o, w8o, cg * 128 + n * 8192, lbase + L_WO + n * 1024); } } while (0)
  LOAD_WO();

  {
    const u32 ka = L_KV + hp * 40960 + hd * 1024 + lane * 16;
    const bool vdat = (r32 >> 4) == hd;
    const u32 va = vdat ? L_KV + hp * 40960 + 2048 + lane * 16 : L_ONES + (lane & 15) * 16;
    const u32 vs = vdat ? 4096u : 0u;
    f32x16 cm;
#pragma unroll
    for (int g = 0; g < 16; ++g) cm[g] = (g & 3) + 8 * (g >> 2) + 4 * hh <= r32 ? 0.f : NEGBIG;
    const f32x16 zero = {};
    f32x16 O = {};
    float m = NEGBIG;
    auto rowmax = [&](const f32x16& s, float mm) {
#pragma unroll
      for (int g = 0; g < 16; g += 2) mm = fmaxf(fmaxf(mm, s[g]), s[g + 1]);
      return mm;
    };
    auto ldk = [&](int lb) { return asbf(lds_ld128(L, ka + lb * 4096)); };
#pragma nounroll
    for (int st = 0; st < 2; ++st) {
      int lm = 8 + qb, lo = 8, hi = 8 + qb;
      if (st) {
        STAMP(7);
        if (u1 == 0) break;
        if (!qb) {
          const int g0 = 2 * u1 - 8 + 4 * hd, gl = g0 + (lane & 3);
          wait_flags(FLAG(WS_FKV, gl < 0 ? ((u1 >> 1) * 16 + 2 * q1 + hp) * 4 + 2 * (u1 & 1) : ((gl >> 2) * 16 + 2 * q1 + hp) * 4 + (gl & 3)), tag);
          const u32x4 srd_w = make_srd(ws, WS_BYTES);
#pragma unroll
          for (int k = 0; k < 4; ++k) {
            const int g = g0 + k;
            const u32 so = g < 0 ? 0x7ff00000u : WS_KV + (((g >> 2) * 16 + 2 * q1 + hp) * 4 + (g & 3)) * 4096;
#pragma unroll
            for (int n = 0; n < 4; ++n) glds_sc1(srd_w, lane * 16, so + n * 1024, lbase + L_KV + hp * 40960 + hd * 16384 + (4 * k + n) * 1024);
          }
          WAITV(0);
        }
        const int lmin = 8 - 2 * u1;
#pragma unroll
        for (int g = 0; g < 16; ++g) { float u = lmin > qb ? NEGBIG : NEGBIG - cm[g]; asm("" : "+v"(u)); cm[g] = u; }
        lm = qb; lo = lmin > qb ? lmin : qb + 1; hi = 8;
        BAR();
        STAMP(8);
      }
      bf16x8 kA = ldk(lo), kB = ldk(lo + 1);
      f32x16 sA = MFMA(ldk(lm), qf, cm), sB, sC;
      const bf16x8 k0 = kA;
      float mn = m;
      for (int lb = lo; lb < hi; lb += 2) {
        sB = MFMA(kA, qf, zero);
        sC = MFMA(kB, qf, zero);
        kA = ldk(lb + 2); kB = ldk(lb + 3);
        SB();
        mn = rowmax(sB, mn);
        if (lb + 1 < hi) mn = rowmax(sC, mn);
      }
      mn = rowmax(sA, mn);
      {
        auto r_ = __builtin_amdgcn_permlane32_swap(__float_as_uint(mn), __float_as_uint(mn), false, false);
        mn = fmaxf(__uint_as_float(r_[0]), __uint_as_float(r_[1]));
      }
      u32 vv = va + lm * vs;
      bf16x8 v0 = asbf(lds_ld128(L, vv)), v1 = asbf(lds_ld128(L, vv + 1024));
      f32x16 negm;
#pragma unroll
      for (int g = 0; g < 16; ++g) negm[g] = -mn;
#pragma unroll
      for (int g = 0; g < 16; ++g) { float u = sA[g] - mn; asm("" : "+v"(u)); sA[g] = u; }
      if (st) {
        const float al = __builtin_amdgcn_exp2f(m - mn);
#pragma unroll
        for (int g = 0; g < 16; ++g) { float o = O[g] * al; asm("" : "+v"(o)); O[g] = o; }
      }
      m = mn;
      kA = k0;
      vv = va + lo * vs;
#define P2STEP(CUR, NXT)                                                                      \
  {                                                                                           \
    NXT = MFMA(kA, qf, negm);                                                                 \
    PIN(NXT);                                                                                 \
    kA = ldk(lb + 1);                                                                         \
    const bf16x8 n0_ = asbf(lds_ld128(L, vv)), n1_ = asbf(lds_ld128(L, vv + 1024));           \
    vv += vs;                                                                                 \
    SB();                                                                                     \
    _Pragma("unroll") for (int g = 0; g < 8; ++g) CUR[g] = __builtin_amdgcn_exp2f(CUR[g]);    \
    O = MFMA(v0, asbf(pack8<0>(CUR)), O);                                                     \
    _Pragma("unroll") for (int g = 8; g < 16; ++g) CUR[g] = __builtin_amdgcn_exp2f(CUR[g]);   \
    O = MFMA(v1, asbf(pack8<8>(CUR)), O);                                                     \
    SB();                                                                                     \
    v0 = n0_; v1 = n1_;                                                                       \
  }
      for (int lb = lo;; ++lb) {
        P2STEP(sA, sB);
        if (lb >= hi) break;
        ++lb;
        P2STEP(sB, sA);
        if (lb >= hi) break;
      }
    }
    u32x4 of;
    {
      const float inv = __builtin_amdgcn_rcpf(hd ? O[0] : O[8]);
      float o[8];
#pragma unroll
      for (int g = 0; g < 8; ++g) { o[g] = (hd ? O[8 + g] : O[g]) * inv; asm("" : "+v"(o[g])); }
      of = (u32x4){pk(o[0], o[1]), pk(o[2], o[3]), pk(o[4], o[5]), pk(o[6], o[7])};
    }
    const int oi = (2 * u1 + qb) * 32 + 4 * q1 + 2 * hp + hd;
    __builtin_amdgcn_raw_buffer_store_b128(of, rs, WS_O + oi * 1024 + lane * 16, 0, 16);
    WAITV(0);
    if (lane == 0) __hip_atomic_store(FLAG(WS_FO, oi), tag, RLX_AGENT);
  }
  STAMP(9);
  {
    if (u1 == 0 && q1 == 0 && w == 7) {
#pragma nounroll
      for (int it = 0; it < SPIN_MAX; ++it) {
        bool ok = true;
#pragma unroll
        for (int k = 0; k < 4; ++k) ok &= __hip_atomic_load((const GAS u32*)(ws + WS_ARR) + lane + 64 * k, RLX_AGENT) == tag;
        if (__all(ok)) break;
        __builtin_amdgcn_s_sleep(1);
      }
      if (lane == 0) __hip_atomic_store((GAS u32*)(ws + WS_EPOCH), tag, RLX_AGENT);
    }
  }
  BAR();

  const u32 wt0 = (4 * hh + q4) * 128 + ((32 * blk + 8 * p4) ^ ((q4 >> 1) << 6)), wt1 = wt0 ^ 64u;
  auto red_write = [&](u32 base, u32 S8, const f32x16& a0, const f32x16& a1) {
    u32 ln = lane_id();
    const u32 o = base + ((ln & 31) >> 3) * S8 + (ln & 7) * 256, sw = (ln & 7) ^ (ln >> 5);
#pragma unroll
    for (int g = 0; g < 4; ++g) {
      *(LAS f32x4*)(L + o + (((2 * g) ^ sw) << 4)) = (f32x4){a0[4 * g], a0[4 * g + 1], a0[4 * g + 2], a0[4 * g + 3]};
      *(LAS f32x4*)(L + o + (((2 * g) ^ sw) << 4) + 128) = (f32x4){a1[4 * g], a1[4 * g + 1], a1[4 * g + 2], a1[4 * g + 3]};
    }
  };
  auto red_read = [&](u32 base, u32 S8, u32 SK, f32x4& lo, f32x4& hi) {
    u32 ln = lane_id();
    const u32 o0 = base + w * S8 + (ln >> 3) * 256 + (((2 * (ln & 7)) ^ (ln >> 3)) << 4), o1 = o0 ^ 16u;
    lo = lds_ldf4(L, o0); hi = lds_ldf4(L, o1);
#pragma unroll
    for (int k = 1; k < 4; ++k) { lo += lds_ldf4(L, o0 + k * SK); hi += lds_ldf4(L, o1 + k * SK); }
  };
  u32x4 x1p;
  {
    if (mw) {
      bf16x8 wa[8], wb[8];
#pragma unroll
      for (int i = 0; i < 8; ++i) {
        const u32 o = L_WO + w * 16384 + i * 2048;
        wa[i] = lds_tr(L, o + wt0, o + wt0 + 1024); wb[i] = lds_tr(L, o + wt1, o + wt1 + 1024);
      }
      wait_flags(FLAG(WS_FO, (t3 * 4 + 2 * hf + (l3 & 1)) * 32 + 8 * w + l7), tag);
      STAMP(10);
      u32x4 of0[8], of1[8];
      const u32 oo = WS_O + ((t3 * 4 + 2 * hf) * 32 + 8 * w) * 1024 + lane * 16;
#pragma unroll
      for (int i = 0; i < 8; ++i) {
        of0[i] = __builtin_amdgcn_raw_buffer_load_b128(rs, oo + i * 1024, 0, 16);
        of1[i] = __builtin_amdgcn_raw_buffer_load_b128(rs, oo + 32768 + i * 1024, 0, 16);
      }
      SB();
      f32x16 a00 = {}, a01 = {}, a10 = {}, a11 = {};
#pragma unroll
      for (int i = 0; i < 8; ++i) {
        a00 = MFMA(wa[i], asbf(of0[i]), a00); a01 = MFMA(wb[i], asbf(of0[i]), a01);
        a10 = MFMA(wa[i], asbf(of1[i]), a10); a11 = MFMA(wb[i], asbf(of1[i]), a11);
      }
      STAMP(11);
      red_write(L_WO + w * 16384, 2048, a00, a01);
      red_write(L_WO + w * 16384 + 8192, 2048, a10, a11);
    } else {
      load_gu(0, gu_slot(0));
      u32x4 bv[2];
      {
        u32x4 q[4];
        u32 v[3];
        const GAS u64* g = (const GAS u64*)(ws + WS_MODG);
        const u32 qo = (((r32 & 1) ? 768 : 1024) + rot4 * 32 + (t4 >> 3) * 16 + 4 * hh) * 8;
#pragma nounroll
        for (int it = 0; it < SPIN_MAX; ++it) {
          const u64 x1_ = __hip_atomic_load(g + 1024 + (tid - 256), RLX_AGENT);
          const u64 x2 = __hip_atomic_load(g + (lane < 32 ? 512 : 1248) + cg * 32 + lane, RLX_AGENT);
          bool ok = (u32)(x1_ >> 32) == tag && (u32)(x2 >> 32) == tag;
#pragma unroll
          for (int k = 0; k < 4; ++k) { q[k] = __builtin_amdgcn_raw_buffer_load_b128(rs, qo + (k >> 1) * 64 + (k & 1) * 16, WS_MODG, 16); ok &= q[k][1] == tag && q[k][3] == tag; }
          v[1] = (u32)x1_; v[2] = (u32)x2;
          if (__all(ok)) break;
          __builtin_amdgcn_s_sleep(1);
        }
        *(LAS f32x2*)(L + L_C1B + (tid - 256) * 8) = (f32x2){rb(1.f + bf_lo(v[1])), rb(1.f + bf_hi(v[1]))};
        if (w == 4) *(LAS f32x2*)(L + L_GA + lane * 8) = (f32x2){bf_lo(v[2]), bf_hi(v[2])};
        const float one = (r32 & 1) ? 0.f : 1.f;
#pragma unroll
        for (int s = 0; s < 2; ++s)
          bv[s] = (u32x4){pk(one + bf_lo(q[2 * s][0]), one + bf_hi(q[2 * s][0])), pk(one + bf_lo(q[2 * s][2]), one + bf_hi(q[2 * s][2])),
                          pk(one + bf_lo(q[2 * s + 1][0]), one + bf_hi(q[2 * s + 1][0])), pk(one + bf_lo(q[2 * s + 1][2]), one + bf_hi(q[2 * s + 1][2]))};
      }
      STAMP(10);
      lds_post(lbase + L_LAND + w4 * 4, 1);
      load_gu(1, gu_slot(1));
      lds_wait(L, L_LAND, 1);
      {
        const u32 gl = blk * 14336 + (8 * hh + q4) * 224 + 8 * p4 + w4 * 32 + (t4 >> 3) * 7168 + gu_slot(0);
        const u32 g0 = gl + (blk ? 896u : 0u), g1 = gl + (blk ? 0u : 896u);
        f32x16 a0 = {}, a1 = {};
#pragma unroll
        for (int s = 0; s < 2; ++s) {
          a0 = MFMA(lds_tr(L, g0 + s * 3584, g1 + s * 3584), asbf(bv[s]), a0);
          a1 = MFMA(lds_tr(L, g0 + s * 3584 + 128, g1 + s * 3584 + 128), asbf(bv[s]), a1);
        }
        if (r32 < 2) {
          const u32 o = WS_UV + (j4 * 16 + t4) * 2048 + w4 * 256 + r32 * 128 + hh * 16;
#pragma unroll
          for (int g = 0; g < 4; ++g) {
            __builtin_amdgcn_raw_buffer_store_b128(__builtin_bit_cast(u32x4, (f32x4){a0[4 * g], a0[4 * g + 1], a0[4 * g + 2], a0[4 * g + 3]}), rs, o + g * 32, 0, 16);
            if (w4 < 3) __builtin_amdgcn_raw_buffer_store_b128(__builtin_bit_cast(u32x4, (f32x4){a1[4 * g], a1[4 * g + 1], a1[4 * g + 2], a1[4 * g + 3]}), rs, o + 1024 + g * 32, 0, 16);
          }
        }
      }
      load_gu(2, gu_slot(2));
      STAMP(11);
    }
    BAR();
    f32x4 lo, hi;
    red_read(L_WO, 2048, 16384, lo, hi);
    const f32x4 ga0 = lds_ldf4(L, cso + M_GA), ga1 = lds_ldf4(L, cso + M_GA + 16);
    float v[8];
    v[0] = rb(bf_lo(xr[0]) + rb(ga0[0] * rb(lo[0]))); v[1] = rb(bf_hi(xr[0]) + rb(ga0[1] * rb(lo[1])));
    v[2] = rb(bf_lo(xr[1]) + rb(ga0[2] * rb(lo[2]))); v[3] = rb(bf_hi(xr[1]) + rb(ga0[3] * rb(lo[3])));
    v[4] = rb(bf_lo(xr[2]) + rb(ga1[0] * rb(hi[0]))); v[5] = rb(bf_hi(xr[2]) + rb(ga1[1] * rb(hi[1])));
    v[6] = rb(bf_lo(xr[3]) + rb(ga1[2] * rb(hi[2]))); v[7] = rb(bf_hi(xr[3]) + rb(ga1[3] * rb(hi[3])));
    x1p = (u32x4){pk(v[0], v[1]), pk(v[2], v[3]), pk(v[4], v[5]), pk(v[6], v[7])};
    PIN(x1p);
    {
      const f32x4 c0 = lds_ldf4(L, cso + M_C1B + cg * 256), c1 = lds_ldf4(L, cso + M_C1B + cg * 256 + 16);
      const u32x4 y = {pk(v[0] * c0[0], v[1] * c0[1]), pk(v[2] * c0[2], v[3] * c0[3]), pk(v[4] * c1[0], v[5] * c1[1]), pk(v[6] * c1[2], v[7] * c1[3])};
      __builtin_amdgcn_raw_buffer_store_b128(y, rs, WS_X1 + ((t3 * 4 + 2 * hf + (w >> 2)) * 32 + 4 * cg + (l7 >> 1)) * 1024 + ((l7 & 1) * 32 + 8 * w4 + l3) * 16, 0, 16);
    }
    const float sm = sum8(((v[0] + v[1]) + (v[2] + v[3])) + ((v[4] + v[5]) + (v[6] + v[7])));
    const float mu = sm * (1.f / 64.f);
    float m2 = 0.f;
#pragma unroll
    for (int k = 0; k < 8; ++k) m2 = fmaf(v[k] - mu, v[k] - mu, m2);
    m2 = sum8(m2);
    if (l7 == 0) __hip_atomic_store((GAS u64*)(ws + WS_ST) + orow * 8 + cg, ((u64)__float_as_uint(m2) << 32) | __float_as_uint(sm), RLX_AGENT);
    WAITV(0);
    if (lane == 0) __hip_atomic_store(FLAG(WS_FX, (t3 * 4 + 2 * hf + (w >> 2)) * 32 + cg * 4 + w4), tag, RLX_AGENT);
    if (!mw && lane == 1) __hip_atomic_store(FLAG(WS_FUV, j4 * 64 + t4 * 4 + w4), tag, RLX_AGENT);
  }
  STAMP(12);
  BAR();

  if (mw) {
    u32 ln = lane_id();
    const u32 bk = (ln >> 4) & 1, gl = bk * 14336 + (8 * (ln >> 5) + ((ln & 15) >> 2)) * 224 + 8 * (ln & 3);
    u32 g0 = gl + (bk ? 896u : 0u) + 36 * KB, g1 = gl + (bk ? 0u : 896u) + 36 * KB, g0h = g0 + 56 * KB, g1h = g1 + 56 * KB;
    PIN(g0); PIN(g1); PIN(g0h); PIN(g1h);
    f32x16 ag[7] = {};
    bf16x8 wf_[7];
    u32x4 yf[32];
#define P4S(n) (((n) >> 2) & 3)
#define P4O(n) ((P4S(n) & 1 ? 0 : 28 * KB) + ((n) & 3) * 3584)
#define P4W(n, f) wf_[f] = lds_tr(L, (P4S(n) == 1 || P4S(n) == 2 ? g0h : g0) + P4O(n) + (f) * 32, (P4S(n) == 1 || P4S(n) == 2 ? g1h : g1) + P4O(n) + (f) * 32)
#pragma unroll
    for (int f = 0; f < 7; ++f) P4W(0, f);

    wait_flags(FLAG(WS_FX, (t4 * 4 + w) * 32 + (ln & 31)), tag);
    STAMP(13);
    auto ld_y = [&](int n) { yf[n] = __builtin_amdgcn_raw_buffer_load_b128(rs, ln * 16, WS_X1 + ((t4 * 4 + w) * 32 + ((n + 4 * rot4) & 31)) * 1024, 16); };
#pragma unroll
    for (int n = 0; n < 5; ++n) ld_y(n);
    const u32 a_done = lbase + L_DONE + w * 4;
    u32x4 land;
    static_for<0, 32>([&](auto nc) {
      constexpr int n = nc.value, m = n + 1;
      if constexpr (n + 2 >= 27 && n + 2 < 32) yf[n + 2] = lds_ld128(L, L_YL + (w * 5 + (n + 2 - 27)) * 1024 + ln * 16);
#pragma unroll
      for (int f = 0; f < 7; ++f) {
        SB();
        ag[f] = MFMA(wf_[f], asbf(yf[n]), ag[f]);
        if ((m & 3) == 0 && f == 0) {
          STAMP4(19 + m / 4);
          lds_post(a_done, m / 4);
          if (m >= 12) { PIN(ag[0]); if (!all_ge(land, m / 4)) lds_wait(L, L_LAND, m / 4); }
        }
        if ((m & 3) == 3 && m >= 11 && f == 0) land = *(volatile LAS u32x4*)(L + L_LAND);
        if (m < 32) P4W(m, f);
        if constexpr (n < 11) { if (f == 1 || f == 4) ld_y(5 + 2 * n + (f >> 2)); }
      }
#pragma unroll
      for (int f = 0; f < 7; ++f) PIN(ag[f]);
    });
    const u32x4 bst = lds_ld128(L, L_BST + (32 * w + (ln & 31)) * 16);
    const u32x4 bfr = {ln < 32 ? bst[0] : 0u, ln < 32 ? bst[1] : 0u, 0u, 0u};
#pragma unroll
    for (int f = 0; f < 7; ++f) {
      const u32x2 uv = *(LAS u32x2*)(L + L_UVT + (f * 32 + (ln & 31)) * 8);
      ag[f] = MFMA(asbf((u32x4){uv[0], uv[1], 0u, 0u}), asbf(bfr), ag[f]);
    }
    const float rs_ = __uint_as_float(bst[2]), sd_ = bf_lo(bst[1]);
    const float k1 = -2.0f * 0.7978845608028654f * 1.4426950408889634f * rs_, k3 = k1 * 0.044715f * rs_ * rs_, i2 = sd_ * sd_;
    STAMP(15);
    ln = lane_id();
    const u32 ao = WS_ACT + ((t4 * 4 + w) * 112 + 7 * j4) * 1024 + ln * 16;
#pragma unroll
    for (int f = 0; f < 7; ++f) {
      u32x4 o;
#pragma unroll
      for (int k = 0; k < 4; ++k) { const f32x2 a = geglu2((f32x2){ag[f][2 * k], ag[f][2 * k + 1]}, (f32x2){ag[f][8 + 2 * k], ag[f][9 + 2 * k]}, k1, k3, i2); o[k] = pk(a[0], a[1]); }
      __builtin_amdgcn_raw_buffer_store_b128(o, rs, ao + f * 1024, 0, 16);
    }
    WAITV(0);
    if (ln == 0) __hip_atomic_store(FLAG(WS_FA, (t4 * 4 + w) * 16 + j4), tag, RLX_AGENT);
    STAMP(16);
  } else {
    const u32 lane = lane_id(), r32 = lane & 31, hh = lane >> 5;
    const u32 w8o = (lane >> 3) * 1024u + (((lane & 7) ^ (((lane >> 4) & 1) << 2)) << 4);
    const u32 wt0 = (4 * hh + ((lane & 15) >> 2)) * 128 + ((32 * ((lane >> 4) & 1) + 8 * (lane & 3)) ^ (((lane & 15) >> 3) << 6)), wt1 = wt0 ^ 64u;
    auto load_wd = [&](int i) {
      int ks = rot5; PINS(ks);
      ks += i; ks -= ks >= 28 ? 28 : 0;
      const u32 so = cg * 128 + (w4 * 448 + ks * 16) * 1024, ld = lbase + wd_base(i) + w4 * 2048;
      glds(srd_d, w8o, so, ld);
      glds(srd_d, w8o, so + 8192, ld + 1024);
    };
    const u32 a_land = lbase + L_LAND + w4 * 4;
    bf16x8 wp0[11], wp1[11];
    u32 dC0 = w4 * 2048 + wt0, dC1 = w4 * 2048 + wt1, dB0 = dC0 + 92 * KB, dB1 = dC1 + 92 * KB, dA0 = dC0 + 64 * KB, dA1 = dC1 + 64 * KB;
    PIN(dA0); PIN(dA1); PIN(dB0); PIN(dB1); PIN(dC0); PIN(dC1);
#define WD0(i) (wd_base(i) < 64 * KB ? dC0 + wd_base(i) : wd_base(i) < 92 * KB ? dA0 + (wd_base(i) - 64 * KB) : dB0 + (wd_base(i) - 92 * KB))
#define WD1(i) (wd_base(i) < 64 * KB ? dC1 + wd_base(i) : wd_base(i) < 92 * KB ? dA1 + (wd_base(i) - 64 * KB) : dB1 + (wd_base(i) - 92 * KB))
#define WDTURN(i)                                                                                                                   \
  do {                                                                                                                              \
    _Pragma("unroll") for (int k = (i); k < (i) + 2; ++k) { wp0[k] = lds_tr(L, WD0(k), WD0(k) + 1024); wp1[k] = lds_tr(L, WD1(k), WD1(k) + 1024); } \
    _Pragma("unroll") for (int k = (i); k < (i) + 2; ++k) { PIN(wp0[k]); PIN(wp1[k]); }                                             \
    load_wd((i) + 2); load_wd((i) + 3);                                                                                             \
  } while (0)
    load_gu(3, gu_slot(3));
    load_wd(0); load_wd(1);
    WAITV(4); lds_post(a_land, 3);
    lds_wait(L, L_DONE, 1);
    STAMP(13);
    {
      load_gu(4, gu_slot(4));
      const u32x4 srd_w = make_srd(ws, WS_BYTES);
#pragma unroll
      for (int k = 0; k < 5; ++k) glds_sc1(srd_w, lane * 16, WS_X1 + ((t4 * 4 + w4) * 32 + ((27 + k + 4 * rot4) & 31)) * 1024, lbase + L_YL + (w4 * 5 + k) * 1024);
      STAMP4(21);
      lds_wait(L, L_DONE, 2); load_gu(5, gu_slot(5)); WAITV(12); lds_post(a_land, 4); WDTURN(0); STAMP4(22);
      lds_wait(L, L_DONE, 3); load_gu(6, gu_slot(6)); WAITV(11); lds_post(a_land, 5); STAMP4(23);
      lds_wait(L, L_DONE, 4); load_gu(7, gu_slot(7)); WAITV(7); lds_post(a_land, 6); WDTURN(2); WAITV(4); lds_post(a_land, 7); STAMP4(24);
        wait_flags(FLAG(WS_FUV, j4 * 64 + lane), tag);
        {
          u32x4 pt[16];
          const u32 o = lane < 28 ? (w4 * 28 + lane) * 16 : 0x7fffffffu;
#pragma unroll
          for (int k = 0; k < 16; ++k) pt[k] = __builtin_amdgcn_raw_buffer_load_b128(rs, o, WS_UV + (j4 * 16 + k) * 2048, 16);
          SB();
          f32x4 a = {};
#pragma unroll
          for (int k = 0; k < 16; ++k) {
            const f32x4 t_ = __builtin_bit_cast(f32x4, pt[k]);
#pragma unroll
            for (int e = 0; e < 4; ++e) { float y_ = a[e] + t_[e]; asm("" : "+v"(y_)); a[e] = y_; }
          }
          if (lane < 28) {
            const u32 vi = (w4 * 28 + lane) * 4;
#pragma unroll
            for (int e = 0; e < 4; ++e) {
              const float hi = rb(a[e]);
              *(LAS u32*)(L + L_UVT + ((vi >> 6) * 32 + (vi & 31) + e) * 8 + ((vi >> 5) & 1) * 4) = pk(hi, a[e] - hi);
            }
          }
        }
      WDTURN(4);
      {
        const u32 so = (t4 * 128 + 32 * w4 + r32) * 64 + hh * 32;
        const u32x4 s0 = __builtin_amdgcn_raw_buffer_load_b128(rs, so, WS_ST, 16), s1 = __builtin_amdgcn_raw_buffer_load_b128(rs, so + 16, WS_ST, 16);
        auto xsum = [&](float v) {
          auto r_ = __builtin_amdgcn_permlane32_swap(__float_as_uint(v), __float_as_uint(v), false, false);
          return __uint_as_float(r_[0]) + __uint_as_float(r_[1]);
        };
        const float a0 = __uint_as_float(s0[0]), a1 = __uint_as_float(s0[2]), a2 = __uint_as_float(s1[0]), a3 = __uint_as_float(s1[2]);
        const float me = xsum((a0 + a1) + (a2 + a3)) * (1.f / 512.f);
        const float d0 = a0 * (1.f / 64.f) - me, d1 = a1 * (1.f / 64.f) - me, d2 = a2 * (1.f / 64.f) - me, d3 = a3 * (1.f / 64.f) - me;
        const float m2 = (__uint_as_float(s0[1]) + __uint_as_float(s0[3])) + (__uint_as_float(s1[1]) + __uint_as_float(s1[3]));
        float mb, sd;
        ln_scal(me, xsum(m2 + 64.f * ((d0 * d0 + d1 * d1) + (d2 * d2 + d3 * d3))) * (1.f / 512.f), mb, sd);
        if (lane < 32) lds_st128(L, L_BST + (32 * w4 + lane) * 16, (u32x4){pk(-mb, -mb), pk(sd, sd), __float_as_uint(__builtin_amdgcn_rcpf(sd)), 0u});
      }
      lds_post(a_land, 8);
      STAMP4(25);
      lds_wait(L, L_DONE, 5); load_wd(8); load_wd(9); load_wd(10);
      lds_wait(L, L_DONE, 6); load_wd(11); load_wd(12); load_wd(13);
      lds_wait(L, L_DONE, 7); load_wd(14); load_wd(15); load_wd(16); load_wd(17);
      lds_wait(L, L_DONE, 8);
    }
    STAMP(15);
#pragma unroll
    for (int i = 18; i < 23; ++i) load_wd(i);
    WAITV(24);
#pragma unroll
    for (int k = 6; k < 11; ++k) { wp0[k] = lds_tr(L, WD0(k), WD0(k) + 1024); wp1[k] = lds_tr(L, WD1(k), WD1(k) + 1024); }
#pragma unroll
    for (int k = 6; k < 11; ++k) { PIN(wp0[k]); PIN(wp1[k]); }
#pragma unroll
    for (int i = 23; i < 28; ++i) load_wd(i);
    STAMP(16);
    wait_flags(FLAG(WS_FA, (t3 * 4 + 2 * hf + ((lane >> 2) & 1)) * 16 + 4 * w4 + (lane & 3)), tag);
    STAMP(17);
    const u32 ao = WS_ACT + ((t3 * 4 + 2 * hf) * 112 + 28 * w4) * 1024 + lane * 16;
    u32x4 af0[28], af1[28];
    auto ld_act = [&](int i) {
      int ks = i + rot5; ks -= ks >= 28 ? 28 : 0;
      af0[i] = __builtin_amdgcn_raw_buffer_load_b128(rs, ao, ks * 1024, 16);
      af1[i] = __builtin_amdgcn_raw_buffer_load_b128(rs, ao + 112 * 1024, ks * 1024, 16);
    };
#pragma unroll
    for (int i = 0; i < 5; ++i) ld_act(i);
    f32x16 a00 = {}, a01 = {}, a10 = {}, a11 = {};
    static_for<0, 28>([&](auto ic) {
      constexpr int i = ic.value;
      SB();
      a00 = MFMA(wp0[i % 11], asbf(af0[i]), a00);
      a01 = MFMA(wp1[i % 11], asbf(af0[i]), a01);
      SB();
      a10 = MFMA(wp0[i % 11], asbf(af1[i]), a10);
      if constexpr (i < 6) { ld_act(5 + 2 * i); ld_act(6 + 2 * i); }
      else if constexpr (i + 11 < 28) ld_act(i + 11);
      SB();
      a11 = MFMA(wp1[i % 11], asbf(af1[i]), a11);
      if constexpr (i >= 8 && i + 3 < 28) {
        wp0[(i + 3) % 11] = lds_tr(L, WD0(i + 3), WD0(i + 3) + 1024);
        wp1[(i + 3) % 11] = lds_tr(L, WD1(i + 3), WD1(i + 3) + 1024);
      }
      PIN(a00); PIN(a01); PIN(a10); PIN(a11);
    });
    STAMP(18);
    red_write(20 * KB + w4 * 2048, 8192, a00, a01);
    red_write(92 * KB + w4 * 2048, 8192, a10, a11);
  }
  BAR();
  {
    f32x4 lo, hi;
    red_read(mw ? 20 * KB : 60 * KB, 8192, 2048, lo, hi);
    u32 ln = lane_id();
    const f32x4 gb0 = lds_ldf4(L, L_MISC + M_GB + (ln & 7) * 32), gb1 = lds_ldf4(L, L_MISC + M_GB + (ln & 7) * 32 + 16);
    u32x4 o;
    o[0] = pk(bf_lo(x1p[0]) + rb(gb0[0] * rb(lo[0])), bf_hi(x1p[0]) + rb(gb0[1] * rb(lo[1])));
    o[1] = pk(bf_lo(x1p[1]) + rb(gb0[2] * rb(lo[2])), bf_hi(x1p[1]) + rb(gb0[3] * rb(lo[3])));
    o[2] = pk(bf_lo(x1p[2]) + rb(gb1[0] * rb(hi[0])), bf_hi(x1p[2]) + rb(gb1[1] * rb(hi[1])));
    o[3] = pk(bf_lo(x1p[3]) + rb(gb1[2] * rb(hi[2])), bf_hi(x1p[3]) + rb(gb1[3] * rb(hi[3])));
    *(GAS u32x4*)((GAS char*)out + (t3 * 128 + hf * 64 + 8 * w) * 1024 + cg * 128 + (ln >> 3) * 1024 + (ln & 7) * 16) = o;
  }
  STAMP(19);
  asm volatile(".Ladaln_end:");
}

extern "C" void kernel_launch(void* const* d_in, const int* in_sizes, int n_in, void* d_out, int out_size, void* d_ws, size_t ws_size,
                              hipStream_t stream) {
  adaln_block<<<256, 512, 0, stream>>>((const u16*)d_in[0], (const u16*)d_in[1], (const u16*)d_in[2], (const u16*)d_in[4], (const u16*)d_in[5],
                                       (const u16*)d_in[6], (const u16*)d_in[7], (const u16*)d_in[8], (u16*)d_out, (char*)d_ws);
}
```

```cpp
#include <hip/hip_runtime.h>
#include <stdint.h>

typedef unsigned short u16;
typedef unsigned int u32;
typedef unsigned long long u64;
typedef __attribute__((ext_vector_type(8))) __bf16 bf16x8;
typedef __attribute__((ext_vector_type(2))) __bf16 bf16x2;
typedef __attribute__((ext_vector_type(4))) short s16x4;
typedef __attribute__((ext_vector_type(8))) short s16x8;
typedef __attribute__((ext_vector_type(16))) float f32x16;
typedef __attribute__((ext_vector_type(4))) float f32x4;
typedef __attribute__((ext_vector_type(2))) float f32x2;
typedef __attribute__((ext_vector_type(4))) u32 u32x4;
typedef __attribute__((ext_vector_type(2))) u32 u32x2;
#define LAS __attribute__((address_space(3)))
#define GAS __attribute__((address_space(1)))
#define DI __device__ __forceinline__
#define RLX_AGENT __ATOMIC_RELAXED, __HIP_MEMORY_SCOPE_AGENT

constexpr u32 WS_EPOCH = 0;
constexpr u32 WS_ARR = 1024;
constexpr u32 WS_MODG = 4096;
constexpr u32 WS_ST = 65536;
constexpr u32 WS_UV = 196608;
constexpr u32 WS_KV = 1048576;
constexpr u32 WS_O = WS_KV + 4194304;
constexpr u32 WS_X1 = WS_O + 2097152;
constexpr u32 WS_ACT = WS_X1 + 2097152;
#define WO_AT 0
#define FS 16
#define MREP 8
constexpr u32 WS_FKV = WS_ACT + 7340032;
constexpr u32 WS_FO = WS_FKV + 1024 * 4 * FS;
constexpr u32 WS_FX = WS_FO + 2048 * 4 * FS;
constexpr u32 WS_FA = WS_FX + 2048 * 4 * FS;
constexpr u32 WS_FUV = WS_FA + 1024 * 4 * FS;
constexpr u32 WS_MOD1 = WS_FUV + 1024 * 4 * FS;
constexpr u32 WS_DBG = 20971520;
constexpr u32 WS_BYTES = WS_DBG + 1048576;
static_assert(WS_MOD1 + 8 * 4096 <= WS_DBG && WS_UV + 256 * 2048 <= WS_KV, "workspace map");
#define FLAG(base, i) ((GAS u32*)(ws + (base)) + (i) * FS)

constexpr int SPIN_MAX = 1 << 17;
constexpr u32 KB = 1024;
constexpr u32 L_WO = 0;
constexpr u32 L_KV = 64 * KB;
constexpr u32 L_QX = 144 * KB;
constexpr u32 L_AR1 = 120 * KB;
constexpr u32 L_YL = 0;
constexpr u32 L_MISC = 148 * KB;
constexpr u32 L_C1 = L_MISC, L_SH = L_MISC + 2048, L_C1B = L_MISC + 4096, L_SHB = L_MISC + 6144, M_C1B = 4096;
constexpr u32 L_UVT = L_C1, L_BST = L_SH;
constexpr u32 L_ONES = L_MISC + 8192;
constexpr u32 L_GA = L_ONES + 256, M_GA = 8192 + 256, M_GB = 8192 + 512;
constexpr u32 L_DONE = L_ONES + 784, L_LAND = L_ONES + 800, L_MODF = L_ONES + 816;
constexpr u32 L_TAG = L_ONES + 768, L_JUNK = L_ONES + 1536;
constexpr u32 L_RED = L_MISC + 10240;
constexpr u32 LDS_BYTES = 163840;
DI constexpr u32 gu_slot(int c) { return (c & 3) == 3 ? 36 * KB : 64 * KB + (c & 3) * 28 * KB; }
DI constexpr u32 wd_base(int i) {
  return i < 8 ? 20 * KB + (i & 1) * 8192u : i < 11 ? 64 * KB + (i - 8) * 8192u : i < 18 ? 92 * KB + (i - 11) * 8192u : i < 21 ? 36 * KB + (i - 18) * 8192u
       : i < 23 ? (i - 21) * 8192u : i < 25 ? 20 * KB + (i - 23) * 8192u : 64 * KB + (i - 25) * 8192u;
}
constexpr u32 CODE_LINES = 12;

constexpr float NEGBIG = -1e30f;
constexpr float QSCALE = 0.25f * 1.4426950408889634f;

template <int I> struct IC { static constexpr int value = I; };
template <int I, int N, class F> DI void static_for(F&& f) { if constexpr (I < N) { f(IC<I>{}); static_for<I + 1, N>(f); } }
DI u32 lane_id() { u32 r; asm volatile("v_mbcnt_lo_u32_b32 %0, -1, 0\n\tv_mbcnt_hi_u32_b32 %0, -1, %0" : "=v"(r)); return r; }
DI float bf_lo(u32 v) { return __uint_as_float(v << 16); }
DI float bf_hi(u32 v) { return __uint_as_float(v & 0xffff0000u); }
DI u32 pk(float a, float b) { f32x2 v = {a, b}; return __builtin_bit_cast(u32, __builtin_convertvector(v, bf16x2)); }
DI float rb(float a) { return bf_lo(pk(a, a)); }
template <int O> DI u32x4 pack8(const f32x16& a) { return (u32x4){pk(a[O], a[O + 1]), pk(a[O + 2], a[O + 3]), pk(a[O + 4], a[O + 5]), pk(a[O + 6], a[O + 7])}; }
DI bf16x8 asbf(u32x4 v) { return __builtin_bit_cast(bf16x8, v); }
#define MFMA(a, b, c) __builtin_amdgcn_mfma_f32_32x32x16_bf16((a), (b), (c), 0, 0, 0)
template <int CTRL, int RM = 0xf> DI float dpp(float v) { return __builtin_bit_cast(float, __builtin_amdgcn_update_dpp(0, __builtin_bit_cast(int, v), CTRL, RM, 0xf, true)); }
DI float sum8(float v) { v += dpp<0xB1>(v); v += dpp<0x4E>(v); v += dpp<0x141>(v); return v; }
DI float dot2(u32 a, u32 b, float c) { return __builtin_amdgcn_fdot2_f32_bf16(__builtin_bit_cast(bf16x2, a), __builtin_bit_cast(bf16x2, b), c, false); }

DI u32x4 lds_ld128(LAS char* L, u32 off) { return *(LAS u32x4*)(L + off); }
DI f32x4 lds_ldf4(LAS char* L, u32 off) { return *(LAS f32x4*)(L + off); }
DI void lds_st128(LAS char* L, u32 off, u32x4 v) { *(LAS u32x4*)(L + off) = v; }
DI bf16x8 lds_tr(LAS char* L, u32 off0, u32 off1) {
  s16x4 a = __builtin_amdgcn_ds_read_tr16_b64_v4i16((LAS s16x4*)(L + off0));
  s16x4 b = __builtin_amdgcn_ds_read_tr16_b64_v4i16((LAS s16x4*)(L + off1));
  return __builtin_bit_cast(bf16x8, __builtin_shufflevector(a, b, 0, 1, 2, 3, 4, 5, 6, 7));
}
DI u32x4 make_srd(const void* p, u32 bytes) { const u64 a = (u64)p; return (u32x4){(u32)a, (u32)(a >> 32), bytes, 0x00020000u}; }
DI void glds(const u32x4& srd, u32 voff, u32 soff, u32 ldsaddr) {
  asm volatile("s_mov_b32 m0, %3\n\ts_nop 0\n\tbuffer_load_dwordx4 %0, %1, %2 offen lds" ::"v"(voff), "s"(srd), "s"(soff), "s"(ldsaddr) : "memory");
}
DI void glds_sc1(const u32x4& srd, u32 voff, u32 soff, u32 ldsaddr) {
  asm volatile("s_mov_b32 m0, %3\n\ts_nop 0\n\tbuffer_load_dwordx4 %0, %1, %2 offen sc1 lds" ::"v"(voff), "s"(srd), "s"(soff), "s"(ldsaddr) : "memory");
}
DI void touch(const u32x4& srd, u32 voff, u32 ldsaddr) {
  asm volatile("s_mov_b32 m0, %2\n\ts_nop 0\n\tbuffer_load_dword %0, %1, 0 offen lds" ::"v"(voff), "s"(srd), "s"(ldsaddr) : "memory");
}
DI void lds_post(u32 addr, u32 val) {
  asm volatile("s_mov_b64 exec, 1\n\tds_write_b32 %0, %1\n\ts_mov_b64 exec, -1" ::"v"(addr), "v"(val) : "memory");
}
DI bool all_ge(u32x4 v, u32 k) { return __builtin_amdgcn_readfirstlane(min(min(v[0], v[1]), min(v[2], v[3]))) >= k; }
DI void lds_wait(LAS char* L, u32 off, u32 k) {
#pragma nounroll
  for (int it = 0; it < (SPIN_MAX << 4); ++it) {
    if (all_ge(*(volatile LAS u32x4*)(L + off), k)) break;
    __builtin_amdgcn_s_sleep(3);
  }
  asm volatile("" ::: "memory");
}
#define BAR() asm volatile("s_waitcnt lgkmcnt(0)\n\ts_barrier" ::: "memory")
#define WAITV_BAR(N) asm volatile("s_waitcnt vmcnt(%0) lgkmcnt(0)\n\ts_barrier" ::"i"(N) : "memory")
#define WAITV(N) asm volatile("s_waitcnt vmcnt(%0)" ::"i"(N) : "memory")
#define SB() __builtin_amdgcn_sched_barrier(0)
#define PIN(x) asm volatile("" : "+v"(x))
#define PINS(x) asm volatile("" : "+s"(x))
#define ORDER() __builtin_amdgcn_fence(__ATOMIC_ACQUIRE, "wavefront")

DI void wait_flags(const GAS u32* p, u32 tag) {
  u32 v = tag + 1u;
#pragma nounroll
  for (int it = 0; it < SPIN_MAX; ++it) {
    if (v != tag) v = __hip_atomic_load(p, RLX_AGENT);
    if (__all(v == tag)) break;
    __builtin_amdgcn_s_sleep(4);
  }
  ORDER();
}
template <int N> DI void gather_gran(const GAS u64* g, u32 tag, u32 (&v)[N], int stride) {
#pragma nounroll
  for (int it = 0; it < SPIN_MAX; ++it) {
    bool ok = true;
#pragma unroll
    for (int k = 0; k < N; ++k) { u64 x = __hip_atomic_load(g + k * stride, RLX_AGENT); v[k] = (u32)x; ok &= (u32)(x >> 32) == tag; }
    if (__all(ok)) break;
    __builtin_amdgcn_s_sleep(1);
  }
  ORDER();
}

#define STAMPC(n) do {} while (0)
#define STAMP(n) do {} while (0)

#define STAMP4(n) do {} while (0)
#define STAMP1(n) do {} while (0)
DI void ln_scal(float mean, float var, float& mb, float& sd) {
  mb = rb(mean);
  sd = rb(__builtin_amdgcn_sqrtf(rb(rb(fmaxf(var, 0.f)) + 1e-5f)));
}
DI float fma1(float a, float b, float c) { float r = fmaf(a, b, c); asm("" : "+v"(r)); return r; }
typedef __attribute__((ext_vector_type(2))) _Float16 f16x2;
DI u32x4 znorm8(u32x4 x, float r, float mr) {
  u32x4 o;
#pragma unroll
  for (int e = 0; e < 4; ++e) {
    const f32x2 v = {fmaf(bf_lo(x[e]), r, -mr), fmaf(bf_hi(x[e]), r, -mr)};
    o[e] = __builtin_bit_cast(u32, __builtin_convertvector(v, f16x2));
  }
  return o;
}
DI u32 xform2(u32 z_, u32 c_, u32 s_) {
  const f16x2 z = __builtin_bit_cast(f16x2, z_), c = __builtin_bit_cast(f16x2, c_), s = __builtin_bit_cast(f16x2, s_);
  return pk(fma1((float)z[0], (float)c[0], (float)s[0]), fma1((float)z[1], (float)c[1], (float)s[1]));
}
DI u32x4 xform8(u32x4 x, u32x4 c, u32x4 s) { return (u32x4){xform2(x[0], c[0], s[0]), xform2(x[1], c[1], s[1]), xform2(x[2], c[2], s[2]), xform2(x[3], c[3], s[3])}; }
DI u32 pkh(float a, float b) { const f32x2 v = {a, b}; return __builtin_bit_cast(u32, __builtin_convertvector(v, f16x2)); }
DI f32x2 geglu2(f32x2 ag, f32x2 au, float k1, float k3, float i2) {
  const f32x2 a = ag * (ag * ag * k3 + k1);
  const f32x2 d = (f32x2){__builtin_amdgcn_exp2f(a[0]), __builtin_amdgcn_exp2f(a[1])} * i2 + i2;
  return ag * au * (f32x2){__builtin_amdgcn_rcpf(d[0]), __builtin_amdgcn_rcpf(d[1])};
}

__global__ __launch_bounds__(512, 2) void adaln_block(const u16* __restrict__ x, const u16* __restrict__ wqkv, const u16* __restrict__ wo,
                                                      const u16* __restrict__ wg, const u16* __restrict__ wu, const u16* __restrict__ wd,
                                                      const u16* __restrict__ cvec, const u16* __restrict__ wm, u16* __restrict__ out,
                                                      char* __restrict__ ws) {
  __shared__ __attribute__((aligned(1024))) char lds[LDS_BYTES];
  LAS char* L = (LAS char*)lds;
  const u32 lbase = (u32)(uintptr_t)L;
  const int tid = threadIdx.x, lane = tid & 63;
  const int w = __builtin_amdgcn_readfirstlane(tid >> 6), w4 = w & 3;
  const bool mw = w < 4;
  const int b = blockIdx.x, xcd = b & 7, ii = b >> 3;
  if (w == 7) {
    u64 pc;
    int len;
    asm volatile("s_getpc_b64 %0\n.Ladaln_pf:\n\ts_mov_b32 %1, .Ladaln_end-.Ladaln_pf" : "=s"(pc), "=s"(len));
    touch(make_srd((const void*)pc, len > 0 ? (u32)len : 0u), ((lane < CODE_LINES ? lane : 0) * 32 + ii) * 128u, lbase + L_JUNK);
  }
  const int u1 = 31 - (8 * (xcd >> 1) + (ii >> 2)), q1 = 4 * (xcd & 1) + (ii & 3);
  const int t3 = 2 * xcd + (ii >> 4), hf = (ii >> 3) & 1, cg = ii & 7;
  const int t4 = 8 * (xcd >> 2) + (ii >> 2), j4 = 4 * (xcd & 3) + (ii & 3);
  const int rot1 = (u1 + 2 * q1) & 7;
  const int rot4 = ii >> 2, rot5 = 7 * (ii >> 3);
  const int r32 = lane & 31, hh = lane >> 5, blk = (lane >> 4) & 1, q4 = (lane & 15) >> 2, p4 = lane & 3, l3 = lane >> 3, l7 = lane & 7;
  const __amdgpu_buffer_rsrc_t rs = __builtin_amdgcn_make_buffer_rsrc(ws, 0, WS_BYTES, 0x00020000);
  STAMP(0);

  u32 tag = 0;
  const int orow = t3 * 128 + hf * 64 + 8 * w + l3;
  const int mg = b;
  const u32x4 srd_q = make_srd(wqkv, 512 * 3072);
  const u32 q8o = l3 * 3072u + ((l7 ^ (((lane >> 4) & 1) << 2)) << 4);
  auto load_wq = [&](int c) {
    const u32 so = ((c + rot1) & 7) * 196608 + w * 1024 + q1 * 128;
#pragma unroll
    for (int e = 0; e < 8; ++e) glds(srd_q, q8o, so + e * 24576, lbase + (c % 5) * 24576 + w * 8192 + e * 1024);
  };
  u32x4 X[16];
  u32x4 xr;
  const int hp = w4 >> 1, hd = w >> 2, qb = (w ^ hd) & 1;
  bf16x8 qf_[15], kf_[15], vf_[15];
  u32 wl[3];
#define P1O(n) (wl[(((n) >> 2) % 5) >> 1] + ((((n) >> 2) % 5) & 1) * 24576 + ((n) & 3) * 2048)
#define P1I(n) ((n) < 12 ? (n) : 12 + (n) % 3)
  u32 cso = L_MISC + l7 * 32;
  PIN(cso);
  const u32x4 srd_g = make_srd(wg, 512 * 3584), srd_u = make_srd(wu, 512 * 3584), srd_d = make_srd(wd, 1792 * 1024), srd_o = make_srd(wo, 512 * 1024);

  u32 guo[7];
  const u32x4 srd_3 = w4 < 2 ? srd_g : srd_u;
  auto load_gu = [&](int c, u32 slot) {
    int r_ = rot4; PINS(r_);
    const u32 so = j4 * 224 + ((c + r_) & 7) * (64 * 3584), ld = lbase + slot + w4 * 1024;
#pragma unroll
    for (int n = 0; n < 7; ++n) glds(n < 3 ? srd_g : n == 3 ? srd_3 : srd_u, guo[n], so, ld + n * 4096);
  };
  const u32 w8o = l3 * 1024u + ((l7 ^ (((lane >> 4) & 1) << 2)) << 4);
  bf16x8 qf;
  if (mw) {
    u32x4 M[16];
    u32 cv = 0;
    if (mg < 48) {
      const GAS char* mb = (const GAS char*)wm + mg * 128 + (128 * w + 16 * l3) * 6144 + l7 * 16;
      cv = ((const GAS u32*)cvec)[64 * w + lane];
#pragma unroll
      for (int n = 0; n < 16; ++n) M[n] = *(const GAS u32x4*)(mb + n * 6144);
    }
    const u32 ep = __hip_atomic_load((const GAS u32*)(ws + WS_EPOCH), RLX_AGENT);
    if (lane < 12) *(LAS u32*)(L + L_DONE + lane * 4) = 0u;
    SB();
    BAR();
    STAMP(1);
    if (mg < 48) {
      {
        const float c0 = bf_lo(cv), c1 = bf_hi(cv);
        *(LAS u32*)(L + L_RED + 1024 + w * 256 + lane * 4) = pk(c0 * __builtin_amdgcn_rcpf(1.0f + __builtin_amdgcn_exp2f(-1.4426950408889634f * c0)),
                                                               c1 * __builtin_amdgcn_rcpf(1.0f + __builtin_amdgcn_exp2f(-1.4426950408889634f * c1)));
      }
      const u32x4 s0 = lds_ld128(L, L_RED + 1024 + w * 256 + l3 * 32), s1 = lds_ld128(L, L_RED + 1024 + w * 256 + l3 * 32 + 16);
      f32x2 a[4] = {};
#pragma unroll
      for (int n = 0; n < 8; ++n) {
        const u32 sp = n < 4 ? s0[n & 3] : s1[n & 3];
#pragma unroll
        for (int e = 0; e < 4; ++e) {
          const u32 r0 = M[2 * n][e], r1 = M[2 * n + 1][e];
          a[e][0] = dot2(__builtin_amdgcn_perm(r1, r0, 0x05040100u), sp, a[e][0]);
          a[e][1] = dot2(__builtin_amdgcn_perm(r1, r0, 0x07060302u), sp, a[e][1]);
        }
      }
      float v[8] = {a[0][0], a[0][1], a[1][0], a[1][1], a[2][0], a[2][1], a[3][0], a[3][1]};
#pragma unroll
      for (int e = 0; e < 8; ++e) {
        v[e] += dpp<0x128>(v[e]);
        auto r1_ = __builtin_amdgcn_permlane16_swap(__float_as_uint(v[e]), __float_as_uint(v[e]), false, false);
        v[e] = __uint_as_float(r1_[0]) + __uint_as_float(r1_[1]);
        auto r2_ = __builtin_amdgcn_permlane32_swap(__float_as_uint(v[e]), __float_as_uint(v[e]), false, false);
        v[e] = __uint_as_float(r2_[0]) + __uint_as_float(r2_[1]);
      }
      if (lane < 8) {
        *(LAS f32x4*)(L + L_RED + w * 256 + lane * 32) = (f32x4){v[0], v[1], v[2], v[3]};
        *(LAS f32x4*)(L + L_RED + w * 256 + lane * 32 + 16) = (f32x4){v[4], v[5], v[6], v[7]};
      }
      lds_post(lbase + L_MODF + w * 4, 1);
      if (w == 3) {
        lds_wait(L, L_MODF, 1);
        float s = 0.f;
#pragma unroll
        for (int k = 0; k < 4; ++k) s += *(LAS float*)(L + L_RED + k * 256 + lane * 4);
        const float o = dpp<0xB1>(s);
        const u32 tg = __builtin_amdgcn_readfirstlane(ep) + 1u;
        const u64 gr = ((u64)tg << 32) | ((lane & 1) ? pk(o, s) : pk(s, o));
        if (mg < 16) {
#pragma unroll
          for (int r = 0; r < MREP / 2 + (MREP & 1); ++r)
            if (2 * r + (lane & 1) < MREP) __hip_atomic_store((GAS u64*)(ws + WS_MOD1) + (2 * r + (lane & 1)) * 512 + 32 * mg + (lane >> 1), gr, RLX_AGENT);
        } else if (!(lane & 1)) __hip_atomic_store((GAS u64*)(ws + WS_MODG) + 32 * mg + (lane >> 1), gr, RLX_AGENT);
        STAMP(28);
      }
    }
    xr = *(const GAS u32x4*)((const GAS char*)x + orow * 1024 + cg * 128 + l7 * 16);
    tag = __builtin_amdgcn_readfirstlane(ep) + 1u;
    if (w < 3) {
      load_wq(0); load_wq(1); load_wq(2); load_wq(3); load_wq(4);
      WAITV(16);
    }
    lds_post(lbase + L_MODF + w * 4, 2);
    wl[0] = (8 * hh + q4) * 128 + ((64 * hp + 32 * blk + 8 * p4) ^ ((q4 >> 1) << 6));
    wl[1] = wl[0] + 48 * KB; wl[2] = wl[0] + 96 * KB;
    PIN(wl[1]); PIN(wl[2]);
    lds_wait(L, L_MODF, 2);
#pragma unroll
    for (int n = 0; n < 12; ++n) { kf_[n] = lds_tr(L, P1O(n) + 8192, P1O(n) + 8704); vf_[n] = lds_tr(L, P1O(n) + 16384, P1O(n) + 16896); qf_[n] = lds_tr(L, P1O(n), P1O(n) + 512); }
    lds_post(lbase + L_MODF + w * 4, 3);
#pragma unroll
    for (int n = 0; n < 12; ++n) { PIN(kf_[n]); PIN(vf_[n]); PIN(qf_[n]); }
    if (w < 3) {
      lds_wait(L, L_MODF, 3);
      load_wq(5); load_wq(6); load_wq(7);
    } else {
      if (lane < 32) lds_st128(L, L_ONES + (lane & 15) * 16 + (lane >> 4) * 1024, (u32x4){0x3f803f80u, 0x3f803f80u, 0x3f803f80u, 0x3f803f80u});
      if (lane == 0) *(LAS u32*)(L + L_TAG) = tag;
      u32x4 v[4];
#pragma nounroll
      for (int it = 0; it < SPIN_MAX; ++it) {
        bool ok = true;
#pragma unroll
        for (int k = 0; k < 4; ++k) { v[k] = __builtin_amdgcn_raw_buffer_load_b128(rs, lane * 16, WS_MOD1 + (xcd % MREP) * 4096 + k * 1024, 16); ok &= v[k][1] == tag && v[k][3] == tag; }
        if (__all(ok)) break;
        __builtin_amdgcn_s_sleep(2);
      }
#pragma unroll
      for (int k = 0; k < 2; ++k) {
        const u32 o = L_C1 + ((lane + 64 * k) >> 1) * 32 + (lane & 1) * 8;
        *(LAS u32x2*)(L + o + 16) = (u32x2){pkh(bf_lo(v[k][0]), bf_hi(v[k][0])), pkh(bf_lo(v[k][2]), bf_hi(v[k][2]))};
        *(LAS u32x2*)(L + o) = (u32x2){pkh(rb(1.f + bf_lo(v[2 + k][0])), rb(1.f + bf_hi(v[2 + k][0]))), pkh(rb(1.f + bf_lo(v[2 + k][2])), rb(1.f + bf_hi(v[2 + k][2])))};
      }
    }
    STAMP(3);
    BAR();
    STAMP(4);
    STAMPC(29);
    f32x16 aq = {}, ak = {}, av = {};
    u32 ard[4];
    {
      const int row = 32 * qb + r32, f = (row & 7) ^ ((row >> 3) & 1);
#pragma unroll
      for (int s = 0; s < 4; ++s) { ard[s] = L_AR1 + row * 128 + ((((2 * s) | hh) ^ f) << 4); PIN(ard[s]); }
    }
    bf16x8 hf_[3];
#define P1H(n) hf_[(n) % 3] = asbf(lds_ld128(L, ard[(n) & 3] + (((n) >> 2) % 3) * 8192))
    BAR();
    P1H(0); P1H(1);
    static_for<0, 32>([&](auto nc) {
      constexpr int n = nc.value, m = n + 2;
      constexpr bool bnd = (m & 3) == 0, rd = m >= 12 && m < 32;
      SB();
      ak = MFMA(kf_[P1I(n)], hf_[n % 3], ak);
      if constexpr (m < 32 && !bnd) { P1H(m); if constexpr (rd) kf_[P1I(m)] = lds_tr(L, P1O(m) + 8192, P1O(m) + 8704); }
      SB();
      av = MFMA(hf_[n % 3], vf_[P1I(n)], av);
      if constexpr (m < 32 && bnd) {
        PIN(ak); PIN(av);
        STAMP1(20 + m / 4);
        WAITV_BAR(m >= 12 ? 56 - 2 * m : 63);
        P1H(m);
        if constexpr (rd) kf_[P1I(m)] = lds_tr(L, P1O(m) + 8192, P1O(m) + 8704);
      }
      if constexpr (rd) vf_[P1I(m)] = lds_tr(L, P1O(m) + 16384, P1O(m) + 16896);
      SB();
      aq = MFMA(qf_[P1I(n)], hf_[n % 3], aq);
      if constexpr (rd) qf_[P1I(m)] = lds_tr(L, P1O(m), P1O(m) + 512);
      PIN(ak); PIN(av); PIN(aq);
    });
    STAMP(5);
    STAMPC(30);
    const u32x4 f0 = pack8<0>(ak), f1 = pack8<8>(ak), f2 = pack8<0>(av), f3 = pack8<8>(av);
    const int kvi = ((u1 >> 1) * 16 + 2 * q1 + hp) * 4 + 2 * (u1 & 1) + qb;
    const u32 o = WS_KV + kvi * 4096 + lane * 16;
    __builtin_amdgcn_raw_buffer_store_b128(f0, rs, o, 0, 16);
    __builtin_amdgcn_raw_buffer_store_b128(f1, rs, o + 1024, 0, 16);
    __builtin_amdgcn_raw_buffer_store_b128(f2, rs, o + 2048, 0, 16);
    __builtin_amdgcn_raw_buffer_store_b128(f3, rs, o + 3072, 0, 16);
    aq *= QSCALE;
    qf = asbf(pack8<0>(aq));
    const u32x4 f5 = pack8<8>(aq);
    BAR();
    const u32 ok = L_KV + hp * 40960 + (8 + qb) * 4096 + lane * 16;
    lds_st128(L, ok, f0); lds_st128(L, ok + 1024, f1); lds_st128(L, ok + 2048, f2); lds_st128(L, ok + 3072, f3);
    lds_st128(L, L_QX + w * 1024 + lane * 16, f5);
    BAR();
    WAITV(0);
    if (lane == 0) __hip_atomic_store(FLAG(WS_FKV, kvi), tag, RLX_AGENT);
  } else {
    const __amdgpu_buffer_rsrc_t rx = __builtin_amdgcn_make_buffer_rsrc((void*)x, 0, 2048 * 1024, 0x00020000);
    if (mg < 48) BAR();
#pragma unroll
    for (int c = 0; c < 8; ++c)
#pragma unroll
      for (int g = 0; g < 2; ++g)
        X[2 * c + g] = __builtin_amdgcn_raw_buffer_load_b128(rx, l3 * 1024u + l7 * 16u, (u1 * 64 + 16 * w4 + 8 * g) * 1024 + ((c + rot1) & 7) * 128, 0);
    xr = *(const GAS u32x4*)((const GAS char*)x + orow * 1024 + cg * 128 + l7 * 16);
    SB();
    if (mg >= 48) BAR();
    STAMP(2);
    float sm[2] = {}, sq[2] = {};
#pragma unroll
    for (int c = 0; c < 8; ++c) {
      SB();
#pragma unroll
      for (int e = 0; e < 4; ++e)
#pragma unroll
        for (int g = 0; g < 2; ++g) { sm[g] = dot2(X[2 * c + g][e], 0x3f803f80u, sm[g]); sq[g] = dot2(X[2 * c + g][e], X[2 * c + g][e], sq[g]); }
    }
    float rr[2], mr[2];
#pragma unroll
    for (int g = 0; g < 2; ++g) {
      const float me = sum8(sm[g]) * (1.f / 512.f);
      float mb, sd;
      ln_scal(me, sum8(sq[g]) * (1.f / 512.f) - me * me, mb, sd);
      rr[g] = __builtin_amdgcn_rcpf(sd); mr[g] = mb * rr[g];
    }
#pragma unroll
    for (int n = 0; n < 16; ++n) { X[n] = znorm8(X[n], rr[n & 1], mr[n & 1]); PIN(X[n]); }
    if (w4 < 3) {
      const u32x4 srd_a = make_srd(w4 == 0 ? wo : w4 == 1 ? wg : wu, w4 == 0 ? 512 * 1024 : 512 * 3584);
      const int e1 = 64 + lane;
      const u32 ta0 = w4 ? (16 * ii + lane / 7) * 3584 + (xcd & 3) * 896 + (lane % 7) * 128 : ii * 16384 + lane * 128;
      const u32 ta1 = w4 ? (lane < 48 ? (16 * ii + e1 / 7) * 3584 + (xcd & 3) * 896 + (e1 % 7) * 128 : 0x7fffffffu) : ta0 + 8192;
      touch(srd_a, ta0, lbase + L_JUNK);
      touch(srd_a, ta1, lbase + L_JUNK);
    }
    STAMP(3);
    BAR();
    STAMP(4);
    STAMPC(29);
    tag = __builtin_amdgcn_readfirstlane(*(LAS u32*)(L + L_TAG));
    if (w == 4 && lane == 0) __hip_atomic_store((GAS u32*)(ws + WS_ARR) + b, tag, RLX_AGENT);
    const u32 awE = L_AR1 + (16 * w4 + l3) * 128 + ((l7 ^ l3) << 4), awO = (awE ^ 16u) + 1024;
    u32x4 ca[2], sa[2];
#define CSLOAD(c) do { const u32 o_ = cso + (((c) + rot1) & 7) * 256; ca[(c) & 1] = lds_ld128(L, o_); sa[(c) & 1] = lds_ld128(L, o_ + 16); } while (0)
    CSLOAD(0);
#pragma unroll
    for (int c = 0; c < 8; ++c) {
      if (c < 7) CSLOAD(c + 1);
      SB();
      PIN(X[2 * c]);
      lds_st128(L, awE + (c % 3) * 8192, xform8(X[2 * c], ca[c & 1], sa[c & 1]));
      PIN(X[2 * c + 1]);
      lds_st128(L, awO + (c % 3) * 8192, xform8(X[2 * c + 1], ca[c & 1], sa[c & 1]));
      if (c == 0) BAR();
      if (c >= 2) STAMP1(19 + c);
      if (c >= 2) asm volatile("s_waitcnt lgkmcnt(2)\n\ts_barrier" ::: "memory");
    }
    BAR();
#pragma unroll
    for (int n = 0; n < 7; ++n) {
      const int pc = w4 + 4 * n, up = pc >= 14, e = (pc - 14 * up) * 64 + lane, rho = e / 14, cc = e - 14 * rho;
      guo[n] = (u32)((up ? (rho ^ 4) : rho) * 3584 + cc * 16);
    }
    STAMP(5);
    BAR();
    BAR();
    qf = asbf(lds_ld128(L, L_QX + (w4 ^ 1) * 1024 + lane * 16));

  }
  PIN(xr);
  STAMP(6);
#define LOAD_WO() do { if (!mw && qb) { for (int n = hp * 32; n < hp * 32 + 32; ++n) glds(srd_o, w8o, cg * 128 + n * 8192, lbase + L_WO + n * 1024); } } while (0)
  LOAD_WO();

  {
    const u32 ka = L_KV + hp * 40960 + hd * 1024 + lane * 16;
    const bool vdat = (r32 >> 4) == hd;
    const u32 va = vdat ? L_KV + hp * 40960 + 2048 + lane * 16 : L_ONES + (lane & 15) * 16;
    const u32 vs = vdat ? 4096u : 0u;
    f32x16 cm;
#pragma unroll
    for (int g = 0; g < 16; ++g) cm[g] = (g & 3) + 8 * (g >> 2) + 4 * hh <= r32 ? 0.f : NEGBIG;
    const f32x16 zero = {};
    f32x16 O = {};
    float m = NEGBIG;
    auto rowmax = [&](const f32x16& s, float mm) {
#pragma unroll
      for (int g = 0; g < 16; g += 2) mm = fmaxf(fmaxf(mm, s[g]), s[g + 1]);
      return mm;
    };
    auto ldk = [&](int lb) { return asbf(lds_ld128(L, ka + lb * 4096)); };
#pragma nounroll
    for (int st = 0; st < 2; ++st) {
      int lm = 8 + qb, lo = 8, hi = 8 + qb;
      if (st) {
        STAMP(7);
        if (u1 == 0) break;
        if (!qb) {
          const int g0 = 2 * u1 - 8 + 4 * hd, gl = g0 + (lane & 3);
          wait_flags(FLAG(WS_FKV, gl < 0 ? ((u1 >> 1) * 16 + 2 * q1 + hp) * 4 + 2 * (u1 & 1) : ((gl >> 2) * 16 + 2 * q1 + hp) * 4 + (gl & 3)), tag);
          const u32x4 srd_w = make_srd(ws, WS_BYTES);
#pragma unroll
          for (int k = 0; k < 4; ++k) {
            const int g = g0 + k;
            const u32 so = g < 0 ? 0x7ff00000u : WS_KV + (((g >> 2) * 16 + 2 * q1 + hp) * 4 + (g & 3)) * 4096;
#pragma unroll
            for (int n = 0; n < 4; ++n) glds_sc1(srd_w, lane * 16, so + n * 1024, lbase + L_KV + hp * 40960 + hd * 16384 + (4 * k + n) * 1024);
          }
          WAITV(0);
        }
        const int lmin = 8 - 2 * u1;
#pragma unroll
        for (int g = 0; g < 16; ++g) { float u = lmin > qb ? NEGBIG : NEGBIG - cm[g]; asm("" : "+v"(u)); cm[g] = u; }
        lm = qb; lo = lmin > qb ? lmin : qb + 1; hi = 8;
        BAR();
        STAMP(8);
      }
      bf16x8 kA = ldk(lo), kB = ldk(lo + 1);
      f32x16 sA = MFMA(ldk(lm), qf, cm), sB, sC;
      const bf16x8 k0 = kA;
      float mn = m;
      for (int lb = lo; lb < hi; lb += 2) {
        sB = MFMA(kA, qf, zero);
        sC = MFMA(kB, qf, zero);
        kA = ldk(lb + 2); kB = ldk(lb + 3);
        SB();
        mn = rowmax(sB, mn);
        if (lb + 1 < hi) mn = rowmax(sC, mn);
      }
      mn = rowmax(sA, mn);
      {
        auto r_ = __builtin_amdgcn_permlane32_swap(__float_as_uint(mn), __float_as_uint(mn), false, false);
        mn = fmaxf(__uint_as_float(r_[0]), __uint_as_float(r_[1]));
      }
      u32 vv = va + lm * vs;
      bf16x8 v0 = asbf(lds_ld128(L, vv)), v1 = asbf(lds_ld128(L, vv + 1024));
      f32x16 negm;
#pragma unroll
      for (int g = 0; g < 16; ++g) negm[g] = -mn;
#pragma unroll
      for (int g = 0; g < 16; ++g) { float u = sA[g] - mn; asm("" : "+v"(u)); sA[g] = u; }
      if (st) {
        const float al = __builtin_amdgcn_exp2f(m - mn);
#pragma unroll
        for (int g = 0; g < 16; ++g) { float o = O[g] * al; asm("" : "+v"(o)); O[g] = o; }
      }
      m = mn;
      kA = k0;
      vv = va + lo * vs;
#define P2STEP(CUR, NXT)                                                                      \
  {                                                                                           \
    NXT = MFMA(kA, qf, negm);                                                                 \
    PIN(NXT);                                                                                 \
    kA = ldk(lb + 1);                                                                         \
    const bf16x8 n0_ = asbf(lds_ld128(L, vv)), n1_ = asbf(lds_ld128(L, vv + 1024));           \
    vv += vs;                                                                                 \
    SB();                                                                                     \
    _Pragma("unroll") for (int g = 0; g < 8; ++g) CUR[g] = __builtin_amdgcn_exp2f(CUR[g]);    \
    O = MFMA(v0, asbf(pack8<0>(CUR)), O);                                                     \
    _Pragma("unroll") for (int g = 8; g < 16; ++g) CUR[g] = __builtin_amdgcn_exp2f(CUR[g]);   \
    O = MFMA(v1, asbf(pack8<8>(CUR)), O);                                                     \
    SB();                                                                                     \
    v0 = n0_; v1 = n1_;                                                                       \
  }
      for (int lb = lo;; ++lb) {
        P2STEP(sA, sB);
        if (lb >= hi) break;
        ++lb;
        P2STEP(sB, sA);
        if (lb >= hi) break;
      }
    }
    u32x4 of;
    {
      const float inv = __builtin_amdgcn_rcpf(hd ? O[0] : O[8]);
      float o[8];
#pragma unroll
      for (int g = 0; g < 8; ++g) { o[g] = (hd ? O[8 + g] : O[g]) * inv; asm("" : "+v"(o[g])); }
      of = (u32x4){pk(o[0], o[1]), pk(o[2], o[3]), pk(o[4], o[5]), pk(o[6], o[7])};
    }
    const int oi = (2 * u1 + qb) * 32 + 4 * q1 + 2 * hp + hd;
    __builtin_amdgcn_raw_buffer_store_b128(of, rs, WS_O + oi * 1024 + lane * 16, 0, 16);
    WAITV(0);
    if (lane == 0) __hip_atomic_store(FLAG(WS_FO, oi), tag, RLX_AGENT);
  }
  STAMP(9);
  {
    if (u1 == 0 && q1 == 0 && w == 7) {
#pragma nounroll
      for (int it = 0; it < SPIN_MAX; ++it) {
        bool ok = true;
#pragma unroll
        for (int k = 0; k < 4; ++k) ok &= __hip_atomic_load((const GAS u32*)(ws + WS_ARR) + lane + 64 * k, RLX_AGENT) == tag;
        if (__all(ok)) break;
        __builtin_amdgcn_s_sleep(1);
      }
      if (lane == 0) __hip_atomic_store((GAS u32*)(ws + WS_EPOCH), tag, RLX_AGENT);
    }
  }
  BAR();

  const u32 wt0 = (4 * hh + q4) * 128 + ((32 * blk + 8 * p4) ^ ((q4 >> 1) << 6)), wt1 = wt0 ^ 64u;
  auto red_write = [&](u32 base, u32 S8, const f32x16& a0, const f32x16& a1) {
    u32 ln = lane_id();
    const u32 o = base + ((ln & 31) >> 3) * S8 + (ln & 7) * 256, sw = (ln & 7) ^ (ln >> 5);
#pragma unroll
    for (int g = 0; g < 4; ++g) {
      *(LAS f32x4*)(L + o + (((2 * g) ^ sw) << 4)) = (f32x4){a0[4 * g], a0[4 * g + 1], a0[4 * g + 2], a0[4 * g + 3]};
      *(LAS f32x4*)(L + o + (((2 * g) ^ sw) << 4) + 128) = (f32x4){a1[4 * g], a1[4 * g + 1], a1[4 * g + 2], a1[4 * g + 3]};
    }
  };
  auto red_read = [&](u32 base, u32 S8, u32 SK, f32x4& lo, f32x4& hi) {
    u32 ln = lane_id();
    const u32 o0 = base + w * S8 + (ln >> 3) * 256 + (((2 * (ln & 7)) ^ (ln >> 3)) << 4), o1 = o0 ^ 16u;
    lo = lds_ldf4(L, o0); hi = lds_ldf4(L, o1);
#pragma unroll
    for (int k = 1; k < 4; ++k) { lo += lds_ldf4(L, o0 + k * SK); hi += lds_ldf4(L, o1 + k * SK); }
  };
  u32x4 x1p;
  {
    if (mw) {
      bf16x8 wa[8], wb[8];
#pragma unroll
      for (int i = 0; i < 8; ++i) {
        const u32 o = L_WO + w * 16384 + i * 2048;
        wa[i] = lds_tr(L, o + wt0, o + wt0 + 1024); wb[i] = lds_tr(L, o + wt1, o + wt1 + 1024);
      }
      wait_flags(FLAG(WS_FO, (t3 * 4 + 2 * hf + (l3 & 1)) * 32 + 8 * w + l7), tag);
      STAMP(10);
      u32x4 of0[8], of1[8];
      const u32 oo = WS_O + ((t3 * 4 + 2 * hf) * 32 + 8 * w) * 1024 + lane * 16;
#pragma unroll
      for (int i = 0; i < 8; ++i) {
        of0[i] = __builtin_amdgcn_raw_buffer_load_b128(rs, oo + i * 1024, 0, 16);
        of1[i] = __builtin_amdgcn_raw_buffer_load_b128(rs, oo + 32768 + i * 1024, 0, 16);
      }
      SB();
      f32x16 a00 = {}, a01 = {}, a10 = {}, a11 = {};
#pragma unroll
      for (int i = 0; i < 8; ++i) {
        a00 = MFMA(wa[i], asbf(of0[i]), a00); a01 = MFMA(wb[i], asbf(of0[i]), a01);
        a10 = MFMA(wa[i], asbf(of1[i]), a10); a11 = MFMA(wb[i], asbf(of1[i]), a11);
      }
      STAMP(11);
      red_write(L_WO + w * 16384, 2048, a00, a01);
      red_write(L_WO + w * 16384 + 8192, 2048, a10, a11);
    } else {
      load_gu(0, gu_slot(0));
      u32x4 bv[2];
      {
        u32x4 q[4];
        u32 v[3];
        const GAS u64* g = (const GAS u64*)(ws + WS_MODG);
        const u32 qo = (((r32 & 1) ? 768 : 1024) + rot4 * 32 + (t4 >> 3) * 16 + 4 * hh) * 8;
#pragma nounroll
        for (int it = 0; it < SPIN_MAX; ++it) {
          const u64 x1_ = __hip_atomic_load(g + 1024 + (tid - 256), RLX_AGENT);
          const u64 x2 = __hip_atomic_load(g + (lane < 32 ? 512 : 1248) + cg * 32 + lane, RLX_AGENT);
          bool ok = (u32)(x1_ >> 32) == tag && (u32)(x2 >> 32) == tag;
#pragma unroll
          for (int k = 0; k < 4; ++k) { q[k] = __builtin_amdgcn_raw_buffer_load_b128(rs, qo + (k >> 1) * 64 + (k & 1) * 16, WS_MODG, 16); ok &= q[k][1] == tag && q[k][3] == tag; }
          v[1] = (u32)x1_; v[2] = (u32)x2;
          if (__all(ok)) break;
          __builtin_amdgcn_s_sleep(1);
        }
        *(LAS f32x2*)(L + L_C1B + (tid - 256) * 8) = (f32x2){rb(1.f + bf_lo(v[1])), rb(1.f + bf_hi(v[1]))};
        if (w == 4) *(LAS f32x2*)(L + L_GA + lane * 8) = (f32x2){bf_lo(v[2]), bf_hi(v[2])};
        const float one = (r32 & 1) ? 0.f : 1.f;
#pragma unroll
        for (int s = 0; s < 2; ++s)
          bv[s] = (u32x4){pk(one + bf_lo(q[2 * s][0]), one + bf_hi(q[2 * s][0])), pk(one + bf_lo(q[2 * s][2]), one + bf_hi(q[2 * s][2])),
                          pk(one + bf_lo(q[2 * s + 1][0]), one + bf_hi(q[2 * s + 1][0])), pk(one + bf_lo(q[2 * s + 1][2]), one + bf_hi(q[2 * s + 1][2]))};
      }
      STAMP(10);
      lds_post(lbase + L_LAND + w4 * 4, 1);
      load_gu(1, gu_slot(1));
      lds_wait(L, L_LAND, 1);
      {
        const u32 gl = blk * 14336 + (8 * hh + q4) * 224 + 8 * p4 + w4 * 32 + (t4 >> 3) * 7168 + gu_slot(0);
        const u32 g0 = gl + (blk ? 896u : 0u), g1 = gl + (blk ? 0u : 896u);
        f32x16 a0 = {}, a1 = {};
#pragma unroll
        for (int s = 0; s < 2; ++s) {
          a0 = MFMA(lds_tr(L, g0 + s * 3584, g1 + s * 3584), asbf(bv[s]), a0);
          a1 = MFMA(lds_tr(L, g0 + s * 3584 + 128, g1 + s * 3584 + 128), asbf(bv[s]), a1);
        }
        if (r32 < 2) {
          const u32 o = WS_UV + (j4 * 16 + t4) * 2048 + w4 * 256 + r32 * 128 + hh * 16;
#pragma unroll
          for (int g = 0; g < 4; ++g) {
            __builtin_amdgcn_raw_buffer_store_b128(__builtin_bit_cast(u32x4, (f32x4){a0[4 * g], a0[4 * g + 1], a0[4 * g + 2], a0[4 * g + 3]}), rs, o + g * 32, 0, 16);
            if (w4 < 3) __builtin_amdgcn_raw_buffer_store_b128(__builtin_bit_cast(u32x4, (f32x4){a1[4 * g], a1[4 * g + 1], a1[4 * g + 2], a1[4 * g + 3]}), rs, o + 1024 + g * 32, 0, 16);
          }
        }
      }
      load_gu(2, gu_slot(2));
      STAMP(11);
    }
    BAR();
    f32x4 lo, hi;
    red_read(L_WO, 2048, 16384, lo, hi);
    const f32x4 ga0 = lds_ldf4(L, cso + M_GA), ga1 = lds_ldf4(L, cso + M_GA + 16);
    float v[8];
    v[0] = rb(bf_lo(xr[0]) + rb(ga0[0] * rb(lo[0]))); v[1] = rb(bf_hi(xr[0]) + rb(ga0[1] * rb(lo[1])));
    v[2] = rb(bf_lo(xr[1]) + rb(ga0[2] * rb(lo[2]))); v[3] = rb(bf_hi(xr[1]) + rb(ga0[3] * rb(lo[3])));
    v[4] = rb(bf_lo(xr[2]) + rb(ga1[0] * rb(hi[0]))); v[5] = rb(bf_hi(xr[2]) + rb(ga1[1] * rb(hi[1])));
    v[6] = rb(bf_lo(xr[3]) + rb(ga1[2] * rb(hi[2]))); v[7] = rb(bf_hi(xr[3]) + rb(ga1[3] * rb(hi[3])));
    x1p = (u32x4){pk(v[0], v[1]), pk(v[2], v[3]), pk(v[4], v[5]), pk(v[6], v[7])};
    PIN(x1p);
    {
      const f32x4 c0 = lds_ldf4(L, cso + M_C1B + cg * 256), c1 = lds_ldf4(L, cso + M_C1B + cg * 256 + 16);
      const u32x4 y = {pk(v[0] * c0[0], v[1] * c0[1]), pk(v[2] * c0[2], v[3] * c0[3]), pk(v[4] * c1[0], v[5] * c1[1]), pk(v[6] * c1[2], v[7] * c1[3])};
      __builtin_amdgcn_raw_buffer_store_b128(y, rs, WS_X1 + ((t3 * 4 + 2 * hf + (w >> 2)) * 32 + 4 * cg + (l7 >> 1)) * 1024 + ((l7 & 1) * 32 + 8 * w4 + l3) * 16, 0, 16);
    }
    const float sm = sum8(((v[0] + v[1]) + (v[2] + v[3])) + ((v[4] + v[5]) + (v[6] + v[7])));
    const float mu = sm * (1.f / 64.f);
    float m2 = 0.f;
#pragma unroll
    for (int k = 0; k < 8; ++k) m2 = fmaf(v[k] - mu, v[k] - mu, m2);
    m2 = sum8(m2);
    if (l7 == 0) __hip_atomic_store((GAS u64*)(ws + WS_ST) + orow * 8 + cg, ((u64)__float_as_uint(m2) << 32) | __float_as_uint(sm), RLX_AGENT);
    WAITV(0);
    if (lane == 0) __hip_atomic_store(FLAG(WS_FX, (t3 * 4 + 2 * hf + (w >> 2)) * 32 + cg * 4 + w4), tag, RLX_AGENT);
    if (!mw && lane == 1) __hip_atomic_store(FLAG(WS_FUV, j4 * 64 + t4 * 4 + w4), tag, RLX_AGENT);
  }
  STAMP(12);
  BAR();

  if (mw) {
    u32 ln = lane_id();
    const u32 bk = (ln >> 4) & 1, gl = bk * 14336 + (8 * (ln >> 5) + ((ln & 15) >> 2)) * 224 + 8 * (ln & 3);
    u32 g0 = gl + (bk ? 896u : 0u) + 36 * KB, g1 = gl + (bk ? 0u : 896u) + 36 * KB, g0h = g0 + 56 * KB, g1h = g1 + 56 * KB;
    PIN(g0); PIN(g1); PIN(g0h); PIN(g1h);
    f32x16 ag[7] = {};
    bf16x8 wf_[7];
    u32x4 yf[32];
#define P4S(n) (((n) >> 2) & 3)
#define P4O(n) ((P4S(n) & 1 ? 0 : 28 * KB) + ((n) & 3) * 3584)
#define P4W(n, f) wf_[f] = lds_tr(L, (P4S(n) == 1 || P4S(n) == 2 ? g0h : g0) + P4O(n) + (f) * 32, (P4S(n) == 1 || P4S(n) == 2 ? g1h : g1) + P4O(n) + (f) * 32)
#pragma unroll
    for (int f = 0; f < 7; ++f) P4W(0, f);

    wait_flags(FLAG(WS_FX, (t4 * 4 + w) * 32 + (ln & 31)), tag);
    STAMP(13);
    auto ld_y = [&](int n) { yf[n] = __builtin_amdgcn_raw_buffer_load_b128(rs, ln * 16, WS_X1 + ((t4 * 4 + w) * 32 + ((n + 4 * rot4) & 31)) * 1024, 16); };
#pragma unroll
    for (int n = 0; n < 5; ++n) ld_y(n);
    const u32 a_done = lbase + L_DONE + w * 4;
    u32x4 land;
    static_for<0, 32>([&](auto nc) {
      constexpr int n = nc.value, m = n + 1;
      if constexpr (n + 2 >= 27 && n + 2 < 32) yf[n + 2] = lds_ld128(L, L_YL + (w * 5 + (n + 2 - 27)) * 1024 + ln * 16);
#pragma unroll
      for (int f = 0; f < 7; ++f) {
        SB();
        ag[f] = MFMA(wf_[f], asbf(yf[n]), ag[f]);
        if ((m & 3) == 0 && f == 0) {
          STAMP4(19 + m / 4);
          lds_post(a_done, m / 4);
          if (m >= 12) { PIN(ag[0]); if (!all_ge(land, m / 4)) lds_wait(L, L_LAND, m / 4); }
        }
        if ((m & 3) == 3 && m >= 11 && f == 0) land = *(volatile LAS u32x4*)(L + L_LAND);
        if (m < 32) P4W(m, f);
        if constexpr (n < 11) { if (f == 1 || f == 4) ld_y(5 + 2 * n + (f >> 2)); }
      }
#pragma unroll
      for (int f = 0; f < 7; ++f) PIN(ag[f]);
    });
    const u32x4 bst = lds_ld128(L, L_BST + (32 * w + (ln & 31)) * 16);
    const u32x4 bfr = {ln < 32 ? bst[0] : 0u, ln < 32 ? bst[1] : 0u, 0u, 0u};
#pragma unroll
    for (int f = 0; f < 7; ++f) {
      const u32x2 uv = *(LAS u32x2*)(L + L_UVT + (f * 32 + (ln & 31)) * 8);
      ag[f] = MFMA(asbf((u32x4){uv[0], uv[1], 0u, 0u}), asbf(bfr), ag[f]);
    }
    const float rs_ = __uint_as_float(bst[2]), sd_ = bf_lo(bst[1]);
    const float k1 = -2.0f * 0.7978845608028654f * 1.4426950408889634f * rs_, k3 = k1 * 0.044715f * rs_ * rs_, i2 = sd_ * sd_;
    STAMP(15);
    ln = lane_id();
    const u32 ao = WS_ACT + ((t4 * 4 + w) * 112 + 7 * j4) * 1024 + ln * 16;
#pragma unroll
    for (int f = 0; f < 7; ++f) {
      u32x4 o;
#pragma unroll
      for (int k = 0; k < 4; ++k) { const f32x2 a = geglu2((f32x2){ag[f][2 * k], ag[f][2 * k + 1]}, (f32x2){ag[f][8 + 2 * k], ag[f][9 + 2 * k]}, k1, k3, i2); o[k] = pk(a[0], a[1]); }
      __builtin_amdgcn_raw_buffer_store_b128(o, rs, ao + f * 1024, 0, 16);
    }
    WAITV(0);
    if (ln == 0) __hip_atomic_store(FLAG(WS_FA, (t4 * 4 + w) * 16 + j4), tag, RLX_AGENT);
    STAMP(16);
  } else {
    const u32 lane = lane_id(), r32 = lane & 31, hh = lane >> 5;
    const u32 w8o = (lane >> 3) * 1024u + (((lane & 7) ^ (((lane >> 4) & 1) << 2)) << 4);
    const u32 wt0 = (4 * hh + ((lane & 15) >> 2)) * 128 + ((32 * ((lane >> 4) & 1) + 8 * (lane & 3)) ^ (((lane & 15) >> 3) << 6)), wt1 = wt0 ^ 64u;
    auto load_wd = [&](int i) {
      int ks = rot5; PINS(ks);
      ks += i; ks -= ks >= 28 ? 28 : 0;
      const u32 so = cg * 128 + (w4 * 448 + ks * 16) * 1024, ld = lbase + wd_base(i) + w4 * 2048;
      glds(srd_d, w8o, so, ld);
      glds(srd_d, w8o, so + 8192, ld + 1024);
    };
    const u32 a_land = lbase + L_LAND + w4 * 4;
    bf16x8 wp0[11], wp1[11];
    u32 dC0 = w4 * 2048 + wt0, dC1 = w4 * 2048 + wt1, dB0 = dC0 + 92 * KB, dB1 = dC1 + 92 * KB, dA0 = dC0 + 64 * KB, dA1 = dC1 + 64 * KB;
    PIN(dA0); PIN(dA1); PIN(dB0); PIN(dB1); PIN(dC0); PIN(dC1);
#define WD0(i) (wd_base(i) < 64 * KB ? dC0 + wd_base(i) : wd_base(i) < 92 * KB ? dA0 + (wd_base(i) - 64 * KB) : dB0 + (wd_base(i) - 92 * KB))
#define WD1(i) (wd_base(i) < 64 * KB ? dC1 + wd_base(i) : wd_base(i) < 92 * KB ? dA1 + (wd_base(i) - 64 * KB) : dB1 + (wd_base(i) - 92 * KB))
#define WDTURN(i)                                                                                                                   \
  do {                                                                                                                              \
    _Pragma("unroll") for (int k = (i); k < (i) + 2; ++k) { wp0[k] = lds_tr(L, WD0(k), WD0(k) + 1024); wp1[k] = lds_tr(L, WD1(k), WD1(k) + 1024); } \
    _Pragma("unroll") for (int k = (i); k < (i) + 2; ++k) { PIN(wp0[k]); PIN(wp1[k]); }                                             \
    load_wd((i) + 2); load_wd((i) + 3);                                                                                             \
  } while (0)
    load_gu(3, gu_slot(3));
    load_wd(0); load_wd(1);
    WAITV(4); lds_post(a_land, 3);
    lds_wait(L, L_DONE, 1);
    STAMP(13);
    {
      load_gu(4, gu_slot(4));
      const u32x4 srd_w = make_srd(ws, WS_BYTES);
#pragma unroll
      for (int k = 0; k < 5; ++k) glds_sc1(srd_w, lane * 16, WS_X1 + ((t4 * 4 + w4) * 32 + ((27 + k + 4 * rot4) & 31)) * 1024, lbase + L_YL + (w4 * 5 + k) * 1024);
      STAMP4(21);
      lds_wait(L, L_DONE, 2); load_gu(5, gu_slot(5)); WAITV(12); lds_post(a_land, 4); WDTURN(0); STAMP4(22);
      lds_wait(L, L_DONE, 3); load_gu(6, gu_slot(6)); WAITV(11); lds_post(a_land, 5); STAMP4(23);
      lds_wait(L, L_DONE, 4); load_gu(7, gu_slot(7)); WAITV(7); lds_post(a_land, 6); WDTURN(2); WAITV(4); lds_post(a_land, 7); STAMP4(24);
        wait_flags(FLAG(WS_FUV, j4 * 64 + lane), tag);
        {
          u32x4 pt[16];
          const u32 o = lane < 28 ? (w4 * 28 + lane) * 16 : 0x7fffffffu;
#pragma unroll
          for (int k = 0; k < 16; ++k) pt[k] = __builtin_amdgcn_raw_buffer_load_b128(rs, o, WS_UV + (j4 * 16 + k) * 2048, 16);
          SB();
          f32x4 a = {};
#pragma unroll
          for (int k = 0; k < 16; ++k) {
            const f32x4 t_ = __builtin_bit_cast(f32x4, pt[k]);
#pragma unroll
            for (int e = 0; e < 4; ++e) { float y_ = a[e] + t_[e]; asm("" : "+v"(y_)); a[e] = y_; }
          }
          if (lane < 28) {
            const u32 vi = (w4 * 28 + lane) * 4;
#pragma unroll
            for (int e = 0; e < 4; ++e) {
              const float hi = rb(a[e]);
              *(LAS u32*)(L + L_UVT + ((vi >> 6) * 32 + (vi & 31) + e) * 8 + ((vi >> 5) & 1) * 4) = pk(hi, a[e] - hi);
            }
          }
        }
      WDTURN(4);
      {
        const u32 so = (t4 * 128 + 32 * w4 + r32) * 64 + hh * 32;
        const u32x4 s0 = __builtin_amdgcn_raw_buffer_load_b128(rs, so, WS_ST, 16), s1 = __builtin_amdgcn_raw_buffer_load_b128(rs, so + 16, WS_ST, 16);
        auto xsum = [&](float v) {
          auto r_ = __builtin_amdgcn_permlane32_swap(__float_as_uint(v), __float_as_uint(v), false, false);
          return __uint_as_float(r_[0]) + __uint_as_float(r_[1]);
        };
        const float a0 = __uint_as_float(s0[0]), a1 = __uint_as_float(s0[2]), a2 = __uint_as_float(s1[0]), a3 = __uint_as_float(s1[2]);
        const float me = xsum((a0 + a1) + (a2 + a3)) * (1.f / 512.f);
        const float d0 = a0 * (1.f / 64.f) - me, d1 = a1 * (1.f / 64.f) - me, d2 = a2 * (1.f / 64.f) - me, d3 = a3 * (1.f / 64.f) - me;
        const float m2 = (__uint_as_float(s0[1]) + __uint_as_float(s0[3])) + (__uint_as_float(s1[1]) + __uint_as_float(s1[3]));
        float mb, sd;
        ln_scal(me, xsum(m2 + 64.f * ((d0 * d0 + d1 * d1) + (d2 * d2 + d3 * d3))) * (1.f / 512.f), mb, sd);
        if (lane < 32) lds_st128(L, L_BST + (32 * w4 + lane) * 16, (u32x4){pk(-mb, -mb), pk(sd, sd), __float_as_uint(__builtin_amdgcn_rcpf(sd)), 0u});
      }
      lds_post(a_land, 8);
      STAMP4(25);
      lds_wait(L, L_DONE, 5); load_wd(8); load_wd(9); load_wd(10);
      lds_wait(L, L_DONE, 6); load_wd(11); load_wd(12); load_wd(13);
      lds_wait(L, L_DONE, 7); load_wd(14); load_wd(15); load_wd(16); load_wd(17);
      lds_wait(L, L_DONE, 8);
    }
    STAMP(15);
#pragma unroll
    for (int i = 18; i < 23; ++i) load_wd(i);
    WAITV(24);
#pragma unroll
    for (int k = 6; k < 11; ++k) { wp0[k] = lds_tr(L, WD0(k), WD0(k) + 1024); wp1[k] = lds_tr(L, WD1(k), WD1(k) + 1024); }
#pragma unroll
    for (int k = 6; k < 11; ++k) { PIN(wp0[k]); PIN(wp1[k]); }
#pragma unroll
    for (int i = 23; i < 28; ++i) load_wd(i);
    STAMP(16);
    wait_flags(FLAG(WS_FA, (t3 * 4 + 2 * hf + ((lane >> 2) & 1)) * 16 + 4 * w4 + (lane & 3)), tag);
    STAMP(17);
    const u32 ao = WS_ACT + ((t3 * 4 + 2 * hf) * 112 + 28 * w4) * 1024 + lane * 16;
    u32x4 af0[28], af1[28];
    auto ld_act = [&](int i) {
      int ks = i + rot5; ks -= ks >= 28 ? 28 : 0;
      af0[i] = __builtin_amdgcn_raw_buffer_load_b128(rs, ao, ks * 1024, 16);
      af1[i] = __builtin_amdgcn_raw_buffer_load_b128(rs, ao + 112 * 1024, ks * 1024, 16);
    };
#pragma unroll
    for (int i = 0; i < 5; ++i) ld_act(i);
    f32x16 a00 = {}, a01 = {}, a10 = {}, a11 = {};
    static_for<0, 28>([&](auto ic) {
      constexpr int i = ic.value;
      SB();
      a00 = MFMA(wp0[i % 11], asbf(af0[i]), a00);
      a01 = MFMA(wp1[i % 11], asbf(af0[i]), a01);
      SB();
      a10 = MFMA(wp0[i % 11], asbf(af1[i]), a10);
      if constexpr (i < 6) { ld_act(5 + 2 * i); ld_act(6 + 2 * i); }
      else if constexpr (i + 11 < 28) ld_act(i + 11);
      SB();
      a11 = MFMA(wp1[i % 11], asbf(af1[i]), a11);
      if constexpr (i >= 8 && i + 3 < 28) {
        wp0[(i + 3) % 11] = lds_tr(L, WD0(i + 3), WD0(i + 3) + 1024);
        wp1[(i + 3) % 11] = lds_tr(L, WD1(i + 3), WD1(i + 3) + 1024);
      }
      PIN(a00); PIN(a01); PIN(a10); PIN(a11);
    });
    STAMP(18);
    red_write(20 * KB + w4 * 2048, 8192, a00, a01);
    red_write(92 * KB + w4 * 2048, 8192, a10, a11);
  }
  BAR();
  {
    f32x4 lo, hi;
    red_read(mw ? 20 * KB : 60 * KB, 8192, 2048, lo, hi);
    u32 ln = lane_id();
    const f32x4 gb0 = lds_ldf4(L, L_MISC + M_GB + (ln & 7) * 32), gb1 = lds_ldf4(L, L_MISC + M_GB + (ln & 7) * 32 + 16);
    u32x4 o;
    o[0] = pk(bf_lo(x1p[0]) + rb(gb0[0] * rb(lo[0])), bf_hi(x1p[0]) + rb(gb0[1] * rb(lo[1])));
    o[1] = pk(bf_lo(x1p[1]) + rb(gb0[2] * rb(lo[2])), bf_hi(x1p[1]) + rb(gb0[3] * rb(lo[3])));
    o[2] = pk(bf_lo(x1p[2]) + rb(gb1[0] * rb(hi[0])), bf_hi(x1p[2]) + rb(gb1[1] * rb(hi[1])));
    o[3] = pk(bf_lo(x1p[3]) + rb(gb1[2] * rb(hi[2])), bf_hi(x1p[3]) + rb(gb1[3] * rb(hi[3])));
    *(GAS u32x4*)((GAS char*)out + (t3 * 128 + hf * 64 + 8 * w) * 1024 + cg * 128 + (ln >> 3) * 1024 + (ln & 7) * 16) = o;
  }
  STAMP(19);
  asm volatile(".Ladaln_end:");
}

extern "C" void kernel_launch(void* const* d_in, const int* in_sizes, int n_in, void* d_out, int out_size, void* d_ws, size_t ws_size,
                              hipStream_t stream) {
  adaln_block<<<256, 512, 0, stream>>>((const u16*)d_in[0], (const u16*)d_in[1], (const u16*)d_in[2], (const u16*)d_in[4], (const u16*)d_in[5],
                                       (const u16*)d_in[6], (const u16*)d_in[7], (const u16*)d_in[8], (u16*)d_out, (char*)d_ws);
}
```

```cpp
#include <hip/hip_runtime.h>
#include <stdint.h>

typedef unsigned short u16;
typedef unsigned int u32;
typedef unsigned long long u64;
typedef __attribute__((ext_vector_type(8))) __bf16 bf16x8;
typedef __attribute__((ext_vector_type(2))) __bf16 bf16x2;
typedef __attribute__((ext_vector_type(4))) short s16x4;
typedef __attribute__((ext_vector_type(8))) short s16x8;
typedef __attribute__((ext_vector_type(16))) float f32x16;
typedef __attribute__((ext_vector_type(4))) float f32x4;
typedef __attribute__((ext_vector_type(2))) float f32x2;
typedef __attribute__((ext_vector_type(4))) u32 u32x4;
typedef __attribute__((ext_vector_type(2))) u32 u32x2;
#define LAS __attribute__((address_space(3)))
#define GAS __attribute__((address_space(1)))
#define DI __device__ __forceinline__
#define RLX_AGENT __ATOMIC_RELAXED, __HIP_MEMORY_SCOPE_AGENT

constexpr u32 WS_EPOCH = 0;
constexpr u32 WS_ARR = 1024;
constexpr u32 WS_MODG = 4096;
constexpr u32 WS_ST = 65536;
constexpr u32 WS_UV = 196608;
constexpr u32 WS_KV = 1048576;
constexpr u32 WS_O = WS_KV + 4194304;
constexpr u32 WS_X1 = WS_O + 2097152;
constexpr u32 WS_ACT = WS_X1 + 2097152;
#define WO_AT 0
#define FS 16
#define MREP 8
constexpr u32 WS_FKV = WS_ACT + 7340032;
constexpr u32 WS_FO = WS_FKV + 1024 * 4 * FS;
constexpr u32 WS_FX = WS_FO + 2048 * 4 * FS;
constexpr u32 WS_FA = WS_FX + 2048 * 4 * FS;
constexpr u32 WS_FUV = WS_FA + 1024 * 4 * FS;
constexpr u32 WS_MOD1 = WS_FUV + 1024 * 4 * FS;
constexpr u32 WS_DBG = 20971520;
constexpr u32 WS_BYTES = WS_DBG + 1048576;
static_assert(WS_MOD1 + 8 * 4096 <= WS_DBG && WS_UV + 256 * 2048 <= WS_KV, "workspace map");
#define FLAG(base, i) ((GAS u32*)(ws + (base)) + (i) * FS)

constexpr int SPIN_MAX = 1 << 17;
constexpr u32 KB = 1024;
constexpr u32 L_WO = 0;
constexpr u32 L_KV = 64 * KB;
constexpr u32 L_QX = 144 * KB;
constexpr u32 L_AR1 = 120 * KB;
constexpr u32 L_YL = 0;
constexpr u32 L_MISC = 148 * KB;
constexpr u32 L_C1 = L_MISC, L_SH = L_MISC + 2048, L_C1B = L_MISC + 4096, L_SHB = L_MISC + 6144, M_C1B = 4096;
constexpr u32 L_UVT = L_C1, L_BST = L_SH;
constexpr u32 L_ONES = L_MISC + 8192;
constexpr u32 L_GA = L_ONES + 256, M_GA = 8192 + 256, M_GB = 8192 + 512;
constexpr u32 L_DONE = L_ONES + 784, L_LAND = L_ONES + 800, L_MODF = L_ONES + 816;
constexpr u32 L_TAG = L_ONES + 768, L_JUNK = L_ONES + 1536;
constexpr u32 L_RED = L_MISC + 10240;
constexpr u32 LDS_BYTES = 163840;
DI constexpr u32 gu_slot(int c) { return (c & 3) == 3 ? 36 * KB : 64 * KB + (c & 3) * 28 * KB; }
DI constexpr u32 wd_base(int i) {
  return i < 8 ? 20 * KB + (i & 1) * 8192u : i < 11 ? 64 * KB + (i - 8) * 8192u : i < 18 ? 92 * KB + (i - 11) * 8192u : i < 21 ? 36 * KB + (i - 18) * 8192u
       : i < 23 ? (i - 21) * 8192u : i < 25 ? 20 * KB + (i - 23) * 8192u : 64 * KB + (i - 25) * 8192u;
}
constexpr u32 CODE_LINES = 12;

constexpr float NEGBIG = -1e30f;
constexpr float QSCALE = 0.25f * 1.4426950408889634f;

template <int I> struct IC { static constexpr int value = I; };
template <int I, int N, class F> DI void static_for(F&& f) { if constexpr (I < N) { f(IC<I>{}); static_for<I + 1, N>(f); } }
DI u32 lane_id() { u32 r; asm volatile("v_mbcnt_lo_u32_b32 %0, -1, 0\n\tv_mbcnt_hi_u32_b32 %0, -1, %0" : "=v"(r)); return r; }
DI float bf_lo(u32 v) { return __uint_as_float(v << 16); }
DI float bf_hi(u32 v) { return __uint_as_float(v & 0xffff0000u); }
DI u32 pk(float a, float b) { f32x2 v = {a, b}; return __builtin_bit_cast(u32, __builtin_convertvector(v, bf16x2)); }
DI float rb(float a) { return bf_lo(pk(a, a)); }
template <int O> DI u32x4 pack8(const f32x16& a) { return (u32x4){pk(a[O], a[O + 1]), pk(a[O + 2], a[O + 3]), pk(a[O + 4], a[O + 5]), pk(a[O + 6], a[O + 7])}; }
DI bf16x8 asbf(u32x4 v) { return __builtin_bit_cast(bf16x8, v); }
#define MFMA(a, b, c) __builtin_amdgcn_mfma_f32_32x32x16_bf16((a), (b), (c), 0, 0, 0)
template <int CTRL, int RM = 0xf> DI float dpp(float v) { return __builtin_bit_cast(float, __builtin_amdgcn_update_dpp(0, __builtin_bit_cast(int, v), CTRL, RM, 0xf, true)); }
DI float sum8(float v) { v += dpp<0xB1>(v); v += dpp<0x4E>(v); v += dpp<0x141>(v); return v; }
DI float dot2(u32 a, u32 b, float c) { return __builtin_amdgcn_fdot2_f32_bf16(__builtin_bit_cast(bf16x2, a), __builtin_bit_cast(bf16x2, b), c, false); }

DI u32x4 lds_ld128(LAS char* L, u32 off) { return *(LAS u32x4*)(L + off); }
DI f32x4 lds_ldf4(LAS char* L, u32 off) { return *(LAS f32x4*)(L + off); }
DI void lds_st128(LAS char* L, u32 off, u32x4 v) { *(LAS u32x4*)(L + off) = v; }
DI bf16x8 lds_tr(LAS char* L, u32 off0, u32 off1) {
  s16x4 a = __builtin_amdgcn_ds_read_tr16_b64_v4i16((LAS s16x4*)(L + off0));
  s16x4 b = __builtin_amdgcn_ds_read_tr16_b64_v4i16((LAS s16x4*)(L + off1));
  return __builtin_bit_cast(bf16x8, __builtin_shufflevector(a, b, 0, 1, 2, 3, 4, 5, 6, 7));
}
DI u32x4 make_srd(const void* p, u32 bytes) { const u64 a = (u64)p; return (u32x4){(u32)a, (u32)(a >> 32), bytes, 0x00020000u}; }
DI void glds(const u32x4& srd, u32 voff, u32 soff, u32 ldsaddr) {
  asm volatile("s_mov_b32 m0, %3\n\ts_nop 0\n\tbuffer_load_dwordx4 %0, %1, %2 offen lds" ::"v"(voff), "s"(srd), "s"(soff), "s"(ldsaddr) : "memory");
}
DI void glds_sc1(const u32x4& srd, u32 voff, u32 soff, u32 ldsaddr) {
  asm volatile("s_mov_b32 m0, %3\n\ts_nop 0\n\tbuffer_load_dwordx4 %0, %1, %2 offen sc1 lds" ::"v"(voff), "s"(srd), "s"(soff), "s"(ldsaddr) : "memory");
}
DI void touch(const u32x4& srd, u32 voff, u32 ldsaddr) {
  asm volatile("s_mov_b32 m0, %2\n\ts_nop 0\n\tbuffer_load_dword %0, %1, 0 offen lds" ::"v"(voff), "s"(srd), "s"(ldsaddr) : "memory");
}
DI void lds_post(u32 addr, u32 val) {
  asm volatile("s_mov_b64 exec, 1\n\tds_write_b32 %0, %1\n\ts_mov_b64 exec, -1" ::"v"(addr), "v"(val) : "memory");
}
DI bool all_ge(u32x4 v, u32 k) { return __builtin_amdgcn_readfirstlane(min(min(v[0], v[1]), min(v[2], v[3]))) >= k; }
DI void lds_wait(LAS char* L, u32 off, u32 k) {
#pragma nounroll
  for (int it = 0; it < (SPIN_MAX << 4); ++it) {
    if (all_ge(*(volatile LAS u32x4*)(L + off), k)) break;
    __builtin_amdgcn_s_sleep(3);
  }
  asm volatile("" ::: "memory");
}
#define BAR() asm volatile("s_waitcnt lgkmcnt(0)\n\ts_barrier" ::: "memory")
#define WAITV_BAR(N) asm volatile("s_waitcnt vmcnt(%0) lgkmcnt(0)\n\ts_barrier" ::"i"(N) : "memory")
#define WAITV(N) asm volatile("s_waitcnt vmcnt(%0)" ::"i"(N) : "memory")
#define SB() __builtin_amdgcn_sched_barrier(0)
#define PIN(x) asm volatile("" : "+v"(x))
#define PINS(x) asm volatile("" : "+s"(x))
#define ORDER() __builtin_amdgcn_fence(__ATOMIC_ACQUIRE, "wavefront")

DI void wait_flags(const GAS u32* p, u32 tag) {
  u32 v = tag + 1u;
#pragma nounroll
  for (int it = 0; it < SPIN_MAX; ++it) {
    if (v != tag) v = __hip_atomic_load(p, RLX_AGENT);
    if (__all(v == tag)) break;
    __builtin_amdgcn_s_sleep(4);
  }
  ORDER();
}
template <int N> DI void gather_gran(const GAS u64* g, u32 tag, u32 (&v)[N], int stride) {
#pragma nounroll
  for (int it = 0; it < SPIN_MAX; ++it) {
    bool ok = true;
#pragma unroll
    for (int k = 0; k < N; ++k) { u64 x = __hip_atomic_load(g + k * stride, RLX_AGENT); v[k] = (u32)x; ok &= (u32)(x >> 32) == tag; }
    if (__all(ok)) break;
    __builtin_amdgcn_s_sleep(1);
  }
  ORDER();
}

#define STAMPC(n) do {} while (0)
#define STAMP(n) do {} while (0)

#define STAMP4(n) do {} while (0)
#define STAMP1(n) do {} while (0)
DI void ln_scal(float mean, float var, float& mb, float& sd) {
  mb = rb(mean);
  sd = rb(__builtin_amdgcn_sqrtf(rb(rb(fmaxf(var, 0.f)) + 1e-5f)));
}
DI float fma1(float a, float b, float c) { float r = fmaf(a, b, c); asm("" : "+v"(r)); return r; }
typedef __attribute__((ext_vector_type(2))) _Float16 f16x2;
DI u32x4 znorm8(u32x4 x, float r, float mr) {
  u32x4 o;
#pragma unroll
  for (int e = 0; e < 4; ++e) {
    const f32x2 v = {fmaf(bf_lo(x[e]), r, -mr), fmaf(bf_hi(x[e]), r, -mr)};
    o[e] = __builtin_bit_cast(u32, __builtin_convertvector(v, f16x2));
  }
  return o;
}
DI u32 xform2(u32 z_, u32 c_, u32 s_) {
  const f16x2 z = __builtin_bit_cast(f16x2, z_), c = __builtin_bit_cast(f16x2, c_), s = __builtin_bit_cast(f16x2, s_);
  return pk(fma1((float)z[0], (float)c[0], (float)s[0]), fma1((float)z[1], (float)c[1], (float)s[1]));
}
DI u32x4 xform8(u32x4 x, u32x4 c, u32x4 s) { return (u32x4){xform2(x[0], c[0], s[0]), xform2(x[1], c[1], s[1]), xform2(x[2], c[2], s[2]), xform2(x[3], c[3], s[3])}; }
DI u32 pkh(float a, float b) { const f32x2 v = {a, b}; return __builtin_bit_cast(u32, __builtin_convertvector(v, f16x2)); }
DI f32x2 geglu2(f32x2 ag, f32x2 au, float k1, float k3, float i2) {
  const f32x2 a = ag * (ag * ag * k3 + k1);
  const f32x2 d = (f32x2){__builtin_amdgcn_exp2f(a[0]), __builtin_amdgcn_exp2f(a[1])} * i2 + i2;
  return ag * au * (f32x2){__builtin_amdgcn_rcpf(d[0]), __builtin_amdgcn_rcpf(d[1])};
}

__global__ __launch_bounds__(512, 2) void adaln_block(const u16* __restrict__ x, const u16* __restrict__ wqkv, const u16* __restrict__ wo,
                                                      const u16* __restrict__ wg, const u16* __restrict__ wu, const u16* __restrict__ wd,
                                                      const u16* __restrict__ cvec, const u16* __restrict__ wm, u16* __restrict__ out,
                                                      char* __restrict__ ws) {
  __shared__ __attribute__((aligned(1024))) char lds[LDS_BYTES];
  LAS char* L = (LAS char*)lds;
  const u32 lbase = (u32)(uintptr_t)L;
  const int tid = threadIdx.x, lane = tid & 63;
  const int w = __builtin_amdgcn_readfirstlane(tid >> 6), w4 = w & 3;
  const bool mw = w < 4;
  const int b = blockIdx.x, xcd = b & 7, ii = b >> 3;
  if (w == 7) {
    u64 pc;
    int len;
    asm volatile("s_getpc_b64 %0\n.Ladaln_pf:\n\ts_mov_b32 %1, .Ladaln_end-.Ladaln_pf" : "=s"(pc), "=s"(len));
    touch(make_srd((const void*)pc, len > 0 ? (u32)len : 0u), ((lane < CODE_LINES ? lane : 0) * 32 + ii) * 128u, lbase + L_JUNK);
  }
  const int u1 = 31 - (8 * (xcd >> 1) + (ii >> 2)), q1 = 4 * (xcd & 1) + (ii & 3);
  const int t3 = 2 * xcd + (ii >> 4), hf = (ii >> 3) & 1, cg = ii & 7;
  const int t4 = 8 * (xcd >> 2) + (ii >> 2), j4 = 4 * (xcd & 3) + (ii & 3);
  const int rot1 = (u1 + 2 * q1) & 7;
  const int rot4 = ii >> 2, rot5 = 7 * (ii >> 3);
  const int r32 = lane & 31, hh = lane >> 5, blk = (lane >> 4) & 1, q4 = (lane & 15) >> 2, p4 = lane & 3, l3 = lane >> 3, l7 = lane & 7;
  const __amdgpu_buffer_rsrc_t rs = __builtin_amdgcn_make_buffer_rsrc(ws, 0, WS_BYTES, 0x00020000);
  STAMP(0);

  u32 tag = 0;
  const int orow = t3 * 128 + hf * 64 + 8 * w + l3;
  const int mg = b;
  const u32x4 srd_q = make_srd(wqkv, 512 * 3072);
  const u32 q8o = l3 * 3072u + ((l7 ^ (((lane >> 4) & 1) << 2)) << 4);
  auto load_wq = [&](int c) {
    const u32 so = ((c + rot1) & 7) * 196608 + w * 1024 + q1 * 128;
#pragma unroll
    for (int e = 0; e < 8; ++e) glds(srd_q, q8o, so + e * 24576, lbase + (c % 5) * 24576 + w * 8192 + e * 1024);
  };
  u32 fto;
  {
    const int hp_ = w4 >> 1, hd_ = w >> 2, qb_ = (w ^ hd_) & 1;
    const u32 o = lane == 0 ? WS_FO + ((2 * u1 + qb_) * 32 + 4 * q1 + 2 * hp_ + hd_) * 4 * FS
                : lane == 1 ? WS_FX + ((t3 * 4 + 2 * hf + (w >> 2)) * 32 + cg * 4 + w4) * 4 * FS
                : lane == 2 ? (mw ? WS_FKV + (((u1 >> 1) * 16 + 2 * q1 + hp_) * 4 + 2 * (u1 & 1) + qb_) * 4 * FS : WS_FUV + (j4 * 64 + t4 * 4 + w4) * 4 * FS)
                : lane == 3 ? WS_FA + ((t4 * 4 + w4) * 16 + j4) * 4 * FS
                : lane < 8 ? WS_ST + (t3 * 128 + hf * 64 + 8 * w) * 64 + (lane - 4) * 128 : 0x7fffffffu;
    fto = o;
  }
  u32x4 X[16];
  u32x4 xr;
  const int hp = w4 >> 1, hd = w >> 2, qb = (w ^ hd) & 1;
  bf16x8 qf_[15], kf_[15], vf_[15];
  u32 wl[3];
#define P1O(n) (wl[(((n) >> 2) % 5) >> 1] + ((((n) >> 2) % 5) & 1) * 24576 + ((n) & 3) * 2048)
#define P1I(n) ((n) < 12 ? (n) : 12 + (n) % 3)
  u32 cso = L_MISC + l7 * 32;
  PIN(cso);
  const u32x4 srd_g = make_srd(wg, 512 * 3584), srd_u = make_srd(wu, 512 * 3584), srd_d = make_srd(wd, 1792 * 1024), srd_o = make_srd(wo, 512 * 1024);

  u32 guo[7];
  const u32x4 srd_3 = w4 < 2 ? srd_g : srd_u;
  auto load_gu = [&](int c, u32 slot) {
    int r_ = rot4; PINS(r_);
    const u32 so = j4 * 224 + ((c + r_) & 7) * (64 * 3584), ld = lbase + slot + w4 * 1024;
#pragma unroll
    for (int n = 0; n < 7; ++n) glds(n < 3 ? srd_g : n == 3 ? srd_3 : srd_u, guo[n], so, ld + n * 4096);
  };
  const u32 w8o = l3 * 1024u + ((l7 ^ (((lane >> 4) & 1) << 2)) << 4);
  bf16x8 qf;
  if (mw) {
    u32x4 M[16];
    u32 cv = 0;
    if (mg < 48) {
      const GAS char* mb = (const GAS char*)wm + mg * 128 + (128 * w + 16 * l3) * 6144 + l7 * 16;
      cv = ((const GAS u32*)cvec)[64 * w + lane];
#pragma unroll
      for (int n = 0; n < 16; ++n) M[n] = *(const GAS u32x4*)(mb + n * 6144);
    }
    const u32 ep = __hip_atomic_load((const GAS u32*)(ws + WS_EPOCH), RLX_AGENT);
    if (lane < 12) *(LAS u32*)(L + L_DONE + lane * 4) = 0u;
    SB();
    BAR();
    STAMP(1);
    if (mg < 48) {
      {
        const float c0 = bf_lo(cv), c1 = bf_hi(cv);
        *(LAS u32*)(L + L_RED + 1024 + w * 256 + lane * 4) = pk(c0 * __builtin_amdgcn_rcpf(1.0f + __builtin_amdgcn_exp2f(-1.4426950408889634f * c0)),
                                                               c1 * __builtin_amdgcn_rcpf(1.0f + __builtin_amdgcn_exp2f(-1.4426950408889634f * c1)));
      }
      const u32x4 s0 = lds_ld128(L, L_RED + 1024 + w * 256 + l3 * 32), s1 = lds_ld128(L, L_RED + 1024 + w * 256 + l3 * 32 + 16);
      f32x2 a[4] = {};
#pragma unroll
      for (int n = 0; n < 8; ++n) {
        const u32 sp = n < 4 ? s0[n & 3] : s1[n & 3];
#pragma unroll
        for (int e = 0; e < 4; ++e) {
          const u32 r0 = M[2 * n][e], r1 = M[2 * n + 1][e];
          a[e][0] = dot2(__builtin_amdgcn_perm(r1, r0, 0x05040100u), sp, a[e][0]);
          a[e][1] = dot2(__builtin_amdgcn_perm(r1, r0, 0x07060302u), sp, a[e][1]);
        }
      }
      float v[8] = {a[0][0], a[0][1], a[1][0], a[1][1], a[2][0], a[2][1], a[3][0], a[3][1]};
#pragma unroll
      for (int e = 0; e < 8; ++e) {
        v[e] += dpp<0x128>(v[e]);
        auto r1_ = __builtin_amdgcn_permlane16_swap(__float_as_uint(v[e]), __float_as_uint(v[e]), false, false);
        v[e] = __uint_as_float(r1_[0]) + __uint_as_float(r1_[1]);
        auto r2_ = __builtin_amdgcn_permlane32_swap(__float_as_uint(v[e]), __float_as_uint(v[e]), false, false);
        v[e] = __uint_as_float(r2_[0]) + __uint_as_float(r2_[1]);
      }
      if (lane < 8) {
        *(LAS f32x4*)(L + L_RED + w * 256 + lane * 32) = (f32x4){v[0], v[1], v[2], v[3]};
        *(LAS f32x4*)(L + L_RED + w * 256 + lane * 32 + 16) = (f32x4){v[4], v[5], v[6], v[7]};
      }
      lds_post(lbase + L_MODF + w * 4, 1);
      if (w == 3) {
        lds_wait(L, L_MODF, 1);
        float s = 0.f;
#pragma unroll
        for (int k = 0; k < 4; ++k) s += *(LAS float*)(L + L_RED + k * 256 + lane * 4);
        const float o = dpp<0xB1>(s);
        const u32 tg = __builtin_amdgcn_readfirstlane(ep) + 1u;
        const u64 gr = ((u64)tg << 32) | ((lane & 1) ? pk(o, s) : pk(s, o));
        if (mg < 16) {
#pragma unroll
          for (int r = 0; r < MREP / 2 + (MREP & 1); ++r)
            if (2 * r + (lane & 1) < MREP) __hip_atomic_store((GAS u64*)(ws + WS_MOD1) + (2 * r + (lane & 1)) * 512 + 32 * mg + (lane >> 1), gr, RLX_AGENT);
        } else if (!(lane & 1)) __hip_atomic_store((GAS u64*)(ws + WS_MODG) + 32 * mg + (lane >> 1), gr, RLX_AGENT);
        STAMP(28);
      }
    }
    xr = *(const GAS u32x4*)((const GAS char*)x + orow * 1024 + cg * 128 + l7 * 16);
    tag = __builtin_amdgcn_readfirstlane(ep) + 1u;
    if (w < 3) {
      load_wq(0); load_wq(1); load_wq(2); load_wq(3); load_wq(4);
      WAITV(16);
    }
    lds_post(lbase + L_MODF + w * 4, 2);
    wl[0] = (8 * hh + q4) * 128 + ((64 * hp + 32 * blk + 8 * p4) ^ ((q4 >> 1) << 6));
    wl[1] = wl[0] + 48 * KB; wl[2] = wl[0] + 96 * KB;
    PIN(wl[1]); PIN(wl[2]);
    lds_wait(L, L_MODF, 2);
#pragma unroll
    for (int n = 0; n < 12; ++n) { kf_[n] = lds_tr(L, P1O(n) + 8192, P1O(n) + 8704); vf_[n] = lds_tr(L, P1O(n) + 16384, P1O(n) + 16896); qf_[n] = lds_tr(L, P1O(n), P1O(n) + 512); }
    lds_post(lbase + L_MODF + w * 4, 3);
#pragma unroll
    for (int n = 0; n < 12; ++n) { PIN(kf_[n]); PIN(vf_[n]); PIN(qf_[n]); }
    if (w < 3) {
      lds_wait(L, L_MODF, 3);
      load_wq(5); load_wq(6); load_wq(7);
    } else {
      if (lane < 32) lds_st128(L, L_ONES + (lane & 15) * 16 + (lane >> 4) * 1024, (u32x4){0x3f803f80u, 0x3f803f80u, 0x3f803f80u, 0x3f803f80u});
      if (lane == 0) *(LAS u32*)(L + L_TAG) = tag;
      u32x4 v[4];
#pragma nounroll
      for (int it = 0; it < SPIN_MAX; ++it) {
        bool ok = true;
#pragma unroll
        for (int k = 0; k < 4; ++k) { v[k] = __builtin_amdgcn_raw_buffer_load_b128(rs, lane * 16, WS_MOD1 + (xcd % MREP) * 4096 + k * 1024, 16); ok &= v[k][1] == tag && v[k][3] == tag; }
        if (__all(ok)) break;
        __builtin_amdgcn_s_sleep(2);
      }
#pragma unroll
      for (int k = 0; k < 2; ++k) {
        const u32 o = L_C1 + ((lane + 64 * k) >> 1) * 32 + (lane & 1) * 8;
        *(LAS u32x2*)(L + o + 16) = (u32x2){pkh(bf_lo(v[k][0]), bf_hi(v[k][0])), pkh(bf_lo(v[k][2]), bf_hi(v[k][2]))};
        *(LAS u32x2*)(L + o) = (u32x2){pkh(rb(1.f + bf_lo(v[2 + k][0])), rb(1.f + bf_hi(v[2 + k][0]))), pkh(rb(1.f + bf_lo(v[2 + k][2])), rb(1.f + bf_hi(v[2 + k][2])))};
      }
    }
    touch(make_srd(ws, WS_BYTES), fto, lbase + L_JUNK);
    STAMP(3);
    BAR();
    STAMP(4);
    STAMPC(29);
    f32x16 aq = {}, ak = {}, av = {};
    u32 ard[4];
    {
      const int row = 32 * qb + r32, f = (row & 7) ^ ((row >> 3) & 1);
#pragma unroll
      for (int s = 0; s < 4; ++s) { ard[s] = L_AR1 + row * 128 + ((((2 * s) | hh) ^ f) << 4); PIN(ard[s]); }
    }
    bf16x8 hf_[3];
#define P1H(n) hf_[(n) % 3] = asbf(lds_ld128(L, ard[(n) & 3] + (((n) >> 2) % 3) * 8192))
    BAR();
    P1H(0); P1H(1);
    static_for<0, 32>([&](auto nc) {
      constexpr int n = nc.value, m = n + 2;
      constexpr bool bnd = (m & 3) == 0, rd = m >= 12 && m < 32;
      SB();
      ak = MFMA(kf_[P1I(n)], hf_[n % 3], ak);
      if constexpr (m < 32 && !bnd) { P1H(m); if constexpr (rd) kf_[P1I(m)] = lds_tr(L, P1O(m) + 8192, P1O(m) + 8704); }
      SB();
      av = MFMA(hf_[n % 3], vf_[P1I(n)], av);
      if constexpr (m < 32 && bnd) {
        PIN(ak); PIN(av);
        STAMP1(20 + m / 4);
        WAITV_BAR(m >= 12 ? 56 - 2 * m : 63);
        P1H(m);
        if constexpr (rd) kf_[P1I(m)] = lds_tr(L, P1O(m) + 8192, P1O(m) + 8704);
      }
      if constexpr (rd) vf_[P1I(m)] = lds_tr(L, P1O(m) + 16384, P1O(m) + 16896);
      SB();
      aq = MFMA(qf_[P1I(n)], hf_[n % 3], aq);
      if constexpr (rd) qf_[P1I(m)] = lds_tr(L, P1O(m), P1O(m) + 512);
      PIN(ak); PIN(av); PIN(aq);
    });
    STAMP(5);
    STAMPC(30);
    const u32x4 f0 = pack8<0>(ak), f1 = pack8<8>(ak), f2 = pack8<0>(av), f3 = pack8<8>(av);
    const int kvi = ((u1 >> 1) * 16 + 2 * q1 + hp) * 4 + 2 * (u1 & 1) + qb;
    const u32 o = WS_KV + kvi * 4096 + lane * 16;
    __builtin_amdgcn_raw_buffer_store_b128(f0, rs, o, 0, 16);
    __builtin_amdgcn_raw_buffer_store_b128(f1, rs, o + 1024, 0, 16);
    __builtin_amdgcn_raw_buffer_store_b128(f2, rs, o + 2048, 0, 16);
    __builtin_amdgcn_raw_buffer_store_b128(f3, rs, o + 3072, 0, 16);
    aq *= QSCALE;
    qf = asbf(pack8<0>(aq));
    const u32x4 f5 = pack8<8>(aq);
    BAR();
    const u32 ok = L_KV + hp * 40960 + (8 + qb) * 4096 + lane * 16;
    lds_st128(L, ok, f0); lds_st128(L, ok + 1024, f1); lds_st128(L, ok + 2048, f2); lds_st128(L, ok + 3072, f3);
    lds_st128(L, L_QX + w * 1024 + lane * 16, f5);
    BAR();
    WAITV(0);
    if (lane == 0) __hip_atomic_store(FLAG(WS_FKV, kvi), tag, RLX_AGENT);
  } else {
    const __amdgpu_buffer_rsrc_t rx = __builtin_amdgcn_make_buffer_rsrc((void*)x, 0, 2048 * 1024, 0x00020000);
    if (mg < 48) BAR();
#pragma unroll
    for (int c = 0; c < 8; ++c)
#pragma unroll
      for (int g = 0; g < 2; ++g)
        X[2 * c + g] = __builtin_amdgcn_raw_buffer_load_b128(rx, l3 * 1024u + l7 * 16u, (u1 * 64 + 16 * w4 + 8 * g) * 1024 + ((c + rot1) & 7) * 128, 0);
    xr = *(const GAS u32x4*)((const GAS char*)x + orow * 1024 + cg * 128 + l7 * 16);
    SB();
    if (mg >= 48) BAR();
    STAMP(2);
    float sm[2] = {}, sq[2] = {};
#pragma unroll
    for (int c = 0; c < 8; ++c) {
      SB();
#pragma unroll
      for (int e = 0; e < 4; ++e)
#pragma unroll
        for (int g = 0; g < 2; ++g) { sm[g] = dot2(X[2 * c + g][e], 0x3f803f80u, sm[g]); sq[g] = dot2(X[2 * c + g][e], X[2 * c + g][e], sq[g]); }
    }
    float rr[2], mr[2];
#pragma unroll
    for (int g = 0; g < 2; ++g) {
      const float me = sum8(sm[g]) * (1.f / 512.f);
      float mb, sd;
      ln_scal(me, sum8(sq[g]) * (1.f / 512.f) - me * me, mb, sd);
      rr[g] = __builtin_amdgcn_rcpf(sd); mr[g] = mb * rr[g];
    }
#pragma unroll
    for (int n = 0; n < 16; ++n) { X[n] = znorm8(X[n], rr[n & 1], mr[n & 1]); PIN(X[n]); }
    if (w4 < 3) {
      const u32x4 srd_a = make_srd(w4 == 0 ? wo : w4 == 1 ? wg : wu, w4 == 0 ? 512 * 1024 : 512 * 3584);
      const int e1 = 64 + lane;
      const u32 ta0 = w4 ? (16 * ii + lane / 7) * 3584 + (xcd & 3) * 896 + (lane % 7) * 128 : ii * 16384 + lane * 128;
      const u32 ta1 = w4 ? (lane < 48 ? (16 * ii + e1 / 7) * 3584 + (xcd & 3) * 896 + (e1 % 7) * 128 : 0x7fffffffu) : ta0 + 8192;
      touch(srd_a, ta0, lbase + L_JUNK);
      touch(srd_a, ta1, lbase + L_JUNK);
    }
    touch(make_srd(ws, WS_BYTES), fto, lbase + L_JUNK);
    STAMP(3);
    BAR();
    STAMP(4);
    STAMPC(29);
    tag = __builtin_amdgcn_readfirstlane(*(LAS u32*)(L + L_TAG));
    if (w == 4 && lane == 0) __hip_atomic_store((GAS u32*)(ws + WS_ARR) + b, tag, RLX_AGENT);
    const u32 awE = L_AR1 + (16 * w4 + l3) * 128 + ((l7 ^ l3) << 4), awO = (awE ^ 16u) + 1024;
    u32x4 ca[2], sa[2];
#define CSLOAD(c) do { const u32 o_ = cso + (((c) + rot1) & 7) * 256; ca[(c) & 1] = lds_ld128(L, o_); sa[(c) & 1] = lds_ld128(L, o_ + 16); } while (0)
    CSLOAD(0);
#pragma unroll
    for (int c = 0; c < 8; ++c) {
      if (c < 7) CSLOAD(c + 1);
      SB();
      PIN(X[2 * c]);
      lds_st128(L, awE + (c % 3) * 8192, xform8(X[2 * c], ca[c & 1], sa[c & 1]));
      PIN(X[2 * c + 1]);
      lds_st128(L, awO + (c % 3) * 8192, xform8(X[2 * c + 1], ca[c & 1], sa[c & 1]));
      if (c == 0) BAR();
      if (c >= 2) STAMP1(19 + c);
      if (c >= 2) asm volatile("s_waitcnt lgkmcnt(2)\n\ts_barrier" ::: "memory");
    }
    BAR();
#pragma unroll
    for (int n = 0; n < 7; ++n) {
      const int pc = w4 + 4 * n, up = pc >= 14, e = (pc - 14 * up) * 64 + lane, rho = e / 14, cc = e - 14 * rho;
      guo[n] = (u32)((up ? (rho ^ 4) : rho) * 3584 + cc * 16);
    }
    STAMP(5);
    BAR();
    BAR();
    qf = asbf(lds_ld128(L, L_QX + (w4 ^ 1) * 1024 + lane * 16));

  }
  PIN(xr);
  STAMP(6);
#define LOAD_WO() do { if (!mw && qb) { for (int n = hp * 32; n < hp * 32 + 32; ++n) glds(srd_o, w8o, cg * 128 + n * 8192, lbase + L_WO + n * 1024); } } while (0)
  LOAD_WO();

  {
    const u32 ka = L_KV + hp * 40960 + hd * 1024 + lane * 16;
    const bool vdat = (r32 >> 4) == hd;
    const u32 va = vdat ? L_KV + hp * 40960 + 2048 + lane * 16 : L_ONES + (lane & 15) * 16;
    const u32 vs = vdat ? 4096u : 0u;
    f32x16 cm;
#pragma unroll
    for (int g = 0; g < 16; ++g) cm[g] = (g & 3) + 8 * (g >> 2) + 4 * hh <= r32 ? 0.f : NEGBIG;
    const f32x16 zero = {};
    f32x16 O = {};
    float m = NEGBIG;
    auto rowmax = [&](const f32x16& s, float mm) {
#pragma unroll
      for (int g = 0; g < 16; g += 2) mm = fmaxf(fmaxf(mm, s[g]), s[g + 1]);
      return mm;
    };
    auto ldk = [&](int lb) { return asbf(lds_ld128(L, ka + lb * 4096)); };
#pragma nounroll
    for (int st = 0; st < 2; ++st) {
      int lm = 8 + qb, lo = 8, hi = 8 + qb;
      if (st) {
        STAMP(7);
        if (u1 == 0) break;
        if (!qb) {
          const int g0 = 2 * u1 - 8 + 4 * hd, gl = g0 + (lane & 3);
          wait_flags(FLAG(WS_FKV, gl < 0 ? ((u1 >> 1) * 16 + 2 * q1 + hp) * 4 + 2 * (u1 & 1) : ((gl >> 2) * 16 + 2 * q1 + hp) * 4 + (gl & 3)), tag);
          const u32x4 srd_w = make_srd(ws, WS_BYTES);
#pragma unroll
          for (int k = 0; k < 4; ++k) {
            const int g = g0 + k;
            const u32 so = g < 0 ? 0x7ff00000u : WS_KV + (((g >> 2) * 16 + 2 * q1 + hp) * 4 + (g & 3)) * 4096;
#pragma unroll
            for (int n = 0; n < 4; ++n) glds_sc1(srd_w, lane * 16, so + n * 1024, lbase + L_KV + hp * 40960 + hd * 16384 + (4 * k + n) * 1024);
          }
          WAITV(0);
        }
        const int lmin = 8 - 2 * u1;
#pragma unroll
        for (int g = 0; g < 16; ++g) { float u = lmin > qb ? NEGBIG : NEGBIG - cm[g]; asm("" : "+v"(u)); cm[g] = u; }
        lm = qb; lo = lmin > qb ? lmin : qb + 1; hi = 8;
        BAR();
        STAMP(8);
      }
      bf16x8 kA = ldk(lo), kB = ldk(lo + 1);
      f32x16 sA = MFMA(ldk(lm), qf, cm), sB, sC;
      const bf16x8 k0 = kA;
      float mn = m;
      for (int lb = lo; lb < hi; lb += 2) {
        sB = MFMA(kA, qf, zero);
        sC = MFMA(kB, qf, zero);
        kA = ldk(lb + 2); kB = ldk(lb + 3);
        SB();
        mn = rowmax(sB, mn);
        if (lb + 1 < hi) mn = rowmax(sC, mn);
      }
      mn = rowmax(sA, mn);
      {
        auto r_ = __builtin_amdgcn_permlane32_swap(__float_as_uint(mn), __float_as_uint(mn), false, false);
        mn = fmaxf(__uint_as_float(r_[0]), __uint_as_float(r_[1]));
      }
      u32 vv = va + lm * vs;
      bf16x8 v0 = asbf(lds_ld128(L, vv)), v1 = asbf(lds_ld128(L, vv + 1024));
      f32x16 negm;
#pragma unroll
      for (int g = 0; g < 16; ++g) negm[g] = -mn;
#pragma unroll
      for (int g = 0; g < 16; ++g) { float u = sA[g] - mn; asm("" : "+v"(u)); sA[g] = u; }
      if (st) {
        const float al = __builtin_amdgcn_exp2f(m - mn);
#pragma unroll
        for (int g = 0; g < 16; ++g) { float o = O[g] * al; asm("" : "+v"(o)); O[g] = o; }
      }
      m = mn;
      kA = k0;
      vv = va + lo * vs;
#define P2STEP(CUR, NXT)                                                                      \
  {                                                                                           \
    NXT = MFMA(kA, qf, negm);                                                                 \
    PIN(NXT);                                                                                 \
    kA = ldk(lb + 1);                                                                         \
    const bf16x8 n0_ = asbf(lds_ld128(L, vv)), n1_ = asbf(lds_ld128(L, vv + 1024));           \
    vv += vs;                                                                                 \
    SB();                                                                                     \
    _Pragma("unroll") for (int g = 0; g < 8; ++g) CUR[g] = __builtin_amdgcn_exp2f(CUR[g]);    \
    O = MFMA(v0, asbf(pack8<0>(CUR)), O);                                                     \
    _Pragma("unroll") for (int g = 8; g < 16; ++g) CUR[g] = __builtin_amdgcn_exp2f(CUR[g]);   \
    O = MFMA(v1, asbf(pack8<8>(CUR)), O);                                                     \
    SB();                                                                                     \
    v0 = n0_; v1 = n1_;                                                                       \
  }
      for (int lb = lo;; ++lb) {
        P2STEP(sA, sB);
        if (lb >= hi) break;
        ++lb;
        P2STEP(sB, sA);
        if (lb >= hi) break;
      }
    }
    u32x4 of;
    {
      const float inv = __builtin_amdgcn_rcpf(hd ? O[0] : O[8]);
      float o[8];
#pragma unroll
      for (int g = 0; g < 8; ++g) { o[g] = (hd ? O[8 + g] : O[g]) * inv; asm("" : "+v"(o[g])); }
      of = (u32x4){pk(o[0], o[1]), pk(o[2], o[3]), pk(o[4], o[5]), pk(o[6], o[7])};
    }
    const int oi = (2 * u1 + qb) * 32 + 4 * q1 + 2 * hp + hd;
    __builtin_amdgcn_raw_buffer_store_b128(of, rs, WS_O + oi * 1024 + lane * 16, 0, 16);
    WAITV(0);
    if (lane == 0) __hip_atomic_store(FLAG(WS_FO, oi), tag, RLX_AGENT);
  }
  STAMP(9);
  {
    if (u1 == 0 && q1 == 0 && w == 7) {
#pragma nounroll
      for (int it = 0; it < SPIN_MAX; ++it) {
        bool ok = true;
#pragma unroll
        for (int k = 0; k < 4; ++k) ok &= __hip_atomic_load((const GAS u32*)(ws + WS_ARR) + lane + 64 * k, RLX_AGENT) == tag;
        if (__all(ok)) break;
        __builtin_amdgcn_s_sleep(1);
      }
      if (lane == 0) __hip_atomic_store((GAS u32*)(ws + WS_EPOCH), tag, RLX_AGENT);
    }
  }
  BAR();

  const u32 wt0 = (4 * hh + q4) * 128 + ((32 * blk + 8 * p4) ^ ((q4 >> 1) << 6)), wt1 = wt0 ^ 64u;
  auto red_write = [&](u32 base, u32 S8, const f32x16& a0, const f32x16& a1) {
    u32 ln = lane_id();
    const u32 o = base + ((ln & 31) >> 3) * S8 + (ln & 7) * 256, sw = (ln & 7) ^ (ln >> 5);
#pragma unroll
    for (int g = 0; g < 4; ++g) {
      *(LAS f32x4*)(L + o + (((2 * g) ^ sw) << 4)) = (f32x4){a0[4 * g], a0[4 * g + 1], a0[4 * g + 2], a0[4 * g + 3]};
      *(LAS f32x4*)(L + o + (((2 * g) ^ sw) << 4) + 128) = (f32x4){a1[4 * g], a1[4 * g + 1], a1[4 * g + 2], a1[4 * g + 3]};
    }
  };
  auto red_read = [&](u32 base, u32 S8, u32 SK, f32x4& lo, f32x4& hi) {
    u32 ln = lane_id();
    const u32 o0 = base + w * S8 + (ln >> 3) * 256 + (((2 * (ln & 7)) ^ (ln >> 3)) << 4), o1 = o0 ^ 16u;
    lo = lds_ldf4(L, o0); hi = lds_ldf4(L, o1);
#pragma unroll
    for (int k = 1; k < 4; ++k) { lo += lds_ldf4(L, o0 + k * SK); hi += lds_ldf4(L, o1 + k * SK); }
  };
  u32x4 x1p;
  {
    if (mw) {
      bf16x8 wa[8], wb[8];
#pragma unroll
      for (int i = 0; i < 8; ++i) {
        const u32 o = L_WO + w * 16384 + i * 2048;
        wa[i] = lds_tr(L, o + wt0, o + wt0 + 1024); wb[i] = lds_tr(L, o + wt1, o + wt1 + 1024);
      }
      wait_flags(FLAG(WS_FO, (t3 * 4 + 2 * hf + (l3 & 1)) * 32 + 8 * w + l7), tag);
      STAMP(10);
      u32x4 of0[8], of1[8];
      const u32 oo = WS_O + ((t3 * 4 + 2 * hf) * 32 + 8 * w) * 1024 + lane * 16;
#pragma unroll
      for (int i = 0; i < 8; ++i) {
        of0[i] = __builtin_amdgcn_raw_buffer_load_b128(rs, oo + i * 1024, 0, 16);
        of1[i] = __builtin_amdgcn_raw_buffer_load_b128(rs, oo + 32768 + i * 1024, 0, 16);
      }
      SB();
      f32x16 a00 = {}, a01 = {}, a10 = {}, a11 = {};
#pragma unroll
      for (int i = 0; i < 8; ++i) {
        a00 = MFMA(wa[i], asbf(of0[i]), a00); a01 = MFMA(wb[i], asbf(of0[i]), a01);
        a10 = MFMA(wa[i], asbf(of1[i]), a10); a11 = MFMA(wb[i], asbf(of1[i]), a11);
      }
      STAMP(11);
      red_write(L_WO + w * 16384, 2048, a00, a01);
      red_write(L_WO + w * 16384 + 8192, 2048, a10, a11);
    } else {
      load_gu(0, gu_slot(0));
      u32x4 bv[2];
      {
        u32x4 q[4];
        u32 v[3];
        const GAS u64* g = (const GAS u64*)(ws + WS_MODG);
        const u32 qo = (((r32 & 1) ? 768 : 1024) + rot4 * 32 + (t4 >> 3) * 16 + 4 * hh) * 8;
#pragma nounroll
        for (int it = 0; it < SPIN_MAX; ++it) {
          const u64 x1_ = __hip_atomic_load(g + 1024 + (tid - 256), RLX_AGENT);
          const u64 x2 = __hip_atomic_load(g + (lane < 32 ? 512 : 1248) + cg * 32 + lane, RLX_AGENT);
          bool ok = (u32)(x1_ >> 32) == tag && (u32)(x2 >> 32) == tag;
#pragma unroll
          for (int k = 0; k < 4; ++k) { q[k] = __builtin_amdgcn_raw_buffer_load_b128(rs, qo + (k >> 1) * 64 + (k & 1) * 16, WS_MODG, 16); ok &= q[k][1] == tag && q[k][3] == tag; }
          v[1] = (u32)x1_; v[2] = (u32)x2;
          if (__all(ok)) break;
          __builtin_amdgcn_s_sleep(1);
        }
        *(LAS f32x2*)(L + L_C1B + (tid - 256) * 8) = (f32x2){rb(1.f + bf_lo(v[1])), rb(1.f + bf_hi(v[1]))};
        if (w == 4) *(LAS f32x2*)(L + L_GA + lane * 8) = (f32x2){bf_lo(v[2]), bf_hi(v[2])};
        const float one = (r32 & 1) ? 0.f : 1.f;
#pragma unroll
        for (int s = 0; s < 2; ++s)
          bv[s] = (u32x4){pk(one + bf_lo(q[2 * s][0]), one + bf_hi(q[2 * s][0])), pk(one + bf_lo(q[2 * s][2]), one + bf_hi(q[2 * s][2])),
                          pk(one + bf_lo(q[2 * s + 1][0]), one + bf_hi(q[2 * s + 1][0])), pk(one + bf_lo(q[2 * s + 1][2]), one + bf_hi(q[2 * s + 1][2]))};
      }
      STAMP(10);
      lds_post(lbase + L_LAND + w4 * 4, 1);
      load_gu(1, gu_slot(1));
      lds_wait(L, L_LAND, 1);
      {
        const u32 gl = blk * 14336 + (8 * hh + q4) * 224 + 8 * p4 + w4 * 32 + (t4 >> 3) * 7168 + gu_slot(0);
        const u32 g0 = gl + (blk ? 896u : 0u), g1 = gl + (blk ? 0u : 896u);
        f32x16 a0 = {}, a1 = {};
#pragma unroll
        for (int s = 0; s < 2; ++s) {
          a0 = MFMA(lds_tr(L, g0 + s * 3584, g1 + s * 3584), asbf(bv[s]), a0);
          a1 = MFMA(lds_tr(L, g0 + s * 3584 + 128, g1 + s * 3584 + 128), asbf(bv[s]), a1);
        }
        if (r32 < 2) {
          const u32 o = WS_UV + (j4 * 16 + t4) * 2048 + w4 * 256 + r32 * 128 + hh * 16;
#pragma unroll
          for (int g = 0; g < 4; ++g) {
            __builtin_amdgcn_raw_buffer_store_b128(__builtin_bit_cast(u32x4, (f32x4){a0[4 * g], a0[4 * g + 1], a0[4 * g + 2], a0[4 * g + 3]}), rs, o + g * 32, 0, 16);
            if (w4 < 3) __builtin_amdgcn_raw_buffer_store_b128(__builtin_bit_cast(u32x4, (f32x4){a1[4 * g], a1[4 * g + 1], a1[4 * g + 2], a1[4 * g + 3]}), rs, o + 1024 + g * 32, 0, 16);
          }
        }
      }
      load_gu(2, gu_slot(2));
      STAMP(11);
    }
    BAR();
    f32x4 lo, hi;
    red_read(L_WO, 2048, 16384, lo, hi);
    const f32x4 ga0 = lds_ldf4(L, cso + M_GA), ga1 = lds_ldf4(L, cso + M_GA + 16);
    float v[8];
    v[0] = rb(bf_lo(xr[0]) + rb(ga0[0] * rb(lo[0]))); v[1] = rb(bf_hi(xr[0]) + rb(ga0[1] * rb(lo[1])));
    v[2] = rb(bf_lo(xr[1]) + rb(ga0[2] * rb(lo[2]))); v[3] = rb(bf_hi(xr[1]) + rb(ga0[3] * rb(lo[3])));
    v[4] = rb(bf_lo(xr[2]) + rb(ga1[0] * rb(hi[0]))); v[5] = rb(bf_hi(xr[2]) + rb(ga1[1] * rb(hi[1])));
    v[6] = rb(bf_lo(xr[3]) + rb(ga1[2] * rb(hi[2]))); v[7] = rb(bf_hi(xr[3]) + rb(ga1[3] * rb(hi[3])));
    x1p = (u32x4){pk(v[0], v[1]), pk(v[2], v[3]), pk(v[4], v[5]), pk(v[6], v[7])};
    PIN(x1p);
    {
      const f32x4 c0 = lds_ldf4(L, cso + M_C1B + cg * 256), c1 = lds_ldf4(L, cso + M_C1B + cg * 256 + 16);
      const u32x4 y = {pk(v[0] * c0[0], v[1] * c0[1]), pk(v[2] * c0[2], v[3] * c0[3]), pk(v[4] * c1[0], v[5] * c1[1]), pk(v[6] * c1[2], v[7] * c1[3])};
      __builtin_amdgcn_raw_buffer_store_b128(y, rs, WS_X1 + ((t3 * 4 + 2 * hf + (w >> 2)) * 32 + 4 * cg + (l7 >> 1)) * 1024 + ((l7 & 1) * 32 + 8 * w4 + l3) * 16, 0, 16);
    }
    const float sm = sum8(((v[0] + v[1]) + (v[2] + v[3])) + ((v[4] + v[5]) + (v[6] + v[7])));
    const float mu = sm * (1.f / 64.f);
    float m2 = 0.f;
#pragma unroll
    for (int k = 0; k < 8; ++k) m2 = fmaf(v[k] - mu, v[k] - mu, m2);
    m2 = sum8(m2);
    if (l7 == 0) __hip_atomic_store((GAS u64*)(ws + WS_ST) + orow * 8 + cg, ((u64)__float_as_uint(m2) << 32) | __float_as_uint(sm), RLX_AGENT);
    WAITV(0);
    if (lane == 0) __hip_atomic_store(FLAG(WS_FX, (t3 * 4 + 2 * hf + (w >> 2)) * 32 + cg * 4 + w4), tag, RLX_AGENT);
    if (!mw && lane == 1) __hip_atomic_store(FLAG(WS_FUV, j4 * 64 + t4 * 4 + w4), tag, RLX_AGENT);
  }
  STAMP(12);
  BAR();

  if (mw) {
    u32 ln = lane_id();
    const u32 bk = (ln >> 4) & 1, gl = bk * 14336 + (8 * (ln >> 5) + ((ln & 15) >> 2)) * 224 + 8 * (ln & 3);
    u32 g0 = gl + (bk ? 896u : 0u) + 36 * KB, g1 = gl + (bk ? 0u : 896u) + 36 * KB, g0h = g0 + 56 * KB, g1h = g1 + 56 * KB;
    PIN(g0); PIN(g1); PIN(g0h); PIN(g1h);
    f32x16 ag[7] = {};
    bf16x8 wf_[7];
    u32x4 yf[32];
#define P4S(n) (((n) >> 2) & 3)
#define P4O(n) ((P4S(n) & 1 ? 0 : 28 * KB) + ((n) & 3) * 3584)
#define P4W(n, f) wf_[f] = lds_tr(L, (P4S(n) == 1 || P4S(n) == 2 ? g0h : g0) + P4O(n) + (f) * 32, (P4S(n) == 1 || P4S(n) == 2 ? g1h : g1) + P4O(n) + (f) * 32)
#pragma unroll
    for (int f = 0; f < 7; ++f) P4W(0, f);

    wait_flags(FLAG(WS_FX, (t4 * 4 + w) * 32 + (ln & 31)), tag);
    STAMP(13);
    auto ld_y = [&](int n) { yf[n] = __builtin_amdgcn_raw_buffer_load_b128(rs, ln * 16, WS_X1 + ((t4 * 4 + w) * 32 + ((n + 4 * rot4) & 31)) * 1024, 16); };
#pragma unroll
    for (int n = 0; n < 5; ++n) ld_y(n);
    const u32 a_done = lbase + L_DONE + w * 4;
    u32x4 land;
    static_for<0, 32>([&](auto nc) {
      constexpr int n = nc.value, m = n + 1;
      if constexpr (n + 2 >= 27 && n + 2 < 32) yf[n + 2] = lds_ld128(L, L_YL + (w * 5 + (n + 2 - 27)) * 1024 + ln * 16);
#pragma unroll
      for (int f = 0; f < 7; ++f) {
        SB();
        ag[f] = MFMA(wf_[f], asbf(yf[n]), ag[f]);
        if ((m & 3) == 0 && f == 0) {
          STAMP4(19 + m / 4);
          lds_post(a_done, m / 4);
          if (m >= 12) { PIN(ag[0]); if (!all_ge(land, m / 4)) lds_wait(L, L_LAND, m / 4); }
        }
        if ((m & 3) == 3 && m >= 11 && f == 0) land = *(volatile LAS u32x4*)(L + L_LAND);
        if (m < 32) P4W(m, f);
        if constexpr (n < 11) { if (f == 1 || f == 4) ld_y(5 + 2 * n + (f >> 2)); }
      }
#pragma unroll
      for (int f = 0; f < 7; ++f) PIN(ag[f]);
    });
    const u32x4 bst = lds_ld128(L, L_BST + (32 * w + (ln & 31)) * 16);
    const u32x4 bfr = {ln < 32 ? bst[0] : 0u, ln < 32 ? bst[1] : 0u, 0u, 0u};
#pragma unroll
    for (int f = 0; f < 7; ++f) {
      const u32x2 uv = *(LAS u32x2*)(L + L_UVT + (f * 32 + (ln & 31)) * 8);
      ag[f] = MFMA(asbf((u32x4){uv[0], uv[1], 0u, 0u}), asbf(bfr), ag[f]);
    }
    const float rs_ = __uint_as_float(bst[2]), sd_ = bf_lo(bst[1]);
    const float k1 = -2.0f * 0.7978845608028654f * 1.4426950408889634f * rs_, k3 = k1 * 0.044715f * rs_ * rs_, i2 = sd_ * sd_;
    STAMP(15);
    ln = lane_id();
    const u32 ao = WS_ACT + ((t4 * 4 + w) * 112 + 7 * j4) * 1024 + ln * 16;
#pragma unroll
    for (int f = 0; f < 7; ++f) {
      u32x4 o;
#pragma unroll
      for (int k = 0; k < 4; ++k) { const f32x2 a = geglu2((f32x2){ag[f][2 * k], ag[f][2 * k + 1]}, (f32x2){ag[f][8 + 2 * k], ag[f][9 + 2 * k]}, k1, k3, i2); o[k] = pk(a[0], a[1]); }
      __builtin_amdgcn_raw_buffer_store_b128(o, rs, ao + f * 1024, 0, 16);
    }
    WAITV(0);
    if (ln == 0) __hip_atomic_store(FLAG(WS_FA, (t4 * 4 + w) * 16 + j4), tag, RLX_AGENT);
    STAMP(16);
  } else {
    const u32 lane = lane_id(), r32 = lane & 31, hh = lane >> 5;
    const u32 w8o = (lane >> 3) * 1024u + (((lane & 7) ^ (((lane >> 4) & 1) << 2)) << 4);
    const u32 wt0 = (4 * hh + ((lane & 15) >> 2)) * 128 + ((32 * ((lane >> 4) & 1) + 8 * (lane & 3)) ^ (((lane & 15) >> 3) << 6)), wt1 = wt0 ^ 64u;
    auto load_wd = [&](int i) {
      int ks = rot5; PINS(ks);
      ks += i; ks -= ks >= 28 ? 28 : 0;
      const u32 so = cg * 128 + (w4 * 448 + ks * 16) * 1024, ld = lbase + wd_base(i) + w4 * 2048;
      glds(srd_d, w8o, so, ld);
      glds(srd_d, w8o, so + 8192, ld + 1024);
    };
    const u32 a_land = lbase + L_LAND + w4 * 4;
    bf16x8 wp0[11], wp1[11];
    u32 dC0 = w4 * 2048 + wt0, dC1 = w4 * 2048 + wt1, dB0 = dC0 + 92 * KB, dB1 = dC1 + 92 * KB, dA0 = dC0 + 64 * KB, dA1 = dC1 + 64 * KB;
    PIN(dA0); PIN(dA1); PIN(dB0); PIN(dB1); PIN(dC0); PIN(dC1);
#define WD0(i) (wd_base(i) < 64 * KB ? dC0 + wd_base(i) : wd_base(i) < 92 * KB ? dA0 + (wd_base(i) - 64 * KB) : dB0 + (wd_base(i) - 92 * KB))
#define WD1(i) (wd_base(i) < 64 * KB ? dC1 + wd_base(i) : wd_base(i) < 92 * KB ? dA1 + (wd_base(i) - 64 * KB) : dB1 + (wd_base(i) - 92 * KB))
#define WDTURN(i)                                                                                                                   \
  do {                                                                                                                              \
    _Pragma("unroll") for (int k = (i); k < (i) + 2; ++k) { wp0[k] = lds_tr(L, WD0(k), WD0(k) + 1024); wp1[k] = lds_tr(L, WD1(k), WD1(k) + 1024); } \
    _Pragma("unroll") for (int k = (i); k < (i) + 2; ++k) { PIN(wp0[k]); PIN(wp1[k]); }                                             \
    load_wd((i) + 2); load_wd((i) + 3);                                                                                             \
  } while (0)
    load_gu(3, gu_slot(3));
    load_wd(0); load_wd(1);
    WAITV(4); lds_post(a_land, 3);
    lds_wait(L, L_DONE, 1);
    STAMP(13);
    {
      load_gu(4, gu_slot(4));
      const u32x4 srd_w = make_srd(ws, WS_BYTES);
#pragma unroll
      for (int k = 0; k < 5; ++k) glds_sc1(srd_w, lane * 16, WS_X1 + ((t4 * 4 + w4) * 32 + ((27 + k + 4 * rot4) & 31)) * 1024, lbase + L_YL + (w4 * 5 + k) * 1024);
      STAMP4(21);
      lds_wait(L, L_DONE, 2); load_gu(5, gu_slot(5)); WAITV(12); lds_post(a_land, 4); WDTURN(0); STAMP4(22);
      lds_wait(L, L_DONE, 3); load_gu(6, gu_slot(6)); WAITV(11); lds_post(a_land, 5); STAMP4(23);
      lds_wait(L, L_DONE, 4); load_gu(7, gu_slot(7)); WAITV(7); lds_post(a_land, 6); WDTURN(2); WAITV(4); lds_post(a_land, 7); STAMP4(24);
        wait_flags(FLAG(WS_FUV, j4 * 64 + lane), tag);
        {
          u32x4 pt[16];
          const u32 o = lane < 28 ? (w4 * 28 + lane) * 16 : 0x7fffffffu;
#pragma unroll
          for (int k = 0; k < 16; ++k) pt[k] = __builtin_amdgcn_raw_buffer_load_b128(rs, o, WS_UV + (j4 * 16 + k) * 2048, 16);
          SB();
          f32x4 a = {};
#pragma unroll
          for (int k = 0; k < 16; ++k) {
            const f32x4 t_ = __builtin_bit_cast(f32x4, pt[k]);
#pragma unroll
            for (int e = 0; e < 4; ++e) { float y_ = a[e] + t_[e]; asm("" : "+v"(y_)); a[e] = y_; }
          }
          if (lane < 28) {
            const u32 vi = (w4 * 28 + lane) * 4;
#pragma unroll
            for (int e = 0; e < 4; ++e) {
              const float hi = rb(a[e]);
              *(LAS u32*)(L + L_UVT + ((vi >> 6) * 32 + (vi & 31) + e) * 8 + ((vi >> 5) & 1) * 4) = pk(hi, a[e] - hi);
            }
          }
        }
      WDTURN(4);
      {
        const u32 so = (t4 * 128 + 32 * w4 + r32) * 64 + hh * 32;
        const u32x4 s0 = __builtin_amdgcn_raw_buffer_load_b128(rs, so, WS_ST, 16), s1 = __builtin_amdgcn_raw_buffer_load_b128(rs, so + 16, WS_ST, 16);
        auto xsum = [&](float v) {
          auto r_ = __builtin_amdgcn_permlane32_swap(__float_as_uint(v), __float_as_uint(v), false, false);
          return __uint_as_float(r_[0]) + __uint_as_float(r_[1]);
        };
        const float a0 = __uint_as_float(s0[0]), a1 = __uint_as_float(s0[2]), a2 = __uint_as_float(s1[0]), a3 = __uint_as_float(s1[2]);
        const float me = xsum((a0 + a1) + (a2 + a3)) * (1.f / 512.f);
        const float d0 = a0 * (1.f / 64.f) - me, d1 = a1 * (1.f / 64.f) - me, d2 = a2 * (1.f / 64.f) - me, d3 = a3 * (1.f / 64.f) - me;
        const float m2 = (__uint_as_float(s0[1]) + __uint_as_float(s0[3])) + (__uint_as_float(s1[1]) + __uint_as_float(s1[3]));
        float mb, sd;
        ln_scal(me, xsum(m2 + 64.f * ((d0 * d0 + d1 * d1) + (d2 * d2 + d3 * d3))) * (1.f / 512.f), mb, sd);
        if (lane < 32) lds_st128(L, L_BST + (32 * w4 + lane) * 16, (u32x4){pk(-mb, -mb), pk(sd, sd), __float_as_uint(__builtin_amdgcn_rcpf(sd)), 0u});
      }
      lds_post(a_land, 8);
      STAMP4(25);
      lds_wait(L, L_DONE, 5); load_wd(8); load_wd(9); load_wd(10);
      lds_wait(L, L_DONE, 6); load_wd(11); load_wd(12); load_wd(13);
      lds_wait(L, L_DONE, 7); load_wd(14); load_wd(15); load_wd(16); load_wd(17);
      lds_wait(L, L_DONE, 8);
    }
    STAMP(15);
#pragma unroll
    for (int i = 18; i < 23; ++i) load_wd(i);
    WAITV(24);
#pragma unroll
    for (int k = 6; k < 11; ++k) { wp0[k] = lds_tr(L, WD0(k), WD0(k) + 1024); wp1[k] = lds_tr(L, WD1(k), WD1(k) + 1024); }
#pragma unroll
    for (int k = 6; k < 11; ++k) { PIN(wp0[k]); PIN(wp1[k]); }
#pragma unroll
    for (int i = 23; i < 28; ++i) load_wd(i);
    STAMP(16);
    wait_flags(FLAG(WS_FA, (t3 * 4 + 2 * hf + ((lane >> 2) & 1)) * 16 + 4 * w4 + (lane & 3)), tag);
    STAMP(17);
    const u32 ao = WS_ACT + ((t3 * 4 + 2 * hf) * 112 + 28 * w4) * 1024 + lane * 16;
    u32x4 af0[28], af1[28];
    auto ld_act = [&](int i) {
      int ks = i + rot5; ks -= ks >= 28 ? 28 : 0;
      af0[i] = __builtin_amdgcn_raw_buffer_load_b128(rs, ao, ks * 1024, 16);
      af1[i] = __builtin_amdgcn_raw_buffer_load_b128(rs, ao + 112 * 1024, ks * 1024, 16);
    };
#pragma unroll
    for (int i = 0; i < 5; ++i) ld_act(i);
    f32x16 a00 = {}, a01 = {}, a10 = {}, a11 = {};
    static_for<0, 28>([&](auto ic) {
      constexpr int i = ic.value;
      SB();
      a00 = MFMA(wp0[i % 11], asbf(af0[i]), a00);
      a01 = MFMA(wp1[i % 11], asbf(af0[i]), a01);
      SB();
      a10 = MFMA(wp0[i % 11], asbf(af1[i]), a10);
      if constexpr (i < 6) { ld_act(5 + 2 * i); ld_act(6 + 2 * i); }
      else if constexpr (i + 11 < 28) ld_act(i + 11);
      SB();
      a11 = MFMA(wp1[i % 11], asbf(af1[i]), a11);
      if constexpr (i >= 8 && i + 3 < 28) {
        wp0[(i + 3) % 11] = lds_tr(L, WD0(i + 3), WD0(i + 3) + 1024);
        wp1[(i + 3) % 11] = lds_tr(L, WD1(i + 3), WD1(i + 3) + 1024);
      }
      PIN(a00); PIN(a01); PIN(a10); PIN(a11);
    });
    STAMP(18);
    red_write(20 * KB + w4 * 2048, 8192, a00, a01);
    red_write(92 * KB + w4 * 2048, 8192, a10, a11);
  }
  BAR();
  {
    f32x4 lo, hi;
    red_read(mw ? 20 * KB : 60 * KB, 8192, 2048, lo, hi);
    u32 ln = lane_id();
    const f32x4 gb0 = lds_ldf4(L, L_MISC + M_GB + (ln & 7) * 32), gb1 = lds_ldf4(L, L_MISC + M_GB + (ln & 7) * 32 + 16);
    u32x4 o;
    o[0] = pk(bf_lo(x1p[0]) + rb(gb0[0] * rb(lo[0])), bf_hi(x1p[0]) + rb(gb0[1] * rb(lo[1])));
    o[1] = pk(bf_lo(x1p[1]) + rb(gb0[2] * rb(lo[2])), bf_hi(x1p[1]) + rb(gb0[3] * rb(lo[3])));
    o[2] = pk(bf_lo(x1p[2]) + rb(gb1[0] * rb(hi[0])), bf_hi(x1p[2]) + rb(gb1[1] * rb(hi[1])));
    o[3] = pk(bf_lo(x1p[3]) + rb(gb1[2] * rb(hi[2])), bf_hi(x1p[3]) + rb(gb1[3] * rb(hi[3])));
    *(GAS u32x4*)((GAS char*)out + (t3 * 128 + hf * 64 + 8 * w) * 1024 + cg * 128 + (ln >> 3) * 1024 + (ln & 7) * 16) = o;
  }
  STAMP(19);
  asm volatile(".Ladaln_end:");
}

extern "C" void kernel_launch(void* const* d_in, const int* in_sizes, int n_in, void* d_out, int out_size, void* d_ws, size_t ws_size,
                              hipStream_t stream) {
  adaln_block<<<256, 512, 0, stream>>>((const u16*)d_in[0], (const u16*)d_in[1], (const u16*)d_in[2], (const u16*)d_in[4], (const u16*)d_in[5],
                                       (const u16*)d_in[6], (const u16*)d_in[7], (const u16*)d_in[8], (u16*)d_out, (char*)d_ws);
}
```

```cpp
#include <hip/hip_runtime.h>
#include <stdint.h>

typedef unsigned short u16;
typedef unsigned int u32;
typedef unsigned long long u64;
typedef __attribute__((ext_vector_type(8))) __bf16 bf16x8;
typedef __attribute__((ext_vector_type(2))) __bf16 bf16x2;
typedef __attribute__((ext_vector_type(4))) short s16x4;
typedef __attribute__((ext_vector_type(8))) short s16x8;
typedef __attribute__((ext_vector_type(16))) float f32x16;
typedef __attribute__((ext_vector_type(4))) float f32x4;
typedef __attribute__((ext_vector_type(2))) float f32x2;
typedef __attribute__((ext_vector_type(4))) u32 u32x4;
typedef __attribute__((ext_vector_type(2))) u32 u32x2;
#define LAS __attribute__((address_space(3)))
#define GAS __attribute__((address_space(1)))
#define DI __device__ __forceinline__
#define RLX_AGENT __ATOMIC_RELAXED, __HIP_MEMORY_SCOPE_AGENT

constexpr u32 WS_EPOCH = 0;
constexpr u32 WS_ARR = 1024;
constexpr u32 WS_MODG = 4096;
constexpr u32 WS_ST = 65536;
constexpr u32 WS_UV = 196608;
constexpr u32 WS_KV = 1048576;
constexpr u32 WS_O = WS_KV + 4194304;
constexpr u32 WS_X1 = WS_O + 2097152;
constexpr u32 WS_ACT = WS_X1 + 2097152;
constexpr u32 FS = 16, MREP = 8;
constexpr u32 WS_FKV = WS_ACT + 7340032;
constexpr u32 WS_FO = WS_FKV + 1024 * 4 * FS;
constexpr u32 WS_FX = WS_FO + 2048 * 4 * FS;
constexpr u32 WS_FA = WS_FX + 2048 * 4 * FS;
constexpr u32 WS_FUV = WS_FA + 1024 * 4 * FS;
constexpr u32 WS_MOD1 = WS_FUV + 1024 * 4 * FS;
constexpr u32 WS_BYTES = WS_MOD1 + MREP * 4096;
static_assert(WS_UV + 256 * 2048 <= WS_KV && WS_BYTES <= (32u << 20), "workspace map");
#define FLAG(base, i) ((GAS u32*)(ws + (base)) + (i) * FS)

constexpr int SPIN_MAX = 1 << 17;
constexpr u32 KB = 1024;
constexpr u32 L_WO = 0;
constexpr u32 L_KV = 64 * KB;
constexpr u32 L_QX = 144 * KB;
constexpr u32 L_AR1 = 120 * KB;
constexpr u32 L_YL = 0;
constexpr u32 L_MISC = 148 * KB;
constexpr u32 L_C1 = L_MISC, L_SH = L_MISC + 2048, L_C1B = L_MISC + 4096, M_C1B = 4096;
constexpr u32 L_UVT = L_C1, L_BST = L_SH;
constexpr u32 L_ONES = L_MISC + 8192;
constexpr u32 L_GA = L_ONES + 256, M_GA = 8192 + 256, M_GB = 8192 + 512;
constexpr u32 L_DONE = L_ONES + 784, L_LAND = L_ONES + 800, L_MODF = L_ONES + 816;
constexpr u32 L_TAG = L_ONES + 768, L_JUNK = L_ONES + 1536;
constexpr u32 L_RED = L_MISC + 10240;
constexpr u32 LDS_BYTES = 163840;
DI constexpr u32 gu_slot(int c) { return (c & 3) == 3 ? 36 * KB : 64 * KB + (c & 3) * 28 * KB; }
DI constexpr u32 wd_base(int i) {
  return i < 8 ? 20 * KB + (i & 1) * 8192u : i < 11 ? 64 * KB + (i - 8) * 8192u : i < 18 ? 92 * KB + (i - 11) * 8192u : i < 21 ? 36 * KB + (i - 18) * 8192u
       : i < 23 ? (i - 21) * 8192u : i < 25 ? 20 * KB + (i - 23) * 8192u : 64 * KB + (i - 25) * 8192u;
}
constexpr u32 CODE_LINES = 12;

constexpr float NEGBIG = -1e30f;
constexpr float QSCALE = 0.25f * 1.4426950408889634f;

template <int I> struct IC { static constexpr int value = I; };
template <int I, int N, class F> DI void static_for(F&& f) { if constexpr (I < N) { f(IC<I>{}); static_for<I + 1, N>(f); } }
DI u32 lane_id() { u32 r; asm volatile("v_mbcnt_lo_u32_b32 %0, -1, 0\n\tv_mbcnt_hi_u32_b32 %0, -1, %0" : "=v"(r)); return r; }
DI float bf_lo(u32 v) { return __uint_as_float(v << 16); }
DI float bf_hi(u32 v) { return __uint_as_float(v & 0xffff0000u); }
DI u32 pk(float a, float b) { f32x2 v = {a, b}; return __builtin_bit_cast(u32, __builtin_convertvector(v, bf16x2)); }
DI float rb(float a) { return bf_lo(pk(a, a)); }
template <int O> DI u32x4 pack8(const f32x16& a) { return (u32x4){pk(a[O], a[O + 1]), pk(a[O + 2], a[O + 3]), pk(a[O + 4], a[O + 5]), pk(a[O + 6], a[O + 7])}; }
DI bf16x8 asbf(u32x4 v) { return __builtin_bit_cast(bf16x8, v); }
#define MFMA(a, b, c) __builtin_amdgcn_mfma_f32_32x32x16_bf16((a), (b), (c), 0, 0, 0)
template <int CTRL, int RM = 0xf> DI float dpp(float v) { return __builtin_bit_cast(float, __builtin_amdgcn_update_dpp(0, __builtin_bit_cast(int, v), CTRL, RM, 0xf, true)); }
DI float sum8(float v) { v += dpp<0xB1>(v); v += dpp<0x4E>(v); v += dpp<0x141>(v); return v; }
DI float dot2(u32 a, u32 b, float c) { return __builtin_amdgcn_fdot2_f32_bf16(__builtin_bit_cast(bf16x2, a), __builtin_bit_cast(bf16x2, b), c, false); }

DI u32x4 lds_ld128(LAS char* L, u32 off) { return *(LAS u32x4*)(L + off); }
DI f32x4 lds_ldf4(LAS char* L, u32 off) { return *(LAS f32x4*)(L + off); }
DI void lds_st128(LAS char* L, u32 off, u32x4 v) { *(LAS u32x4*)(L + off) = v; }
DI bf16x8 lds_tr(LAS char* L, u32 off0, u32 off1) {
  s16x4 a = __builtin_amdgcn_ds_read_tr16_b64_v4i16((LAS s16x4*)(L + off0));
  s16x4 b = __builtin_amdgcn_ds_read_tr16_b64_v4i16((LAS s16x4*)(L + off1));
  return __builtin_bit_cast(bf16x8, __builtin_shufflevector(a, b, 0, 1, 2, 3, 4, 5, 6, 7));
}
DI u32x4 make_srd(const void* p, u32 bytes) { const u64 a = (u64)p; return (u32x4){(u32)a, (u32)(a >> 32), bytes, 0x00020000u}; }
DI void glds(const u32x4& srd, u32 voff, u32 soff, u32 ldsaddr) {
  asm volatile("s_mov_b32 m0, %3\n\ts_nop 0\n\tbuffer_load_dwordx4 %0, %1, %2 offen lds" ::"v"(voff), "s"(srd), "s"(soff), "s"(ldsaddr) : "memory");
}
DI void glds_sc1(const u32x4& srd, u32 voff, u32 soff, u32 ldsaddr) {
  asm volatile("s_mov_b32 m0, %3\n\ts_nop 0\n\tbuffer_load_dwordx4 %0, %1, %2 offen sc1 lds" ::"v"(voff), "s"(srd), "s"(soff), "s"(ldsaddr) : "memory");
}
DI void touch(const u32x4& srd, u32 voff, u32 ldsaddr) {
  asm volatile("s_mov_b32 m0, %2\n\ts_nop 0\n\tbuffer_load_dword %0, %1, 0 offen lds" ::"v"(voff), "s"(srd), "s"(ldsaddr) : "memory");
}
DI void lds_post(u32 addr, u32 val) {
  asm volatile("s_mov_b64 exec, 1\n\tds_write_b32 %0, %1\n\ts_mov_b64 exec, -1" ::"v"(addr), "v"(val) : "memory");
}
DI bool all_ge(u32x4 v, u32 k) { return __builtin_amdgcn_readfirstlane(min(min(v[0], v[1]), min(v[2], v[3]))) >= k; }
DI void lds_wait(LAS char* L, u32 off, u32 k) {
#pragma nounroll
  for (int it = 0; it < (SPIN_MAX << 4); ++it) {
    if (all_ge(*(volatile LAS u32x4*)(L + off), k)) break;
    __builtin_amdgcn_s_sleep(3);
  }
  asm volatile("" ::: "memory");
}
#define BAR() asm volatile("s_waitcnt lgkmcnt(0)\n\ts_barrier" ::: "memory")
#define WAITV_BAR(N) asm volatile("s_waitcnt vmcnt(%0) lgkmcnt(0)\n\ts_barrier" ::"i"(N) : "memory")
#define WAITV(N) asm volatile("s_waitcnt vmcnt(%0)" ::"i"(N) : "memory")
#define SB() __builtin_amdgcn_sched_barrier(0)
#define PIN(x) asm volatile("" : "+v"(x))
#define PINS(x) asm volatile("" : "+s"(x))
#define ORDER() __builtin_amdgcn_fence(__ATOMIC_ACQUIRE, "wavefront")

DI void wait_flags(const GAS u32* p, u32 tag) {
  u32 v = tag + 1u;
#pragma nounroll
  for (int it = 0; it < SPIN_MAX; ++it) {
    if (v != tag) v = __hip_atomic_load(p, RLX_AGENT);
    if (__all(v == tag)) break;
    __builtin_amdgcn_s_sleep(4);
  }
  ORDER();
}
DI void ln_scal(float mean, float var, float& mb, float& sd) {
  mb = rb(mean);
  sd = rb(__builtin_amdgcn_sqrtf(rb(rb(fmaxf(var, 0.f)) + 1e-5f)));
}
DI float fma1(float a, float b, float c) { float r = fmaf(a, b, c); asm("" : "+v"(r)); return r; }
typedef __attribute__((ext_vector_type(2))) _Float16 f16x2;
DI u32x4 znorm8(u32x4 x, float r, float mr) {
  u32x4 o;
#pragma unroll
  for (int e = 0; e < 4; ++e) {
    const f32x2 v = {fmaf(bf_lo(x[e]), r, -mr), fmaf(bf_hi(x[e]), r, -mr)};
    o[e] = __builtin_bit_cast(u32, __builtin_convertvector(v, f16x2));
  }
  return o;
}
DI u32 xform2(u32 z_, u32 c_, u32 s_) {
  const f16x2 z = __builtin_bit_cast(f16x2, z_), c = __builtin_bit_cast(f16x2, c_), s = __builtin_bit_cast(f16x2, s_);
  return pk(fma1((float)z[0], (float)c[0], (float)s[0]), fma1((float)z[1], (float)c[1], (float)s[1]));
}
DI u32x4 xform8(u32x4 x, u32x4 c, u32x4 s) { return (u32x4){xform2(x[0], c[0], s[0]), xform2(x[1], c[1], s[1]), xform2(x[2], c[2], s[2]), xform2(x[3], c[3], s[3])}; }
DI u32 pkh(float a, float b) { const f32x2 v = {a, b}; return __builtin_bit_cast(u32, __builtin_convertvector(v, f16x2)); }
DI f32x2 geglu2(f32x2 ag, f32x2 au, float k1, float k3, float i2) {
  const f32x2 a = ag * (ag * ag * k3 + k1);
  const f32x2 d = (f32x2){__builtin_amdgcn_exp2f(a[0]), __builtin_amdgcn_exp2f(a[1])} * i2 + i2;
  return ag * au * (f32x2){__builtin_amdgcn_rcpf(d[0]), __builtin_amdgcn_rcpf(d[1])};
}

__global__ __launch_bounds__(512, 2) void adaln_block(const u16* __restrict__ x, const u16* __restrict__ wqkv, const u16* __restrict__ wo,
                                                      const u16* __restrict__ wg, const u16* __restrict__ wu, const u16* __restrict__ wd,
                                                      const u16* __restrict__ cvec, const u16* __restrict__ wm, u16* __restrict__ out,
                                                      char* __restrict__ ws) {
  __shared__ __attribute__((aligned(1024))) char lds[LDS_BYTES];
  LAS char* L = (LAS char*)lds;
  const u32 lbase = (u32)(uintptr_t)L;
  const int tid = threadIdx.x, lane = tid & 63;
  const int w = __builtin_amdgcn_readfirstlane(tid >> 6), w4 = w & 3;
  const bool mw = w < 4;
  const int b = blockIdx.x, xcd = b & 7, ii = b >> 3;
  if (w == 7) {
    u64 pc;
    int len;
    asm volatile("s_getpc_b64 %0\n.Ladaln_pf:\n\ts_mov_b32 %1, .Ladaln_end-.Ladaln_pf" : "=s"(pc), "=s"(len));
    touch(make_srd((const void*)pc, len > 0 ? (u32)len : 0u), ((lane < CODE_LINES ? lane : 0) * 32 + ii) * 128u, lbase + L_JUNK);
  }
  const int u1 = 31 - (8 * (xcd >> 1) + (ii >> 2)), q1 = 4 * (xcd & 1) + (ii & 3);
  const int t3 = 2 * xcd + (ii >> 4), hf = (ii >> 3) & 1, cg = ii & 7;
  const int t4 = 8 * (xcd >> 2) + (ii >> 2), j4 = 4 * (xcd & 3) + (ii & 3);
  const int rot1 = (u1 + 2 * q1) & 7;
  const int rot4 = ii >> 2, rot5 = 7 * (ii >> 3);
  const int r32 = lane & 31, hh = lane >> 5, blk = (lane >> 4) & 1, q4 = (lane & 15) >> 2, p4 = lane & 3, l3 = lane >> 3, l7 = lane & 7;
  const __amdgpu_buffer_rsrc_t rs = __builtin_amdgcn_make_buffer_rsrc(ws, 0, WS_BYTES, 0x00020000);
  u32 tag = 0;
  const int orow = t3 * 128 + hf * 64 + 8 * w + l3;
  const int mg = b;
  const u32x4 srd_q = make_srd(wqkv, 512 * 3072);
  const u32 q8o = l3 * 3072u + ((l7 ^ (((lane >> 4) & 1) << 2)) << 4);
  auto load_wq = [&](int c) {
    const u32 so = ((c + rot1) & 7) * 196608 + w * 1024 + q1 * 128;
#pragma unroll
    for (int e = 0; e < 8; ++e) glds(srd_q, q8o, so + e * 24576, lbase + (c % 5) * 24576 + w * 8192 + e * 1024);
  };
  u32x4 X[16];
  u32x4 xr;
  const int hp = w4 >> 1, hd = w >> 2, qb = (w ^ hd) & 1;
  bf16x8 qf_[15], kf_[15], vf_[15];
  u32 wl[3];
#define P1O(n) (wl[(((n) >> 2) % 5) >> 1] + ((((n) >> 2) % 5) & 1) * 24576 + ((n) & 3) * 2048)
#define P1I(n) ((n) < 12 ? (n) : 12 + (n) % 3)
  u32 cso = L_MISC + l7 * 32;
  PIN(cso);
  const u32x4 srd_g = make_srd(wg, 512 * 3584), srd_u = make_srd(wu, 512 * 3584), srd_d = make_srd(wd, 1792 * 1024), srd_o = make_srd(wo, 512 * 1024);

  u32 guo[7];
  const u32x4 srd_3 = w4 < 2 ? srd_g : srd_u;
  auto load_gu = [&](int c, u32 slot) {
    int r_ = rot4; PINS(r_);
    const u32 so = j4 * 224 + ((c + r_) & 7) * (64 * 3584), ld = lbase + slot + w4 * 1024;
#pragma unroll
    for (int n = 0; n < 7; ++n) glds(n < 3 ? srd_g : n == 3 ? srd_3 : srd_u, guo[n], so, ld + n * 4096);
  };
  const u32 w8o = l3 * 1024u + ((l7 ^ (((lane >> 4) & 1) << 2)) << 4);
  bf16x8 qf;
  if (mw) {
    u32x4 M[16];
    u32 cv = 0;
    if (mg < 48) {
      const GAS char* mb = (const GAS char*)wm + mg * 128 + (128 * w + 16 * l3) * 6144 + l7 * 16;
      cv = ((const GAS u32*)cvec)[64 * w + lane];
#pragma unroll
      for (int n = 0; n < 16; ++n) M[n] = *(const GAS u32x4*)(mb + n * 6144);
    }
    const u32 ep = __hip_atomic_load((const GAS u32*)(ws + WS_EPOCH), RLX_AGENT);
    if (lane < 12) *(LAS u32*)(L + L_DONE + lane * 4) = 0u;
    SB();
    BAR();
    if (mg < 48) {
      {
        const float c0 = bf_lo(cv), c1 = bf_hi(cv);
        *(LAS u32*)(L + L_RED + 1024 + w * 256 + lane * 4) = pk(c0 * __builtin_amdgcn_rcpf(1.0f + __builtin_amdgcn_exp2f(-1.4426950408889634f * c0)),
                                                               c1 * __builtin_amdgcn_rcpf(1.0f + __builtin_amdgcn_exp2f(-1.4426950408889634f * c1)));
      }
      const u32x4 s0 = lds_ld128(L, L_RED + 1024 + w * 256 + l3 * 32), s1 = lds_ld128(L, L_RED + 1024 + w * 256 + l3 * 32 + 16);
      f32x2 a[4] = {};
#pragma unroll
      for (int n = 0; n < 8; ++n) {
        const u32 sp = n < 4 ? s0[n & 3] : s1[n & 3];
#pragma unroll
        for (int e = 0; e < 4; ++e) {
          const u32 r0 = M[2 * n][e], r1 = M[2 * n + 1][e];
          a[e][0] = dot2(__builtin_amdgcn_perm(r1, r0, 0x05040100u), sp, a[e][0]);
          a[e][1] = dot2(__builtin_amdgcn_perm(r1, r0, 0x07060302u), sp, a[e][1]);
        }
      }
      float v[8] = {a[0][0], a[0][1], a[1][0], a[1][1], a[2][0], a[2][1], a[3][0], a[3][1]};
#pragma unroll
      for (int e = 0; e < 8; ++e) {
        v[e] += dpp<0x128>(v[e]);
        auto r1_ = __builtin_amdgcn_permlane16_swap(__float_as_uint(v[e]), __float_as_uint(v[e]), false, false);
        v[e] = __uint_as_float(r1_[0]) + __uint_as_float(r1_[1]);
        auto r2_ = __builtin_amdgcn_permlane32_swap(__float_as_uint(v[e]), __float_as_uint(v[e]), false, false);
        v[e] = __uint_as_float(r2_[0]) + __uint_as_float(r2_[1]);
      }
      if (lane < 8) {
        *(LAS f32x4*)(L + L_RED + w * 256 + lane * 32) = (f32x4){v[0], v[1], v[2], v[3]};
        *(LAS f32x4*)(L + L_RED + w * 256 + lane * 32 + 16) = (f32x4){v[4], v[5], v[6], v[7]};
      }
      lds_post(lbase + L_MODF + w * 4, 1);
      if (w == 3) {
        lds_wait(L, L_MODF, 1);
        float s = 0.f;
#pragma unroll
        for (int k = 0; k < 4; ++k) s += *(LAS float*)(L + L_RED + k * 256 + lane * 4);
        const float o = dpp<0xB1>(s);
        const u32 tg = __builtin_amdgcn_readfirstlane(ep) + 1u;
        const u64 gr = ((u64)tg << 32) | ((lane & 1) ? pk(o, s) : pk(s, o));
        if (mg < 16) {
#pragma unroll
          for (int r = 0; r < MREP / 2 + (MREP & 1); ++r)
            if (2 * r + (lane & 1) < MREP) __hip_atomic_store((GAS u64*)(ws + WS_MOD1) + (2 * r + (lane & 1)) * 512 + 32 * mg + (lane >> 1), gr, RLX_AGENT);
        } else if (!(lane & 1)) __hip_atomic_store((GAS u64*)(ws + WS_MODG) + 32 * mg + (lane >> 1), gr, RLX_AGENT);
      }
    }
    xr = *(const GAS u32x4*)((const GAS char*)x + orow * 1024 + cg * 128 + l7 * 16);
    tag = __builtin_amdgcn_readfirstlane(ep) + 1u;
    if (w < 3) {
      load_wq(0); load_wq(1); load_wq(2); load_wq(3); load_wq(4);
      WAITV(16);
    }
    lds_post(lbase + L_MODF + w * 4, 2);
    wl[0] = (8 * hh + q4) * 128 + ((64 * hp + 32 * blk + 8 * p4) ^ ((q4 >> 1) << 6));
    wl[1] = wl[0] + 48 * KB; wl[2] = wl[0] + 96 * KB;
    PIN(wl[1]); PIN(wl[2]);
    lds_wait(L, L_MODF, 2);
#pragma unroll
    for (int n = 0; n < 12; ++n) { kf_[n] = lds_tr(L, P1O(n) + 8192, P1O(n) + 8704); vf_[n] = lds_tr(L, P1O(n) + 16384, P1O(n) + 16896); qf_[n] = lds_tr(L, P1O(n), P1O(n) + 512); }
    lds_post(lbase + L_MODF + w * 4, 3);
#pragma unroll
    for (int n = 0; n < 12; ++n) { PIN(kf_[n]); PIN(vf_[n]); PIN(qf_[n]); }
    if (w < 3) {
      lds_wait(L, L_MODF, 3);
      load_wq(5); load_wq(6); load_wq(7);
    } else {
      if (lane < 32) lds_st128(L, L_ONES + (lane & 15) * 16 + (lane >> 4) * 1024, (u32x4){0x3f803f80u, 0x3f803f80u, 0x3f803f80u, 0x3f803f80u});
      if (lane == 0) *(LAS u32*)(L + L_TAG) = tag;
      u32x4 v[4];
#pragma nounroll
      for (int it = 0; it < SPIN_MAX; ++it) {
        bool ok = true;
#pragma unroll
        for (int k = 0; k < 4; ++k) { v[k] = __builtin_amdgcn_raw_buffer_load_b128(rs, lane * 16, WS_MOD1 + (xcd % MREP) * 4096 + k * 1024, 16); ok &= v[k][1] == tag && v[k][3] == tag; }
        if (__all(ok)) break;
        __builtin_amdgcn_s_sleep(2);
      }
#pragma unroll
      for (int k = 0; k < 2; ++k) {
        const u32 o = L_C1 + ((lane + 64 * k) >> 1) * 32 + (lane & 1) * 8;
        *(LAS u32x2*)(L + o + 16) = (u32x2){pkh(bf_lo(v[k][0]), bf_hi(v[k][0])), pkh(bf_lo(v[k][2]), bf_hi(v[k][2]))};
        *(LAS u32x2*)(L + o) = (u32x2){pkh(rb(1.f + bf_lo(v[2 + k][0])), rb(1.f + bf_hi(v[2 + k][0]))), pkh(rb(1.f + bf_lo(v[2 + k][2])), rb(1.f + bf_hi(v[2 + k][2])))};
      }
    }
    BAR();
    f32x16 aq = {}, ak = {}, av = {};
    u32 ard[4];
    {
      const int row = 32 * qb + r32, f = (row & 7) ^ ((row >> 3) & 1);
#pragma unroll
      for (int s = 0; s < 4; ++s) { ard[s] = L_AR1 + row * 128 + ((((2 * s) | hh) ^ f) << 4); PIN(ard[s]); }
    }
    bf16x8 hf_[3];
#define P1H(n) hf_[(n) % 3] = asbf(lds_ld128(L, ard[(n) & 3] + (((n) >> 2) % 3) * 8192))
    BAR();
    P1H(0); P1H(1);
    static_for<0, 32>([&](auto nc) {
      constexpr int n = nc.value, m = n + 2;
      constexpr bool bnd = (m & 3) == 0, rd = m >= 12 && m < 32;
      SB();
      ak = MFMA(kf_[P1I(n)], hf_[n % 3], ak);
      if constexpr (m < 32 && !bnd) { P1H(m); if constexpr (rd) kf_[P1I(m)] = lds_tr(L, P1O(m) + 8192, P1O(m) + 8704); }
      SB();
      av = MFMA(hf_[n % 3], vf_[P1I(n)], av);
      if constexpr (m < 32 && bnd) {
        PIN(ak); PIN(av);
        WAITV_BAR(m >= 12 ? 56 - 2 * m : 63);
        P1H(m);
        if constexpr (rd) kf_[P1I(m)] = lds_tr(L, P1O(m) + 8192, P1O(m) + 8704);
      }
      if constexpr (rd) vf_[P1I(m)] = lds_tr(L, P1O(m) + 16384, P1O(m) + 16896);
      SB();
      aq = MFMA(qf_[P1I(n)], hf_[n % 3], aq);
      if constexpr (rd) qf_[P1I(m)] = lds_tr(L, P1O(m), P1O(m) + 512);
      PIN(ak); PIN(av); PIN(aq);
    });
    const u32x4 f0 = pack8<0>(ak), f1 = pack8<8>(ak), f2 = pack8<0>(av), f3 = pack8<8>(av);
    const int kvi = ((u1 >> 1) * 16 + 2 * q1 + hp) * 4 + 2 * (u1 & 1) + qb;
    const u32 o = WS_KV + kvi * 4096 + lane * 16;
    __builtin_amdgcn_raw_buffer_store_b128(f0, rs, o, 0, 16);
    __builtin_amdgcn_raw_buffer_store_b128(f1, rs, o + 1024, 0, 16);
    __builtin_amdgcn_raw_buffer_store_b128(f2, rs, o + 2048, 0, 16);
    __builtin_amdgcn_raw_buffer_store_b128(f3, rs, o + 3072, 0, 16);
    aq *= QSCALE;
    qf = asbf(pack8<0>(aq));
    const u32x4 f5 = pack8<8>(aq);
    BAR();
    const u32 ok = L_KV + hp * 40960 + (8 + qb) * 4096 + lane * 16;
    lds_st128(L, ok, f0); lds_st128(L, ok + 1024, f1); lds_st128(L, ok + 2048, f2); lds_st128(L, ok + 3072, f3);
    lds_st128(L, L_QX + w * 1024 + lane * 16, f5);
    BAR();
    WAITV(0);
    if (lane == 0) __hip_atomic_store(FLAG(WS_FKV, kvi), tag, RLX_AGENT);
  } else {
    const __amdgpu_buffer_rsrc_t rx = __builtin_amdgcn_make_buffer_rsrc((void*)x, 0, 2048 * 1024, 0x00020000);
    if (mg < 48) BAR();
#pragma unroll
    for (int c = 0; c < 8; ++c)
#pragma unroll
      for (int g = 0; g < 2; ++g)
        X[2 * c + g] = __builtin_amdgcn_raw_buffer_load_b128(rx, l3 * 1024u + l7 * 16u, (u1 * 64 + 16 * w4 + 8 * g) * 1024 + ((c + rot1) & 7) * 128, 0);
    xr = *(const GAS u32x4*)((const GAS char*)x + orow * 1024 + cg * 128 + l7 * 16);
    SB();
    if (mg >= 48) BAR();
    float sm[2] = {}, sq[2] = {};
#pragma unroll
    for (int c = 0; c < 8; ++c) {
      SB();
#pragma unroll
      for (int e = 0; e < 4; ++e)
#pragma unroll
        for (int g = 0; g < 2; ++g) { sm[g] = dot2(X[2 * c + g][e], 0x3f803f80u, sm[g]); sq[g] = dot2(X[2 * c + g][e], X[2 * c + g][e], sq[g]); }
    }
    float rr[2], mr[2];
#pragma unroll
    for (int g = 0; g < 2; ++g) {
      const float me = sum8(sm[g]) * (1.f / 512.f);
      float mb, sd;
      ln_scal(me, sum8(sq[g]) * (1.f / 512.f) - me * me, mb, sd);
      rr[g] = __builtin_amdgcn_rcpf(sd); mr[g] = mb * rr[g];
    }
#pragma unroll
    for (int n = 0; n < 16; ++n) { X[n] = znorm8(X[n], rr[n & 1], mr[n & 1]); PIN(X[n]); }
    if (w4 < 3) {
      const u32x4 srd_a = make_srd(w4 == 0 ? wo : w4 == 1 ? wg : wu, w4 == 0 ? 512 * 1024 : 512 * 3584);
      const int e1 = 64 + lane;
      const u32 ta0 = w4 ? (16 * ii + lane / 7) * 3584 + (xcd & 3) * 896 + (lane % 7) * 128 : ii * 16384 + lane * 128;
      const u32 ta1 = w4 ? (lane < 48 ? (16 * ii + e1 / 7) * 3584 + (xcd & 3) * 896 + (e1 % 7) * 128 : 0x7fffffffu) : ta0 + 8192;
      touch(srd_a, ta0, lbase + L_JUNK);
      touch(srd_a, ta1, lbase + L_JUNK);
    }
    BAR();
    tag = __builtin_amdgcn_readfirstlane(*(LAS u32*)(L + L_TAG));
    if (w == 4 && lane == 0) __hip_atomic_store((GAS u32*)(ws + WS_ARR) + b, tag, RLX_AGENT);
    const u32 awE = L_AR1 + (16 * w4 + l3) * 128 + ((l7 ^ l3) << 4), awO = (awE ^ 16u) + 1024;
    u32x4 ca[2], sa[2];
#define CSLOAD(c) do { const u32 o_ = cso + (((c) + rot1) & 7) * 256; ca[(c) & 1] = lds_ld128(L, o_); sa[(c) & 1] = lds_ld128(L, o_ + 16); } while (0)
    CSLOAD(0);
#pragma unroll
    for (int c = 0; c < 8; ++c) {
      if (c < 7) CSLOAD(c + 1);
      SB();
      PIN(X[2 * c]);
      lds_st128(L, awE + (c % 3) * 8192, xform8(X[2 * c], ca[c & 1], sa[c & 1]));
      PIN(X[2 * c + 1]);
      lds_st128(L, awO + (c % 3) * 8192, xform8(X[2 * c + 1], ca[c & 1], sa[c & 1]));
      if (c == 0) BAR();
      if (c >= 2) asm volatile("s_waitcnt lgkmcnt(2)\n\ts_barrier" ::: "memory");
    }
    BAR();
#pragma unroll
    for (int n = 0; n < 7; ++n) {
      const int pc = w4 + 4 * n, up = pc >= 14, e = (pc - 14 * up) * 64 + lane, rho = e / 14, cc = e - 14 * rho;
      guo[n] = (u32)((up ? (rho ^ 4) : rho) * 3584 + cc * 16);
    }
    BAR();
    BAR();
    qf = asbf(lds_ld128(L, L_QX + (w4 ^ 1) * 1024 + lane * 16));

  }
  PIN(xr);
  if (!mw && qb) {
    for (int n = hp * 32; n < hp * 32 + 32; ++n) glds(srd_o, w8o, cg * 128 + n * 8192, lbase + L_WO + n * 1024);
  }

  {
    const u32 ka = L_KV + hp * 40960 + hd * 1024 + lane * 16;
    const bool vdat = (r32 >> 4) == hd;
    const u32 va = vdat ? L_KV + hp * 40960 + 2048 + lane * 16 : L_ONES + (lane & 15) * 16;
    const u32 vs = vdat ? 4096u : 0u;
    f32x16 cm;
#pragma unroll
    for (int g = 0; g < 16; ++g) cm[g] = (g & 3) + 8 * (g >> 2) + 4 * hh <= r32 ? 0.f : NEGBIG;
    const f32x16 zero = {};
    f32x16 O = {};
    float m = NEGBIG;
    auto rowmax = [&](const f32x16& s, float mm) {
#pragma unroll
      for (int g = 0; g < 16; g += 2) mm = fmaxf(fmaxf(mm, s[g]), s[g + 1]);
      return mm;
    };
    auto ldk = [&](int lb) { return asbf(lds_ld128(L, ka + lb * 4096)); };
#pragma nounroll
    for (int st = 0; st < 2; ++st) {
      int lm = 8 + qb, lo = 8, hi = 8 + qb;
      if (st) {
        if (u1 == 0) break;
        if (!qb) {
          const int g0 = 2 * u1 - 8 + 4 * hd, gl = g0 + (lane & 3);
          wait_flags(FLAG(WS_FKV, gl < 0 ? ((u1 >> 1) * 16 + 2 * q1 + hp) * 4 + 2 * (u1 & 1) : ((gl >> 2) * 16 + 2 * q1 + hp) * 4 + (gl & 3)), tag);
          const u32x4 srd_w = make_srd(ws, WS_BYTES);
#pragma unroll
          for (int k = 0; k < 4; ++k) {
            const int g = g0 + k;
            const u32 so = g < 0 ? 0x7ff00000u : WS_KV + (((g >> 2) * 16 + 2 * q1 + hp) * 4 + (g & 3)) * 4096;
#pragma unroll
            for (int n = 0; n < 4; ++n) glds_sc1(srd_w, lane * 16, so + n * 1024, lbase + L_KV + hp * 40960 + hd * 16384 + (4 * k + n) * 1024);
          }
          WAITV(0);
        }
        const int lmin = 8 - 2 * u1;
#pragma unroll
        for (int g = 0; g < 16; ++g) { float u = lmin > qb ? NEGBIG : NEGBIG - cm[g]; asm("" : "+v"(u)); cm[g] = u; }
        lm = qb; lo = lmin > qb ? lmin : qb + 1; hi = 8;
        BAR();
      }
      bf16x8 kA = ldk(lo), kB = ldk(lo + 1);
      f32x16 sA = MFMA(ldk(lm), qf, cm), sB, sC;
      const bf16x8 k0 = kA;
      float mn = m;
      for (int lb = lo; lb < hi; lb += 2) {
        sB = MFMA(kA, qf, zero);
        sC = MFMA(kB, qf, zero);
        kA = ldk(lb + 2); kB = ldk(lb + 3);
        SB();
        mn = rowmax(sB, mn);
        if (lb + 1 < hi) mn = rowmax(sC, mn);
      }
      mn = rowmax(sA, mn);
      {
        auto r_ = __builtin_amdgcn_permlane32_swap(__float_as_uint(mn), __float_as_uint(mn), false, false);
        mn = fmaxf(__uint_as_float(r_[0]), __uint_as_float(r_[1]));
      }
      u32 vv = va + lm * vs;
      bf16x8 v0 = asbf(lds_ld128(L, vv)), v1 = asbf(lds_ld128(L, vv + 1024));
      f32x16 negm;
#pragma unroll
      for (int g = 0; g < 16; ++g) negm[g] = -mn;
#pragma unroll
      for (int g = 0; g < 16; ++g) { float u = sA[g] - mn; asm("" : "+v"(u)); sA[g] = u; }
      if (st) {
        const float al = __builtin_amdgcn_exp2f(m - mn);
#pragma unroll
        for (int g = 0; g < 16; ++g) { float o = O[g] * al; asm("" : "+v"(o)); O[g] = o; }
      }
      m = mn;
      kA = k0;
      vv = va + lo * vs;
#define P2STEP(CUR, NXT)                                                                      \
  {                                                                                           \
    NXT = MFMA(kA, qf, negm);                                                                 \
    PIN(NXT);                                                                                 \
    kA = ldk(lb + 1);                                                                         \
    const bf16x8 n0_ = asbf(lds_ld128(L, vv)), n1_ = asbf(lds_ld128(L, vv + 1024));           \
    vv += vs;                                                                                 \
    SB();                                                                                     \
    _Pragma("unroll") for (int g = 0; g < 8; ++g) CUR[g] = __builtin_amdgcn_exp2f(CUR[g]);    \
    O = MFMA(v0, asbf(pack8<0>(CUR)), O);                                                     \
    _Pragma("unroll") for (int g = 8; g < 16; ++g) CUR[g] = __builtin_amdgcn_exp2f(CUR[g]);   \
    O = MFMA(v1, asbf(pack8<8>(CUR)), O);                                                     \
    SB();                                                                                     \
    v0 = n0_; v1 = n1_;                                                                       \
  }
      for (int lb = lo;; ++lb) {
        P2STEP(sA, sB);
        if (lb >= hi) break;
        ++lb;
        P2STEP(sB, sA);
        if (lb >= hi) break;
      }
    }
    u32x4 of;
    {
      const float inv = __builtin_amdgcn_rcpf(hd ? O[0] : O[8]);
      float o[8];
#pragma unroll
      for (int g = 0; g < 8; ++g) { o[g] = (hd ? O[8 + g] : O[g]) * inv; asm("" : "+v"(o[g])); }
      of = (u32x4){pk(o[0], o[1]), pk(o[2], o[3]), pk(o[4], o[5]), pk(o[6], o[7])};
    }
    const int oi = (2 * u1 + qb) * 32 + 4 * q1 + 2 * hp + hd;
    __builtin_amdgcn_raw_buffer_store_b128(of, rs, WS_O + oi * 1024 + lane * 16, 0, 16);
    WAITV(0);
    if (lane == 0) __hip_atomic_store(FLAG(WS_FO, oi), tag, RLX_AGENT);
  }
  {
    if (u1 == 0 && q1 == 0 && w == 7) {
#pragma nounroll
      for (int it = 0; it < SPIN_MAX; ++it) {
        bool ok = true;
#pragma unroll
        for (int k = 0; k < 4; ++k) ok &= __hip_atomic_load((const GAS u32*)(ws + WS_ARR) + lane + 64 * k, RLX_AGENT) == tag;
        if (__all(ok)) break;
        __builtin_amdgcn_s_sleep(1);
      }
      if (lane == 0) __hip_atomic_store((GAS u32*)(ws + WS_EPOCH), tag, RLX_AGENT);
    }
  }
  BAR();

  const u32 wt0 = (4 * hh + q4) * 128 + ((32 * blk + 8 * p4) ^ ((q4 >> 1) << 6)), wt1 = wt0 ^ 64u;
  auto red_write = [&](u32 base, u32 S8, const f32x16& a0, const f32x16& a1) {
    u32 ln = lane_id();
    const u32 o = base + ((ln & 31) >> 3) * S8 + (ln & 7) * 256, sw = (ln & 7) ^ (ln >> 5);
#pragma unroll
    for (int g = 0; g < 4; ++g) {
      *(LAS f32x4*)(L + o + (((2 * g) ^ sw) << 4)) = (f32x4){a0[4 * g], a0[4 * g + 1], a0[4 * g + 2], a0[4 * g + 3]};
      *(LAS f32x4*)(L + o + (((2 * g) ^ sw) << 4) + 128) = (f32x4){a1[4 * g], a1[4 * g + 1], a1[4 * g + 2], a1[4 * g + 3]};
    }
  };
  auto red_read = [&](u32 base, u32 S8, u32 SK, f32x4& lo, f32x4& hi) {
    u32 ln = lane_id();
    const u32 o0 = base + w * S8 + (ln >> 3) * 256 + (((2 * (ln & 7)) ^ (ln >> 3)) << 4), o1 = o0 ^ 16u;
    lo = lds_ldf4(L, o0); hi = lds_ldf4(L, o1);
#pragma unroll
    for (int k = 1; k < 4; ++k) { lo += lds_ldf4(L, o0 + k * SK); hi += lds_ldf4(L, o1 + k * SK); }
  };
  u32x4 x1p;
  {
    if (mw) {
      bf16x8 wa[8], wb[8];
#pragma unroll
      for (int i = 0; i < 8; ++i) {
        const u32 o = L_WO + w * 16384 + i * 2048;
        wa[i] = lds_tr(L, o + wt0, o + wt0 + 1024); wb[i] = lds_tr(L, o + wt1, o + wt1 + 1024);
      }
      wait_flags(FLAG(WS_FO, (t3 * 4 + 2 * hf + (l3 & 1)) * 32 + 8 * w + l7), tag);
      u32x4 of0[8], of1[8];
      const u32 oo = WS_O + ((t3 * 4 + 2 * hf) * 32 + 8 * w) * 1024 + lane * 16;
#pragma unroll
      for (int i = 0; i < 8; ++i) {
        of0[i] = __builtin_amdgcn_raw_buffer_load_b128(rs, oo + i * 1024, 0, 16);
        of1[i] = __builtin_amdgcn_raw_buffer_load_b128(rs, oo + 32768 + i * 1024, 0, 16);
      }
      SB();
      f32x16 a00 = {}, a01 = {}, a10 = {}, a11 = {};
#pragma unroll
      for (int i = 0; i < 8; ++i) {
        a00 = MFMA(wa[i], asbf(of0[i]), a00); a01 = MFMA(wb[i], asbf(of0[i]), a01);
        a10 = MFMA(wa[i], asbf(of1[i]), a10); a11 = MFMA(wb[i], asbf(of1[i]), a11);
      }
      red_write(L_WO + w * 16384, 2048, a00, a01);
      red_write(L_WO + w * 16384 + 8192, 2048, a10, a11);
    } else {
      load_gu(0, gu_slot(0));
      u32x4 bv[2];
      {
        u32x4 q[4];
        u32 v[3];
        const GAS u64* g = (const GAS u64*)(ws + WS_MODG);
        const u32 qo = (((r32 & 1) ? 768 : 1024) + rot4 * 32 + (t4 >> 3) * 16 + 4 * hh) * 8;
#pragma nounroll
        for (int it = 0; it < SPIN_MAX; ++it) {
          const u64 x1_ = __hip_atomic_load(g + 1024 + (tid - 256), RLX_AGENT);
          const u64 x2 = __hip_atomic_load(g + (lane < 32 ? 512 : 1248) + cg * 32 + lane, RLX_AGENT);
          bool ok = (u32)(x1_ >> 32) == tag && (u32)(x2 >> 32) == tag;
#pragma unroll
          for (int k = 0; k < 4; ++k) { q[k] = __builtin_amdgcn_raw_buffer_load_b128(rs, qo + (k >> 1) * 64 + (k & 1) * 16, WS_MODG, 16); ok &= q[k][1] == tag && q[k][3] == tag; }
          v[1] = (u32)x1_; v[2] = (u32)x2;
          if (__all(ok)) break;
          __builtin_amdgcn_s_sleep(1);
        }
        *(LAS f32x2*)(L + L_C1B + (tid - 256) * 8) = (f32x2){rb(1.f + bf_lo(v[1])), rb(1.f + bf_hi(v[1]))};
        if (w == 4) *(LAS f32x2*)(L + L_GA + lane * 8) = (f32x2){bf_lo(v[2]), bf_hi(v[2])};
        const float one = (r32 & 1) ? 0.f : 1.f;
#pragma unroll
        for (int s = 0; s < 2; ++s)
          bv[s] = (u32x4){pk(one + bf_lo(q[2 * s][0]), one + bf_hi(q[2 * s][0])), pk(one + bf_lo(q[2 * s][2]), one + bf_hi(q[2 * s][2])),
                          pk(one + bf_lo(q[2 * s + 1][0]), one + bf_hi(q[2 * s + 1][0])), pk(one + bf_lo(q[2 * s + 1][2]), one + bf_hi(q[2 * s + 1][2]))};
      }
      lds_post(lbase + L_LAND + w4 * 4, 1);
      load_gu(1, gu_slot(1));
      lds_wait(L, L_LAND, 1);
      {
        const u32 gl = blk * 14336 + (8 * hh + q4) * 224 + 8 * p4 + w4 * 32 + (t4 >> 3) * 7168 + gu_slot(0);
        const u32 g0 = gl + (blk ? 896u : 0u), g1 = gl + (blk ? 0u : 896u);
        f32x16 a0 = {}, a1 = {};
#pragma unroll
        for (int s = 0; s < 2; ++s) {
          a0 = MFMA(lds_tr(L, g0 + s * 3584, g1 + s * 3584), asbf(bv[s]), a0);
          a1 = MFMA(lds_tr(L, g0 + s * 3584 + 128, g1 + s * 3584 + 128), asbf(bv[s]), a1);
        }
        if (r32 < 2) {
          const u32 o = WS_UV + (j4 * 16 + t4) * 2048 + w4 * 256 + r32 * 128 + hh * 16;
#pragma unroll
          for (int g = 0; g < 4; ++g) {
            __builtin_amdgcn_raw_buffer_store_b128(__builtin_bit_cast(u32x4, (f32x4){a0[4 * g], a0[4 * g + 1], a0[4 * g + 2], a0[4 * g + 3]}), rs, o + g * 32, 0, 16);
            if (w4 < 3) __builtin_amdgcn_raw_buffer_store_b128(__builtin_bit_cast(u32x4, (f32x4){a1[4 * g], a1[4 * g + 1], a1[4 * g + 2], a1[4 * g + 3]}), rs, o + 1024 + g * 32, 0, 16);
          }
        }
      }
      load_gu(2, gu_slot(2));
    }
    BAR();
    f32x4 lo, hi;
    red_read(L_WO, 2048, 16384, lo, hi);
    const f32x4 ga0 = lds_ldf4(L, cso + M_GA), ga1 = lds_ldf4(L, cso + M_GA + 16);
    float v[8];
    v[0] = rb(bf_lo(xr[0]) + rb(ga0[0] * rb(lo[0]))); v[1] = rb(bf_hi(xr[0]) + rb(ga0[1] * rb(lo[1])));
    v[2] = rb(bf_lo(xr[1]) + rb(ga0[2] * rb(lo[2]))); v[3] = rb(bf_hi(xr[1]) + rb(ga0[3] * rb(lo[3])));
    v[4] = rb(bf_lo(xr[2]) + rb(ga1[0] * rb(hi[0]))); v[5] = rb(bf_hi(xr[2]) + rb(ga1[1] * rb(hi[1])));
    v[6] = rb(bf_lo(xr[3]) + rb(ga1[2] * rb(hi[2]))); v[7] = rb(bf_hi(xr[3]) + rb(ga1[3] * rb(hi[3])));
    x1p = (u32x4){pk(v[0], v[1]), pk(v[2], v[3]), pk(v[4], v[5]), pk(v[6], v[7])};
    PIN(x1p);
    {
      const f32x4 c0 = lds_ldf4(L, cso + M_C1B + cg * 256), c1 = lds_ldf4(L, cso + M_C1B + cg * 256 + 16);
      const u32x4 y = {pk(v[0] * c0[0], v[1] * c0[1]), pk(v[2] * c0[2], v[3] * c0[3]), pk(v[4] * c1[0], v[5] * c1[1]), pk(v[6] * c1[2], v[7] * c1[3])};
      __builtin_amdgcn_raw_buffer_store_b128(y, rs, WS_X1 + ((t3 * 4 + 2 * hf + (w >> 2)) * 32 + 4 * cg + (l7 >> 1)) * 1024 + ((l7 & 1) * 32 + 8 * w4 + l3) * 16, 0, 16);
    }
    const float sm = sum8(((v[0] + v[1]) + (v[2] + v[3])) + ((v[4] + v[5]) + (v[6] + v[7])));
    const float mu = sm * (1.f / 64.f);
    float m2 = 0.f;
#pragma unroll
    for (int k = 0; k < 8; ++k) m2 = fmaf(v[k] - mu, v[k] - mu, m2);
    m2 = sum8(m2);
    if (l7 == 0) __hip_atomic_store((GAS u64*)(ws + WS_ST) + orow * 8 + cg, ((u64)__float_as_uint(m2) << 32) | __float_as_uint(sm), RLX_AGENT);
    WAITV(0);
    if (lane == 0) __hip_atomic_store(FLAG(WS_FX, (t3 * 4 + 2 * hf + (w >> 2)) * 32 + cg * 4 + w4), tag, RLX_AGENT);
    if (!mw && lane == 1) __hip_atomic_store(FLAG(WS_FUV, j4 * 64 + t4 * 4 + w4), tag, RLX_AGENT);
  }
  BAR();

  if (mw) {
    u32 ln = lane_id();
    const u32 bk = (ln >> 4) & 1, gl = bk * 14336 + (8 * (ln >> 5) + ((ln & 15) >> 2)) * 224 + 8 * (ln & 3);
    u32 g0 = gl + (bk ? 896u : 0u) + 36 * KB, g1 = gl + (bk ? 0u : 896u) + 36 * KB, g0h = g0 + 56 * KB, g1h = g1 + 56 * KB;
    PIN(g0); PIN(g1); PIN(g0h); PIN(g1h);
    f32x16 ag[7] = {};
    bf16x8 wf_[7];
    u32x4 yf[32];
#define P4S(n) (((n) >> 2) & 3)
#define P4O(n) ((P4S(n) & 1 ? 0 : 28 * KB) + ((n) & 3) * 3584)
#define P4W(n, f) wf_[f] = lds_tr(L, (P4S(n) == 1 || P4S(n) == 2 ? g0h : g0) + P4O(n) + (f) * 32, (P4S(n) == 1 || P4S(n) == 2 ? g1h : g1) + P4O(n) + (f) * 32)
#pragma unroll
    for (int f = 0; f < 7; ++f) P4W(0, f);

    wait_flags(FLAG(WS_FX, (t4 * 4 + w) * 32 + (ln & 31)), tag);
    auto ld_y = [&](int n) { yf[n] = __builtin_amdgcn_raw_buffer_load_b128(rs, ln * 16, WS_X1 + ((t4 * 4 + w) * 32 + ((n + 4 * rot4) & 31)) * 1024, 16); };
#pragma unroll
    for (int n = 0; n < 5; ++n) ld_y(n);
    const u32 a_done = lbase + L_DONE + w * 4;
    u32x4 land;
    static_for<0, 32>([&](auto nc) {
      constexpr int n = nc.value, m = n + 1;
      if constexpr (n + 2 >= 27 && n + 2 < 32) yf[n + 2] = lds_ld128(L, L_YL + (w * 5 + (n + 2 - 27)) * 1024 + ln * 16);
#pragma unroll
      for (int f = 0; f < 7; ++f) {
        SB();
        ag[f] = MFMA(wf_[f], asbf(yf[n]), ag[f]);
        if ((m & 3) == 0 && f == 0) {
          lds_post(a_done, m / 4);
          if (m >= 12) { PIN(ag[0]); if (!all_ge(land, m / 4)) lds_wait(L, L_LAND, m / 4); }
        }
        if ((m & 3) == 3 && m >= 11 && f == 0) land = *(volatile LAS u32x4*)(L + L_LAND);
        if (m < 32) P4W(m, f);
        if constexpr (n < 11) { if (f == 1 || f == 4) ld_y(5 + 2 * n + (f >> 2)); }
      }
#pragma unroll
      for (int f = 0; f < 7; ++f) PIN(ag[f]);
    });
    const u32x4 bst = lds_ld128(L, L_BST + (32 * w + (ln & 31)) * 16);
    const u32x4 bfr = {ln < 32 ? bst[0] : 0u, ln < 32 ? bst[1] : 0u, 0u, 0u};
#pragma unroll
    for (int f = 0; f < 7; ++f) {
      const u32x2 uv = *(LAS u32x2*)(L + L_UVT + (f * 32 + (ln & 31)) * 8);
      ag[f] = MFMA(asbf((u32x4){uv[0], uv[1], 0u, 0u}), asbf(bfr), ag[f]);
    }
    const float rs_ = __uint_as_float(bst[2]), sd_ = bf_lo(bst[1]);
    const float k1 = -2.0f * 0.7978845608028654f * 1.4426950408889634f * rs_, k3 = k1 * 0.044715f * rs_ * rs_, i2 = sd_ * sd_;
    ln = lane_id();
    const u32 ao = WS_ACT + ((t4 * 4 + w) * 112 + 7 * j4) * 1024 + ln * 16;
#pragma unroll
    for (int f = 0; f < 7; ++f) {
      u32x4 o;
#pragma unroll
      for (int k = 0; k < 4; ++k) { const f32x2 a = geglu2((f32x2){ag[f][2 * k], ag[f][2 * k + 1]}, (f32x2){ag[f][8 + 2 * k], ag[f][9 + 2 * k]}, k1, k3, i2); o[k] = pk(a[0], a[1]); }
      __builtin_amdgcn_raw_buffer_store_b128(o, rs, ao + f * 1024, 0, 16);
    }
    WAITV(0);
    if (ln == 0) __hip_atomic_store(FLAG(WS_FA, (t4 * 4 + w) * 16 + j4), tag, RLX_AGENT);
  } else {
    const u32 lane = lane_id(), r32 = lane & 31, hh = lane >> 5;
    const u32 w8o = (lane >> 3) * 1024u + (((lane & 7) ^ (((lane >> 4) & 1) << 2)) << 4);
    const u32 wt0 = (4 * hh + ((lane & 15) >> 2)) * 128 + ((32 * ((lane >> 4) & 1) + 8 * (lane & 3)) ^ (((lane & 15) >> 3) << 6)), wt1 = wt0 ^ 64u;
    auto load_wd = [&](int i) {
      int ks = rot5; PINS(ks);
      ks += i; ks -= ks >= 28 ? 28 : 0;
      const u32 so = cg * 128 + (w4 * 448 + ks * 16) * 1024, ld = lbase + wd_base(i) + w4 * 2048;
      glds(srd_d, w8o, so, ld);
      glds(srd_d, w8o, so + 8192, ld + 1024);
    };
    const u32 a_land = lbase + L_LAND + w4 * 4;
    bf16x8 wp0[11], wp1[11];
    u32 dC0 = w4 * 2048 + wt0, dC1 = w4 * 2048 + wt1, dB0 = dC0 + 92 * KB, dB1 = dC1 + 92 * KB, dA0 = dC0 + 64 * KB, dA1 = dC1 + 64 * KB;
    PIN(dA0); PIN(dA1); PIN(dB0); PIN(dB1); PIN(dC0); PIN(dC1);
#define WD0(i) (wd_base(i) < 64 * KB ? dC0 + wd_base(i) : wd_base(i) < 92 * KB ? dA0 + (wd_base(i) - 64 * KB) : dB0 + (wd_base(i) - 92 * KB))
#define WD1(i) (wd_base(i) < 64 * KB ? dC1 + wd_base(i) : wd_base(i) < 92 * KB ? dA1 + (wd_base(i) - 64 * KB) : dB1 + (wd_base(i) - 92 * KB))
#define WDTURN(i)                                                                                                                   \
  do {                                                                                                                              \
    _Pragma("unroll") for (int k = (i); k < (i) + 2; ++k) { wp0[k] = lds_tr(L, WD0(k), WD0(k) + 1024); wp1[k] = lds_tr(L, WD1(k), WD1(k) + 1024); } \
    _Pragma("unroll") for (int k = (i); k < (i) + 2; ++k) { PIN(wp0[k]); PIN(wp1[k]); }                                             \
    load_wd((i) + 2); load_wd((i) + 3);                                                                                             \
  } while (0)
    load_gu(3, gu_slot(3));
    load_wd(0); load_wd(1);
    WAITV(4); lds_post(a_land, 3);
    lds_wait(L, L_DONE, 1);
    {
      load_gu(4, gu_slot(4));
      const u32x4 srd_w = make_srd(ws, WS_BYTES);
#pragma unroll
      for (int k = 0; k < 5; ++k) glds_sc1(srd_w, lane * 16, WS_X1 + ((t4 * 4 + w4) * 32 + ((27 + k + 4 * rot4) & 31)) * 1024, lbase + L_YL + (w4 * 5 + k) * 1024);
      lds_wait(L, L_DONE, 2); load_gu(5, gu_slot(5)); WAITV(12); lds_post(a_land, 4); WDTURN(0);
      lds_wait(L, L_DONE, 3); load_gu(6, gu_slot(6)); WAITV(11); lds_post(a_land, 5);
      lds_wait(L, L_DONE, 4); load_gu(7, gu_slot(7)); WAITV(7); lds_post(a_land, 6); WDTURN(2); WAITV(4); lds_post(a_land, 7);
        wait_flags(FLAG(WS_FUV, j4 * 64 + lane), tag);
        {
          u32x4 pt[16];
          const u32 o = lane < 28 ? (w4 * 28 + lane) * 16 : 0x7fffffffu;
#pragma unroll
          for (int k = 0; k < 16; ++k) pt[k] = __builtin_amdgcn_raw_buffer_load_b128(rs, o, WS_UV + (j4 * 16 + k) * 2048, 16);
          SB();
          f32x4 a = {};
#pragma unroll
          for (int k = 0; k < 16; ++k) {
            const f32x4 t_ = __builtin_bit_cast(f32x4, pt[k]);
#pragma unroll
            for (int e = 0; e < 4; ++e) { float y_ = a[e] + t_[e]; asm("" : "+v"(y_)); a[e] = y_; }
          }
          if (lane < 28) {
            const u32 vi = (w4 * 28 + lane) * 4;
#pragma unroll
            for (int e = 0; e < 4; ++e) {
              const float hi = rb(a[e]);
              *(LAS u32*)(L + L_UVT + ((vi >> 6) * 32 + (vi & 31) + e) * 8 + ((vi >> 5) & 1) * 4) = pk(hi, a[e] - hi);
            }
          }
        }
      WDTURN(4);
      {
        const u32 so = (t4 * 128 + 32 * w4 + r32) * 64 + hh * 32;
        const u32x4 s0 = __builtin_amdgcn_raw_buffer_load_b128(rs, so, WS_ST, 16), s1 = __builtin_amdgcn_raw_buffer_load_b128(rs, so + 16, WS_ST, 16);
        auto xsum = [&](float v) {
          auto r_ = __builtin_amdgcn_permlane32_swap(__float_as_uint(v), __float_as_uint(v), false, false);
          return __uint_as_float(r_[0]) + __uint_as_float(r_[1]);
        };
        const float a0 = __uint_as_float(s0[0]), a1 = __uint_as_float(s0[2]), a2 = __uint_as_float(s1[0]), a3 = __uint_as_float(s1[2]);
        const float me = xsum((a0 + a1) + (a2 + a3)) * (1.f / 512.f);
        const float d0 = a0 * (1.f / 64.f) - me, d1 = a1 * (1.f / 64.f) - me, d2 = a2 * (1.f / 64.f) - me, d3 = a3 * (1.f / 64.f) - me;
        const float m2 = (__uint_as_float(s0[1]) + __uint_as_float(s0[3])) + (__uint_as_float(s1[1]) + __uint_as_float(s1[3]));
        float mb, sd;
        ln_scal(me, xsum(m2 + 64.f * ((d0 * d0 + d1 * d1) + (d2 * d2 + d3 * d3))) * (1.f / 512.f), mb, sd);
        if (lane < 32) lds_st128(L, L_BST + (32 * w4 + lane) * 16, (u32x4){pk(-mb, -mb), pk(sd, sd), __float_as_uint(__builtin_amdgcn_rcpf(sd)), 0u});
      }
      lds_post(a_land, 8);
      lds_wait(L, L_DONE, 5); load_wd(8); load_wd(9); load_wd(10);
      lds_wait(L, L_DONE, 6); load_wd(11); load_wd(12); load_wd(13);
      lds_wait(L, L_DONE, 7); load_wd(14); load_wd(15); load_wd(16); load_wd(17);
      lds_wait(L, L_DONE, 8);
    }
#pragma unroll
    for (int i = 18; i < 23; ++i) load_wd(i);
    WAITV(24);
#pragma unroll
    for (int k = 6; k < 11; ++k) { wp0[k] = lds_tr(L, WD0(k), WD0(k) + 1024); wp1[k] = lds_tr(L, WD1(k), WD1(k) + 1024); }
#pragma unroll
    for (int k = 6; k < 11; ++k) { PIN(wp0[k]); PIN(wp1[k]); }
#pragma unroll
    for (int i = 23; i < 28; ++i) load_wd(i);
    wait_flags(FLAG(WS_FA, (t3 * 4 + 2 * hf + ((lane >> 2) & 1)) * 16 + 4 * w4 + (lane & 3)), tag);
    const u32 ao = WS_ACT + ((t3 * 4 + 2 * hf) * 112 + 28 * w4) * 1024 + lane * 16;
    u32x4 af0[28], af1[28];
    auto ld_act = [&](int i) {
      int ks = i + rot5; ks -= ks >= 28 ? 28 : 0;
      af0[i] = __builtin_amdgcn_raw_buffer_load_b128(rs, ao, ks * 1024, 16);
      af1[i] = __builtin_amdgcn_raw_buffer_load_b128(rs, ao + 112 * 1024, ks * 1024, 16);
    };
#pragma unroll
    for (int i = 0; i < 5; ++i) ld_act(i);
    f32x16 a00 = {}, a01 = {}, a10 = {}, a11 = {};
    static_for<0, 28>([&](auto ic) {
      constexpr int i = ic.value;
      SB();
      a00 = MFMA(wp0[i % 11], asbf(af0[i]), a00);
      a01 = MFMA(wp1[i % 11], asbf(af0[i]), a01);
      SB();
      a10 = MFMA(wp0[i % 11], asbf(af1[i]), a10);
      if constexpr (i < 6) { ld_act(5 + 2 * i); ld_act(6 + 2 * i); }
      else if constexpr (i + 11 < 28) ld_act(i + 11);
      SB();
      a11 = MFMA(wp1[i % 11], asbf(af1[i]), a11);
      if constexpr (i >= 8 && i + 3 < 28) {
        wp0[(i + 3) % 11] = lds_tr(L, WD0(i + 3), WD0(i + 3) + 1024);
        wp1[(i + 3) % 11] = lds_tr(L, WD1(i + 3), WD1(i + 3) + 1024);
      }
      PIN(a00); PIN(a01); PIN(a10); PIN(a11);
    });
    red_write(20 * KB + w4 * 2048, 8192, a00, a01);
    red_write(92 * KB + w4 * 2048, 8192, a10, a11);
  }
  BAR();
  {
    f32x4 lo, hi;
    red_read(mw ? 20 * KB : 60 * KB, 8192, 2048, lo, hi);
    u32 ln = lane_id();
    const f32x4 gb0 = lds_ldf4(L, L_MISC + M_GB + (ln & 7) * 32), gb1 = lds_ldf4(L, L_MISC + M_GB + (ln & 7) * 32 + 16);
    u32x4 o;
    o[0] = pk(bf_lo(x1p[0]) + rb(gb0[0] * rb(lo[0])), bf_hi(x1p[0]) + rb(gb0[1] * rb(lo[1])));
    o[1] = pk(bf_lo(x1p[1]) + rb(gb0[2] * rb(lo[2])), bf_hi(x1p[1]) + rb(gb0[3] * rb(lo[3])));
    o[2] = pk(bf_lo(x1p[2]) + rb(gb1[0] * rb(hi[0])), bf_hi(x1p[2]) + rb(gb1[1] * rb(hi[1])));
    o[3] = pk(bf_lo(x1p[3]) + rb(gb1[2] * rb(hi[2])), bf_hi(x1p[3]) + rb(gb1[3] * rb(hi[3])));
    *(GAS u32x4*)((GAS char*)out + (t3 * 128 + hf * 64 + 8 * w) * 1024 + cg * 128 + (ln >> 3) * 1024 + (ln & 7) * 16) = o;
  }
  asm volatile(".Ladaln_end:");
}

extern "C" void kernel_launch(void* const* d_in, const int* in_sizes, int n_in, void* d_out, int out_size, void* d_ws, size_t ws_size,
                              hipStream_t stream) {
  adaln_block<<<256, 512, 0, stream>>>((const u16*)d_in[0], (const u16*)d_in[1], (const u16*)d_in[2], (const u16*)d_in[4], (const u16*)d_in[5],
                                       (const u16*)d_in[6], (const u16*)d_in[7], (const u16*)d_in[8], (u16*)d_out, (char*)d_ws);
}
```

```cpp
#include <hip/hip_runtime.h>
#include <stdint.h>

typedef unsigned short u16;
typedef unsigned int u32;
typedef unsigned long long u64;
typedef __attribute__((ext_vector_type(8))) __bf16 bf16x8;
typedef __attribute__((ext_vector_type(2))) __bf16 bf16x2;
typedef __attribute__((ext_vector_type(4))) short s16x4;
typedef __attribute__((ext_vector_type(8))) short s16x8;
typedef __attribute__((ext_vector_type(16))) float f32x16;
typedef __attribute__((ext_vector_type(4))) float f32x4;
typedef __attribute__((ext_vector_type(2))) float f32x2;
typedef __attribute__((ext_vector_type(4))) u32 u32x4;
typedef __attribute__((ext_vector_type(2))) u32 u32x2;
#define LAS __attribute__((address_space(3)))
#define GAS __attribute__((address_space(1)))
#define DI __device__ __forceinline__
#define RLX_AGENT __ATOMIC_RELAXED, __HIP_MEMORY_SCOPE_AGENT

constexpr u32 WS_EPOCH = 0;
constexpr u32 WS_ARR = 1024;
constexpr u32 WS_MODG = 4096;
constexpr u32 WS_ST = 65536;
constexpr u32 WS_UV = 196608;
constexpr u32 WS_KV = 1048576;
constexpr u32 WS_O = WS_KV + 4194304;
constexpr u32 WS_X1 = WS_O + 2097152;
constexpr u32 WS_ACT = WS_X1 + 2097152;
constexpr u32 FS = 16, MREP = 8;
constexpr u32 WS_FKV = WS_ACT + 7340032;
constexpr u32 WS_FO = WS_FKV + 1024 * 4 * FS;
constexpr u32 WS_FX = WS_FO + 2048 * 4 * FS;
constexpr u32 WS_FA = WS_FX + 2048 * 4 * FS;
constexpr u32 WS_FUV = WS_FA + 1024 * 4 * FS;
constexpr u32 WS_MOD1 = WS_FUV + 1024 * 4 * FS;
constexpr u32 WS_BYTES = WS_MOD1 + MREP * 4096;
static_assert(WS_UV + 256 * 2048 <= WS_KV && WS_BYTES <= (32u << 20), "workspace map");
#define FLAG(base, i) ((GAS u32*)(ws + (base)) + (i) * FS)

constexpr int SPIN_MAX = 1 << 17;
constexpr u32 KB = 1024;
constexpr u32 L_WO = 0;
constexpr u32 L_KV = 64 * KB;
constexpr u32 L_QX = 144 * KB;
constexpr u32 L_AR1 = 120 * KB;
constexpr u32 L_YL = 0;
constexpr u32 L_MISC = 148 * KB;
constexpr u32 L_C1 = L_MISC, L_SH = L_MISC + 2048, L_C1B = L_MISC + 4096, M_C1B = 4096;
constexpr u32 L_UVT = L_C1, L_BST = L_SH;
constexpr u32 L_ONES = L_MISC + 8192;
constexpr u32 L_GA = L_ONES + 256, M_GA = 8192 + 256, M_GB = 8192 + 512;
constexpr u32 L_DONE = L_ONES + 784, L_LAND = L_ONES + 800, L_MODF = L_ONES + 816;
constexpr u32 L_TAG = L_ONES + 768, L_JUNK = L_ONES + 1536;
constexpr u32 L_RED = L_MISC + 10240;
constexpr u32 LDS_BYTES = 163840;
DI constexpr u32 gu_slot(int c) { return (c & 3) == 3 ? 36 * KB : 64 * KB + (c & 3) * 28 * KB; }
DI constexpr u32 wd_base(int i) {
  return i < 8 ? 20 * KB + (i & 1) * 8192u : i < 11 ? 64 * KB + (i - 8) * 8192u : i < 18 ? 92 * KB + (i - 11) * 8192u : i < 21 ? 36 * KB + (i - 18) * 8192u
       : i < 23 ? (i - 21) * 8192u : i < 25 ? 20 * KB + (i - 23) * 8192u : 64 * KB + (i - 25) * 8192u;
}
constexpr u32 CODE_LINES = 12;

constexpr float NEGBIG = -1e30f;
constexpr float QSCALE = 0.25f * 1.4426950408889634f;

template <int I> struct IC { static constexpr int value = I; };
template <int I, int N, class F> DI void static_for(F&& f) { if constexpr (I < N) { f(IC<I>{}); static_for<I + 1, N>(f); } }
DI u32 lane_id() { u32 r; asm volatile("v_mbcnt_lo_u32_b32 %0, -1, 0\n\tv_mbcnt_hi_u32_b32 %0, -1, %0" : "=v"(r)); return r; }
DI float bf_lo(u32 v) { return __uint_as_float(v << 16); }
DI float bf_hi(u32 v) { return __uint_as_float(v & 0xffff0000u); }
DI u32 pk(float a, float b) { f32x2 v = {a, b}; return __builtin_bit_cast(u32, __builtin_convertvector(v, bf16x2)); }
DI float rb(float a) { return bf_lo(pk(a, a)); }
template <int O> DI u32x4 pack8(const f32x16& a) { return (u32x4){pk(a[O], a[O + 1]), pk(a[O + 2], a[O + 3]), pk(a[O + 4], a[O + 5]), pk(a[O + 6], a[O + 7])}; }
DI bf16x8 asbf(u32x4 v) { return __builtin_bit_cast(bf16x8, v); }
#define MFMA(a, b, c) __builtin_amdgcn_mfma_f32_32x32x16_bf16((a), (b), (c), 0, 0, 0)
template <int CTRL, int RM = 0xf> DI float dpp(float v) { return __builtin_bit_cast(float, __builtin_amdgcn_update_dpp(0, __builtin_bit_cast(int, v), CTRL, RM, 0xf, true)); }
DI float sum8(float v) { v += dpp<0xB1>(v); v += dpp<0x4E>(v); v += dpp<0x141>(v); return v; }
DI float dot2(u32 a, u32 b, float c) { return __builtin_amdgcn_fdot2_f32_bf16(__builtin_bit_cast(bf16x2, a), __builtin_bit_cast(bf16x2, b), c, false); }

DI u32x4 lds_ld128(LAS char* L, u32 off) { return *(LAS u32x4*)(L + off); }
DI f32x4 lds_ldf4(LAS char* L, u32 off) { return *(LAS f32x4*)(L + off); }
DI void lds_st128(LAS char* L, u32 off, u32x4 v) { *(LAS u32x4*)(L + off) = v; }
DI bf16x8 lds_tr(LAS char* L, u32 off0, u32 off1) {
  s16x4 a = __builtin_amdgcn_ds_read_tr16_b64_v4i16((LAS s16x4*)(L + off0));
  s16x4 b = __builtin_amdgcn_ds_read_tr16_b64_v4i16((LAS s16x4*)(L + off1));
  return __builtin_bit_cast(bf16x8, __builtin_shufflevector(a, b, 0, 1, 2, 3, 4, 5, 6, 7));
}
DI u32x4 make_srd(const void* p, u32 bytes) { const u64 a = (u64)p; return (u32x4){(u32)a, (u32)(a >> 32), bytes, 0x00020000u}; }
DI void glds(const u32x4& srd, u32 voff, u32 soff, u32 ldsaddr) {
  asm volatile("s_mov_b32 m0, %3\n\ts_nop 0\n\tbuffer_load_dwordx4 %0, %1, %2 offen lds" ::"v"(voff), "s"(srd), "s"(soff), "s"(ldsaddr) : "memory");
}
DI void glds_sc1(const u32x4& srd, u32 voff, u32 soff, u32 ldsaddr) {
  asm volatile("s_mov_b32 m0, %3\n\ts_nop 0\n\tbuffer_load_dwordx4 %0, %1, %2 offen sc1 lds" ::"v"(voff), "s"(srd), "s"(soff), "s"(ldsaddr) : "memory");
}
DI void touch(const u32x4& srd, u32 voff, u32 ldsaddr) {
  asm volatile("s_mov_b32 m0, %2\n\ts_nop 0\n\tbuffer_load_dword %0, %1, 0 offen lds" ::"v"(voff), "s"(srd), "s"(ldsaddr) : "memory");
}
DI void lds_post(u32 addr, u32 val) {
  asm volatile("s_mov_b64 exec, 1\n\tds_write_b32 %0, %1\n\ts_mov_b64 exec, -1" ::"v"(addr), "v"(val) : "memory");
}
DI bool all_ge(u32x4 v, u32 k) { return __builtin_amdgcn_readfirstlane(min(min(v[0], v[1]), min(v[2], v[3]))) >= k; }
DI void lds_wait(LAS char* L, u32 off, u32 k) {
#pragma nounroll
  for (int it = 0; it < (SPIN_MAX << 4); ++it) {
    if (all_ge(*(volatile LAS u32x4*)(L + off), k)) break;
    __builtin_amdgcn_s_sleep(3);
  }
  asm volatile("" ::: "memory");
}
#define BAR() asm volatile("s_waitcnt lgkmcnt(0)\n\ts_barrier" ::: "memory")
#define WAITV_BAR(N) asm volatile("s_waitcnt vmcnt(%0) lgkmcnt(0)\n\ts_barrier" ::"i"(N) : "memory")
#define WAITV(N) asm volatile("s_waitcnt vmcnt(%0)" ::"i"(N) : "memory")
#define SB() __builtin_amdgcn_sched_barrier(0)
#define PIN(x) asm volatile("" : "+v"(x))
#define PINS(x) asm volatile("" : "+s"(x))
#define ORDER() __builtin_amdgcn_fence(__ATOMIC_ACQUIRE, "wavefront")

DI void wait_flags(const GAS u32* p, u32 tag) {
  u32 v = tag + 1u;
#pragma nounroll
  for (int it = 0; it < SPIN_MAX; ++it) {
    if (v != tag) v = __hip_atomic_load(p, RLX_AGENT);
    if (__all(v == tag)) break;
    __builtin_amdgcn_s_sleep(4);
  }
  ORDER();
}
DI void ln_scal(float mean, float var, float& mb, float& sd) {
  mb = rb(mean);
  sd = rb(__builtin_amdgcn_sqrtf(rb(rb(fmaxf(var, 0.f)) + 1e-5f)));
}
DI float fma1(float a, float b, float c) { float r = fmaf(a, b, c); asm("" : "+v"(r)); return r; }
typedef __attribute__((ext_vector_type(2))) _Float16 f16x2;
DI u32x4 znorm8(u32x4 x, float r, float mr) {
  u32x4 o;
#pragma unroll
  for (int e = 0; e < 4; ++e) {
    const f32x2 v = {fmaf(bf_lo(x[e]), r, -mr), fmaf(bf_hi(x[e]), r, -mr)};
    o[e] = __builtin_bit_cast(u32, __builtin_convertvector(v, f16x2));
  }
  return o;
}
DI u32 xform2(u32 z_, u32 c_, u32 s_) {
  const f16x2 z = __builtin_bit_cast(f16x2, z_), c = __builtin_bit_cast(f16x2, c_), s = __builtin_bit_cast(f16x2, s_);
  return pk(fma1((float)z[0], (float)c[0], (float)s[0]), fma1((float)z[1], (float)c[1], (float)s[1]));
}
DI u32x4 xform8(u32x4 x, u32x4 c, u32x4 s) { return (u32x4){xform2(x[0], c[0], s[0]), xform2(x[1], c[1], s[1]), xform2(x[2], c[2], s[2]), xform2(x[3], c[3], s[3])}; }
DI u32 pkh(float a, float b) { const f32x2 v = {a, b}; return __builtin_bit_cast(u32, __builtin_convertvector(v, f16x2)); }
DI f32x2 geglu2(f32x2 ag, f32x2 au, float k1, float k3, float i2) {
  const f32x2 a = ag * (ag * ag * k3 + k1);
  const f32x2 d = (f32x2){__builtin_amdgcn_exp2f(a[0]), __builtin_amdgcn_exp2f(a[1])} * i2 + i2;
  return ag * au * (f32x2){__builtin_amdgcn_rcpf(d[0]), __builtin_amdgcn_rcpf(d[1])};
}

__global__ __launch_bounds__(512, 2) void adaln_block(const u16* __restrict__ x, const u16* __restrict__ wqkv, const u16* __restrict__ wo,
                                                      const u16* __restrict__ wg, const u16* __restrict__ wu, const u16* __restrict__ wd,
                                                      const u16* __restrict__ cvec, const u16* __restrict__ wm, u16* __restrict__ out,
                                                      char* __restrict__ ws) {
  __shared__ __attribute__((aligned(1024))) char lds[LDS_BYTES];
  LAS char* L = (LAS char*)lds;
  const u32 lbase = (u32)(uintptr_t)L;
  const int tid = threadIdx.x, lane = tid & 63;
  const int w = __builtin_amdgcn_readfirstlane(tid >> 6), w4 = w & 3;
  const bool mw = w < 4;
  const int b = blockIdx.x, xcd = b & 7, ii = b >> 3;
  if (w == 7) {
    u64 pc;
    int len;
    asm volatile("s_getpc_b64 %0\n.Ladaln_pf:\n\ts_mov_b32 %1, .Ladaln_end-.Ladaln_pf" : "=s"(pc), "=s"(len));
    touch(make_srd((const void*)pc, len > 0 ? (u32)len : 0u), ((lane < CODE_LINES ? lane : 0) * 32 + ii) * 128u, lbase + L_JUNK);
  }
  const int u1 = 31 - (8 * (xcd >> 1) + (ii >> 2)), q1 = 4 * (xcd & 1) + (ii & 3);
  const int t3 = 2 * xcd + (ii >> 4), hf = (ii >> 3) & 1, cg = ii & 7;
  const int t4 = 8 * (xcd >> 2) + (ii >> 2), j4 = 4 * (xcd & 3) + (ii & 3);
  const int rot1 = (u1 + 2 * q1) & 7;
  const int rot4 = ii >> 2, rot5 = 7 * (ii >> 3);
  const int r32 = lane & 31, hh = lane >> 5, blk = (lane >> 4) & 1, q4 = (lane & 15) >> 2, p4 = lane & 3, l3 = lane >> 3, l7 = lane & 7;
  const __amdgpu_buffer_rsrc_t rs = __builtin_amdgcn_make_buffer_rsrc(ws, 0, WS_BYTES, 0x00020000);
  u32 tag = 0;
  const int orow = t3 * 128 + hf * 64 + 8 * w + l3;
  const int mg = b;
  const u32x4 srd_q = make_srd(wqkv, 512 * 3072);
  const u32 q8o = l3 * 3072u + ((l7 ^ (((lane >> 4) & 1) << 2)) << 4);
  auto load_wq = [&](int c) {
    const u32 so = ((c + rot1) & 7) * 196608 + w * 1024 + q1 * 128;
#pragma unroll
    for (int e = 0; e < 8; ++e) glds(srd_q, q8o, so + e * 24576, lbase + (c % 5) * 24576 + w * 8192 + e * 1024);
  };
  u32x4 X[16];
  u32x4 xr;
  const int hp = w4 >> 1, hd = w >> 2, qb = (w ^ hd) & 1;
  bf16x8 qf_[15], kf_[15], vf_[15];
  u32 wl[3];
#define P1O(n) (wl[(((n) >> 2) % 5) >> 1] + ((((n) >> 2) % 5) & 1) * 24576 + ((n) & 3) * 2048)
#define P1I(n) ((n) < 12 ? (n) : 12 + (n) % 3)
  u32 cso = L_MISC + l7 * 32;
  PIN(cso);
  const u32x4 srd_g = make_srd(wg, 512 * 3584), srd_u = make_srd(wu, 512 * 3584), srd_d = make_srd(wd, 1792 * 1024), srd_o = make_srd(wo, 512 * 1024);

  u32 guo[7];
  const u32x4 srd_3 = w4 < 2 ? srd_g : srd_u;
  auto load_gu = [&](int c, u32 slot) {
    int r_ = rot4; PINS(r_);
    const u32 so = j4 * 224 + ((c + r_) & 7) * (64 * 3584), ld = lbase + slot + w4 * 1024;
#pragma unroll
    for (int n = 0; n < 7; ++n) glds(n < 3 ? srd_g : n == 3 ? srd_3 : srd_u, guo[n], so, ld + n * 4096);
  };
  const u32 w8o = l3 * 1024u + ((l7 ^ (((lane >> 4) & 1) << 2)) << 4);
  bf16x8 qf;
  if (mw) {
    u32x4 M[16];
    u32 cv = 0;
    if (mg < 48) {
      const GAS char* mb = (const GAS char*)wm + mg * 128 + (128 * w + 16 * l3) * 6144 + l7 * 16;
      cv = ((const GAS u32*)cvec)[64 * w + lane];
#pragma unroll
      for (int n = 0; n < 16; ++n) M[n] = *(const GAS u32x4*)(mb + n * 6144);
    }
    const u32 ep = __hip_atomic_load((const GAS u32*)(ws + WS_EPOCH), RLX_AGENT);
    if (lane < 12) *(LAS u32*)(L + L_DONE + lane * 4) = 0u;
    SB();
    BAR();
    if (mg < 48) {
      {
        const float c0 = bf_lo(cv), c1 = bf_hi(cv);
        *(LAS u32*)(L + L_RED + 1024 + w * 256 + lane * 4) = pk(c0 * __builtin_amdgcn_rcpf(1.0f + __builtin_amdgcn_exp2f(-1.4426950408889634f * c0)),
                                                               c1 * __builtin_amdgcn_rcpf(1.0f + __builtin_amdgcn_exp2f(-1.4426950408889634f * c1)));
      }
      const u32x4 s0 = lds_ld128(L, L_RED + 1024 + w * 256 + l3 * 32), s1 = lds_ld128(L, L_RED + 1024 + w * 256 + l3 * 32 + 16);
      f32x2 a[4] = {};
#pragma unroll
      for (int n = 0; n < 8; ++n) {
        const u32 sp = n < 4 ? s0[n & 3] : s1[n & 3];
#pragma unroll
        for (int e = 0; e < 4; ++e) {
          const u32 r0 = M[2 * n][e], r1 = M[2 * n + 1][e];
          a[e][0] = dot2(__builtin_amdgcn_perm(r1, r0, 0x05040100u), sp, a[e][0]);
          a[e][1] = dot2(__builtin_amdgcn_perm(r1, r0, 0x07060302u), sp, a[e][1]);
        }
      }
      float v[8] = {a[0][0], a[0][1], a[1][0], a[1][1], a[2][0], a[2][1], a[3][0], a[3][1]};
#pragma unroll
      for (int e = 0; e < 8; ++e) {
        v[e] += dpp<0x128>(v[e]);
        auto r1_ = __builtin_amdgcn_permlane16_swap(__float_as_uint(v[e]), __float_as_uint(v[e]), false, false);
        v[e] = __uint_as_float(r1_[0]) + __uint_as_float(r1_[1]);
        auto r2_ = __builtin_amdgcn_permlane32_swap(__float_as_uint(v[e]), __float_as_uint(v[e]), false, false);
        v[e] = __uint_as_float(r2_[0]) + __uint_as_float(r2_[1]);
      }
      if (lane < 8) {
        *(LAS f32x4*)(L + L_RED + w * 256 + lane * 32) = (f32x4){v[0], v[1], v[2], v[3]};
        *(LAS f32x4*)(L + L_RED + w * 256 + lane * 32 + 16) = (f32x4){v[4], v[5], v[6], v[7]};
      }
      lds_post(lbase + L_MODF + w * 4, 1);
      if (w == 3) {
        lds_wait(L, L_MODF, 1);
        float s = 0.f;
#pragma unroll
        for (int k = 0; k < 4; ++k) s += *(LAS float*)(L + L_RED + k * 256 + lane * 4);
        const float o = dpp<0xB1>(s);
        const u32 tg = __builtin_amdgcn_readfirstlane(ep) + 1u;
        const u64 gr = ((u64)tg << 32) | ((lane & 1) ? pk(o, s) : pk(s, o));
        if (mg < 16) {
#pragma unroll
          for (int r = 0; r < MREP / 2 + (MREP & 1); ++r)
            if (2 * r + (lane & 1) < MREP) __hip_atomic_store((GAS u64*)(ws + WS_MOD1) + (2 * r + (lane & 1)) * 512 + 32 * mg + (lane >> 1), gr, RLX_AGENT);
        } else if (!(lane & 1)) __hip_atomic_store((GAS u64*)(ws + WS_MODG) + 32 * mg + (lane >> 1), gr, RLX_AGENT);
      }
    }
    xr = *(const GAS u32x4*)((const GAS char*)x + orow * 1024 + cg * 128 + l7 * 16);
    tag = __builtin_amdgcn_readfirstlane(ep) + 1u;
    if (w < 3) {
      load_wq(0); load_wq(1); load_wq(2); load_wq(3); load_wq(4);
      WAITV(16);
    }
    lds_post(lbase + L_MODF + w * 4, 2);
    wl[0] = (8 * hh + q4) * 128 + ((64 * hp + 32 * blk + 8 * p4) ^ ((q4 >> 1) << 6));
    wl[1] = wl[0] + 48 * KB; wl[2] = wl[0] + 96 * KB;
    PIN(wl[1]); PIN(wl[2]);
    lds_wait(L, L_MODF, 2);
#pragma unroll
    for (int n = 0; n < 12; ++n) { kf_[n] = lds_tr(L, P1O(n) + 8192, P1O(n) + 8704); vf_[n] = lds_tr(L, P1O(n) + 16384, P1O(n) + 16896); qf_[n] = lds_tr(L, P1O(n), P1O(n) + 512); }
    lds_post(lbase + L_MODF + w * 4, 3);
#pragma unroll
    for (int n = 0; n < 12; ++n) { PIN(kf_[n]); PIN(vf_[n]); PIN(qf_[n]); }
    if (w < 3) {
      lds_wait(L, L_MODF, 3);
      load_wq(5); load_wq(6); load_wq(7);
    } else {
      if (lane < 32) lds_st128(L, L_ONES + (lane & 15) * 16 + (lane >> 4) * 1024, (u32x4){0x3f803f80u, 0x3f803f80u, 0x3f803f80u, 0x3f803f80u});
      if (lane == 0) *(LAS u32*)(L + L_TAG) = tag;
      u32x4 v[4];
#pragma nounroll
      for (int it = 0; it < SPIN_MAX; ++it) {
        bool ok = true;
#pragma unroll
        for (int k = 0; k < 4; ++k) { v[k] = __builtin_amdgcn_raw_buffer_load_b128(rs, lane * 16, WS_MOD1 + (xcd % MREP) * 4096 + k * 1024, 16); ok &= v[k][1] == tag && v[k][3] == tag; }
        if (__all(ok)) break;
        __builtin_amdgcn_s_sleep(2);
      }
#pragma unroll
      for (int k = 0; k < 2; ++k) {
        const u32 o = L_C1 + ((lane + 64 * k) >> 1) * 32 + (lane & 1) * 8;
        *(LAS u32x2*)(L + o + 16) = (u32x2){pkh(bf_lo(v[k][0]), bf_hi(v[k][0])), pkh(bf_lo(v[k][2]), bf_hi(v[k][2]))};
        *(LAS u32x2*)(L + o) = (u32x2){pkh(rb(1.f + bf_lo(v[2 + k][0])), rb(1.f + bf_hi(v[2 + k][0]))), pkh(rb(1.f + bf_lo(v[2 + k][2])), rb(1.f + bf_hi(v[2 + k][2])))};
      }
    }
    BAR();
    f32x16 aq = {}, ak = {}, av = {};
    u32 ard[4];
    {
      const int row = 32 * qb + r32, f = (row & 7) ^ ((row >> 3) & 1);
#pragma unroll
      for (int s = 0; s < 4; ++s) { ard[s] = L_AR1 + row * 128 + ((((2 * s) | hh) ^ f) << 4); PIN(ard[s]); }
    }
    bf16x8 hf_[3];
#define P1H(n) hf_[(n) % 3] = asbf(lds_ld128(L, ard[(n) & 3] + (((n) >> 2) % 3) * 8192))
    BAR();
    P1H(0); P1H(1);
    static_for<0, 32>([&](auto nc) {
      constexpr int n = nc.value, m = n + 2;
      constexpr bool bnd = (m & 3) == 0, rd = m >= 12 && m < 32;
      SB();
      ak = MFMA(kf_[P1I(n)], hf_[n % 3], ak);
      if constexpr (m < 32 && !bnd) { P1H(m); if constexpr (rd) kf_[P1I(m)] = lds_tr(L, P1O(m) + 8192, P1O(m) + 8704); }
      SB();
      av = MFMA(hf_[n % 3], vf_[P1I(n)], av);
      if constexpr (m < 32 && bnd) {
        PIN(ak); PIN(av);
        WAITV_BAR(m >= 12 ? 56 - 2 * m : 63);
        P1H(m);
        if constexpr (rd) kf_[P1I(m)] = lds_tr(L, P1O(m) + 8192, P1O(m) + 8704);
      }
      if constexpr (rd) vf_[P1I(m)] = lds_tr(L, P1O(m) + 16384, P1O(m) + 16896);
      SB();
      aq = MFMA(qf_[P1I(n)], hf_[n % 3], aq);
      if constexpr (rd) qf_[P1I(m)] = lds_tr(L, P1O(m), P1O(m) + 512);
      PIN(ak); PIN(av); PIN(aq);
    });
    const u32x4 f0 = pack8<0>(ak), f1 = pack8<8>(ak), f2 = pack8<0>(av), f3 = pack8<8>(av);
    const int kvi = ((u1 >> 1) * 16 + 2 * q1 + hp) * 4 + 2 * (u1 & 1) + qb;
    const u32 o = WS_KV + kvi * 4096 + lane * 16;
    __builtin_amdgcn_raw_buffer_store_b128(f0, rs, o, 0, 16);
    __builtin_amdgcn_raw_buffer_store_b128(f1, rs, o + 1024, 0, 16);
    __builtin_amdgcn_raw_buffer_store_b128(f2, rs, o + 2048, 0, 16);
    __builtin_amdgcn_raw_buffer_store_b128(f3, rs, o + 3072, 0, 16);
    aq *= QSCALE;
    qf = asbf(pack8<0>(aq));
    const u32x4 f5 = pack8<8>(aq);
    BAR();
    const u32 ok = L_KV + hp * 40960 + (8 + qb) * 4096 + lane * 16;
    lds_st128(L, ok, f0); lds_st128(L, ok + 1024, f1); lds_st128(L, ok + 2048, f2); lds_st128(L, ok + 3072, f3);
    lds_st128(L, L_QX + w * 1024 + lane * 16, f5);
    BAR();
    WAITV(0);
    if (lane == 0) __hip_atomic_store(FLAG(WS_FKV, kvi), tag, RLX_AGENT);
  } else {
    const __amdgpu_buffer_rsrc_t rx = __builtin_amdgcn_make_buffer_rsrc((void*)x, 0, 2048 * 1024, 0x00020000);
    if (mg < 48) BAR();
#pragma unroll
    for (int c = 0; c < 8; ++c)
#pragma unroll
      for (int g = 0; g < 2; ++g)
        X[2 * c + g] = __builtin_amdgcn_raw_buffer_load_b128(rx, l3 * 1024u + l7 * 16u, (u1 * 64 + 16 * w4 + 8 * g) * 1024 + ((c + rot1) & 7) * 128, 0);
    xr = *(const GAS u32x4*)((const GAS char*)x + orow * 1024 + cg * 128 + l7 * 16);
    SB();
    if (mg >= 48) BAR();
    float sm[2] = {}, sq[2] = {};
#pragma unroll
    for (int c = 0; c < 8; ++c) {
      SB();
#pragma unroll
      for (int e = 0; e < 4; ++e)
#pragma unroll
        for (int g = 0; g < 2; ++g) { sm[g] = dot2(X[2 * c + g][e], 0x3f803f80u, sm[g]); sq[g] = dot2(X[2 * c + g][e], X[2 * c + g][e], sq[g]); }
    }
    float rr[2], mr[2];
#pragma unroll
    for (int g = 0; g < 2; ++g) {
      const float me = sum8(sm[g]) * (1.f / 512.f);
      float mb, sd;
      ln_scal(me, sum8(sq[g]) * (1.f / 512.f) - me * me, mb, sd);
      rr[g] = __builtin_amdgcn_rcpf(sd); mr[g] = mb * rr[g];
    }
#pragma unroll
    for (int n = 0; n < 16; ++n) { X[n] = znorm8(X[n], rr[n & 1], mr[n & 1]); PIN(X[n]); }
    if (w4 < 3) {
      const u32x4 srd_a = make_srd(w4 == 0 ? wo : w4 == 1 ? wg : wu, w4 == 0 ? 512 * 1024 : 512 * 3584);
      const int e1 = 64 + lane;
      const u32 ta0 = w4 ? (16 * ii + lane / 7) * 3584 + (xcd & 3) * 896 + (lane % 7) * 128 : ii * 16384 + lane * 128;
      const u32 ta1 = w4 ? (lane < 48 ? (16 * ii + e1 / 7) * 3584 + (xcd & 3) * 896 + (e1 % 7) * 128 : 0x7fffffffu) : ta0 + 8192;
      touch(srd_a, ta0, lbase + L_JUNK);
      touch(srd_a, ta1, lbase + L_JUNK);
    }
    BAR();
    tag = __builtin_amdgcn_readfirstlane(*(LAS u32*)(L + L_TAG));
    if (w == 4 && lane == 0) __hip_atomic_store((GAS u32*)(ws + WS_ARR) + b, tag, RLX_AGENT);
    const u32 awE = L_AR1 + (16 * w4 + l3) * 128 + ((l7 ^ l3) << 4), awO = (awE ^ 16u) + 1024;
    u32x4 ca[2], sa[2];
#define CSLOAD(c) do { const u32 o_ = cso + (((c) + rot1) & 7) * 256; ca[(c) & 1] = lds_ld128(L, o_); sa[(c) & 1] = lds_ld128(L, o_ + 16); } while (0)
    CSLOAD(0);
#pragma unroll
    for (int c = 0; c < 8; ++c) {
      if (c < 7) CSLOAD(c + 1);
      SB();
      PIN(X[2 * c]);
      lds_st128(L, awE + (c % 3) * 8192, xform8(X[2 * c], ca[c & 1], sa[c & 1]));
      PIN(X[2 * c + 1]);
      lds_st128(L, awO + (c % 3) * 8192, xform8(X[2 * c + 1], ca[c & 1], sa[c & 1]));
      if (c == 0) BAR();
      if (c >= 2) asm volatile("s_waitcnt lgkmcnt(2)\n\ts_barrier" ::: "memory");
    }
    BAR();
#pragma unroll
    for (int n = 0; n < 7; ++n) {
      const int pc = w4 + 4 * n, up = pc >= 14, e = (pc - 14 * up) * 64 + lane, rho = e / 14, cc = e - 14 * rho;
      guo[n] = (u32)((up ? (rho ^ 4) : rho) * 3584 + cc * 16);
    }
    BAR();
    BAR();
    qf = asbf(lds_ld128(L, L_QX + (w4 ^ 1) * 1024 + lane * 16));

  }
  PIN(xr);
  if (!mw && qb) {
    for (int n = hp * 32; n < hp * 32 + 32; ++n) glds(srd_o, w8o, cg * 128 + n * 8192, lbase + L_WO + n * 1024);
  }

  {
    const u32 ka = L_KV + hp * 40960 + hd * 1024 + lane * 16;
    const bool vdat = (r32 >> 4) == hd;
    const u32 va = vdat ? L_KV + hp * 40960 + 2048 + lane * 16 : L_ONES + (lane & 15) * 16;
    const u32 vs = vdat ? 4096u : 0u;
    f32x16 cm;
#pragma unroll
    for (int g = 0; g < 16; ++g) cm[g] = (g & 3) + 8 * (g >> 2) + 4 * hh <= r32 ? 0.f : NEGBIG;
    const f32x16 zero = {};
    f32x16 O = {};
    float m = NEGBIG;
    auto rowmax = [&](const f32x16& s, float mm) {
#pragma unroll
      for (int g = 0; g < 16; g += 2) mm = fmaxf(fmaxf(mm, s[g]), s[g + 1]);
      return mm;
    };
    auto ldk = [&](int lb) { return asbf(lds_ld128(L, ka + lb * 4096)); };
#pragma nounroll
    for (int st = 0; st < 2; ++st) {
      int lm = 8 + qb, lo = 8, hi = 8 + qb;
      if (st) {
        if (u1 == 0) break;
        if (!qb) {
          const int g0 = 2 * u1 - 8 + 4 * hd, gl = g0 + (lane & 3);
          wait_flags(FLAG(WS_FKV, gl < 0 ? ((u1 >> 1) * 16 + 2 * q1 + hp) * 4 + 2 * (u1 & 1) : ((gl >> 2) * 16 + 2 * q1 + hp) * 4 + (gl & 3)), tag);
          const u32x4 srd_w = make_srd(ws, WS_BYTES);
#pragma unroll
          for (int k = 0; k < 4; ++k) {
            const int g = g0 + k;
            const u32 so = g < 0 ? 0x7ff00000u : WS_KV + (((g >> 2) * 16 + 2 * q1 + hp) * 4 + (g & 3)) * 4096;
#pragma unroll
            for (int n = 0; n < 4; ++n) glds_sc1(srd_w, lane * 16, so + n * 1024, lbase + L_KV + hp * 40960 + hd * 16384 + (4 * k + n) * 1024);
          }
          WAITV(0);
        }
        const int lmin = 8 - 2 * u1;
#pragma unroll
        for (int g = 0; g < 16; ++g) { float u = lmin > qb ? NEGBIG : NEGBIG - cm[g]; asm("" : "+v"(u)); cm[g] = u; }
        lm = qb; lo = lmin > qb ? lmin : qb + 1; hi = 8;
        BAR();
      }
      bf16x8 kA = ldk(lo), kB = ldk(lo + 1);
      f32x16 sA = MFMA(ldk(lm), qf, cm), sB, sC;
      const bf16x8 k0 = kA;
      float mn = m;
      for (int lb = lo; lb < hi; lb += 2) {
        sB = MFMA(kA, qf, zero);
        sC = MFMA(kB, qf, zero);
        kA = ldk(lb + 2); kB = ldk(lb + 3);
        SB();
        mn = rowmax(sB, mn);
        if (lb + 1 < hi) mn = rowmax(sC, mn);
      }
      mn = rowmax(sA, mn);
      {
        auto r_ = __builtin_amdgcn_permlane32_swap(__float_as_uint(mn), __float_as_uint(mn), false, false);
        mn = fmaxf(__uint_as_float(r_[0]), __uint_as_float(r_[1]));
      }
      u32 vv = va + lm * vs;
      bf16x8 v0 = asbf(lds_ld128(L, vv)), v1 = asbf(lds_ld128(L, vv + 1024));
      f32x16 negm;
#pragma unroll
      for (int g = 0; g < 16; ++g) negm[g] = -mn;
#pragma unroll
      for (int g = 0; g < 16; ++g) { float u = sA[g] - mn; asm("" : "+v"(u)); sA[g] = u; }
      if (st) {
        const float al = __builtin_amdgcn_exp2f(m - mn);
#pragma unroll
        for (int g = 0; g < 16; ++g) { float o = O[g] * al; asm("" : "+v"(o)); O[g] = o; }
      }
      m = mn;
      kA = k0;
      vv = va + lo * vs;
#define P2STEP(CUR, NXT)                                                                      \
  {                                                                                           \
    NXT = MFMA(kA, qf, negm);                                                                 \
    PIN(NXT);                                                                                 \
    kA = ldk(lb + 1);                                                                         \
    const bf16x8 n0_ = asbf(lds_ld128(L, vv)), n1_ = asbf(lds_ld128(L, vv + 1024));           \
    vv += vs;                                                                                 \
    SB();                                                                                     \
    _Pragma("unroll") for (int g = 0; g < 8; ++g) CUR[g] = __builtin_amdgcn_exp2f(CUR[g]);    \
    O = MFMA(v0, asbf(pack8<0>(CUR)), O);                                                     \
    _Pragma("unroll") for (int g = 8; g < 16; ++g) CUR[g] = __builtin_amdgcn_exp2f(CUR[g]);   \
    O = MFMA(v1, asbf(pack8<8>(CUR)), O);                                                     \
    SB();                                                                                     \
    v0 = n0_; v1 = n1_;                                                                       \
  }
      for (int lb = lo;; ++lb) {
        P2STEP(sA, sB);
        if (lb >= hi) break;
        ++lb;
        P2STEP(sB, sA);
        if (lb >= hi) break;
      }
    }
    u32x4 of;
    {
      const float inv = __builtin_amdgcn_rcpf(hd ? O[0] : O[8]);
      float o[8];
#pragma unroll
      for (int g = 0; g < 8; ++g) { o[g] = (hd ? O[8 + g] : O[g]) * inv; asm("" : "+v"(o[g])); }
      of = (u32x4){pk(o[0], o[1]), pk(o[2], o[3]), pk(o[4], o[5]), pk(o[6], o[7])};
    }
    const int oi = (2 * u1 + qb) * 32 + 4 * q1 + 2 * hp + hd;
    __builtin_amdgcn_raw_buffer_store_b128(of, rs, WS_O + oi * 1024 + lane * 16, 0, 16);
    WAITV(0);
    if (lane == 0) __hip_atomic_store(FLAG(WS_FO, oi), tag, RLX_AGENT);
  }
  {
    if (u1 == 0 && q1 == 0 && w == 7) {
#pragma nounroll
      for (int it = 0; it < SPIN_MAX; ++it) {
        bool ok = true;
#pragma unroll
        for (int k = 0; k < 4; ++k) ok &= __hip_atomic_load((const GAS u32*)(ws + WS_ARR) + lane + 64 * k, RLX_AGENT) == tag;
        if (__all(ok)) break;
        __builtin_amdgcn_s_sleep(1);
      }
      if (lane == 0) __hip_atomic_store((GAS u32*)(ws + WS_EPOCH), tag, RLX_AGENT);
    }
  }
  BAR();

  const u32 wt0 = (4 * hh + q4) * 128 + ((32 * blk + 8 * p4) ^ ((q4 >> 1) << 6)), wt1 = wt0 ^ 64u;
  auto red_write = [&](u32 base, u32 S8, const f32x16& a0, const f32x16& a1) {
    u32 ln = lane_id();
    const u32 o = base + ((ln & 31) >> 3) * S8 + (ln & 7) * 256, sw = (ln & 7) ^ (ln >> 5);
#pragma unroll
    for (int g = 0; g < 4; ++g) {
      *(LAS f32x4*)(L + o + (((2 * g) ^ sw) << 4)) = (f32x4){a0[4 * g], a0[4 * g + 1], a0[4 * g + 2], a0[4 * g + 3]};
      *(LAS f32x4*)(L + o + (((2 * g) ^ sw) << 4) + 128) = (f32x4){a1[4 * g], a1[4 * g + 1], a1[4 * g + 2], a1[4 * g + 3]};
    }
  };
  auto red_read = [&](u32 base, u32 S8, u32 SK, f32x4& lo, f32x4& hi) {
    u32 ln = lane_id();
    const u32 o0 = base + w * S8 + (ln >> 3) * 256 + (((2 * (ln & 7)) ^ (ln >> 3)) << 4), o1 = o0 ^ 16u;
    lo = lds_ldf4(L, o0); hi = lds_ldf4(L, o1);
#pragma unroll
    for (int k = 1; k < 4; ++k) { lo += lds_ldf4(L, o0 + k * SK); hi += lds_ldf4(L, o1 + k * SK); }
  };
  u32x4 x1p;
  {
    if (mw) {
      bf16x8 wa[8], wb[8];
#pragma unroll
      for (int i = 0; i < 8; ++i) {
        const u32 o = L_WO + w * 16384 + i * 2048;
        wa[i] = lds_tr(L, o + wt0, o + wt0 + 1024); wb[i] = lds_tr(L, o + wt1, o + wt1 + 1024);
      }
      wait_flags(FLAG(WS_FO, (t3 * 4 + 2 * hf + (l3 & 1)) * 32 + 8 * w + l7), tag);
      u32x4 of0[8], of1[8];
      const u32 oo = WS_O + ((t3 * 4 + 2 * hf) * 32 + 8 * w) * 1024 + lane * 16;
#pragma unroll
      for (int i = 0; i < 8; ++i) {
        of0[i] = __builtin_amdgcn_raw_buffer_load_b128(rs, oo + i * 1024, 0, 16);
        of1[i] = __builtin_amdgcn_raw_buffer_load_b128(rs, oo + 32768 + i * 1024, 0, 16);
      }
      SB();
      f32x16 a00 = {}, a01 = {}, a10 = {}, a11 = {};
#pragma unroll
      for (int i = 0; i < 8; ++i) {
        a00 = MFMA(wa[i], asbf(of0[i]), a00); a01 = MFMA(wb[i], asbf(of0[i]), a01);
        a10 = MFMA(wa[i], asbf(of1[i]), a10); a11 = MFMA(wb[i], asbf(of1[i]), a11);
      }
      red_write(L_WO + w * 16384, 2048, a00, a01);
      red_write(L_WO + w * 16384 + 8192, 2048, a10, a11);
    } else {
      load_gu(0, gu_slot(0));
      u32x4 bv[2];
      {
        u32x4 q[4];
        u32 v[3];
        const GAS u64* g = (const GAS u64*)(ws + WS_MODG);
        const u32 qo = (((r32 & 1) ? 768 : 1024) + rot4 * 32 + (t4 >> 3) * 16 + 4 * hh) * 8;
#pragma nounroll
        for (int it = 0; it < SPIN_MAX; ++it) {
          const u64 x1_ = __hip_atomic_load(g + 1024 + (tid - 256), RLX_AGENT);
          const u64 x2 = __hip_atomic_load(g + (lane < 32 ? 512 : 1248) + cg * 32 + lane, RLX_AGENT);
          bool ok = (u32)(x1_ >> 32) == tag && (u32)(x2 >> 32) == tag;
#pragma unroll
          for (int k = 0; k < 4; ++k) { q[k] = __builtin_amdgcn_raw_buffer_load_b128(rs, qo + (k >> 1) * 64 + (k & 1) * 16, WS_MODG, 16); ok &= q[k][1] == tag && q[k][3] == tag; }
          v[1] = (u32)x1_; v[2] = (u32)x2;
          if (__all(ok)) break;
          __builtin_amdgcn_s_sleep(1);
        }
        *(LAS f32x2*)(L + L_C1B + (tid - 256) * 8) = (f32x2){rb(1.f + bf_lo(v[1])), rb(1.f + bf_hi(v[1]))};
        if (w == 4) *(LAS f32x2*)(L + L_GA + lane * 8) = (f32x2){bf_lo(v[2]), bf_hi(v[2])};
        const float one = (r32 & 1) ? 0.f : 1.f;
#pragma unroll
        for (int s = 0; s < 2; ++s)
          bv[s] = (u32x4){pk(one + bf_lo(q[2 * s][0]), one + bf_hi(q[2 * s][0])), pk(one + bf_lo(q[2 * s][2]), one + bf_hi(q[2 * s][2])),
                          pk(one + bf_lo(q[2 * s + 1][0]), one + bf_hi(q[2 * s + 1][0])), pk(one + bf_lo(q[2 * s + 1][2]), one + bf_hi(q[2 * s + 1][2]))};
      }
      lds_post(lbase + L_LAND + w4 * 4, 1);
      load_gu(1, gu_slot(1));
      lds_wait(L, L_LAND, 1);
      {
        const u32 gl = blk * 14336 + (8 * hh + q4) * 224 + 8 * p4 + w4 * 32 + (t4 >> 3) * 7168 + gu_slot(0);
        const u32 g0 = gl + (blk ? 896u : 0u), g1 = gl + (blk ? 0u : 896u);
        f32x16 a0 = {}, a1 = {};
#pragma unroll
        for (int s = 0; s < 2; ++s) {
          a0 = MFMA(lds_tr(L, g0 + s * 3584, g1 + s * 3584), asbf(bv[s]), a0);
          a1 = MFMA(lds_tr(L, g0 + s * 3584 + 128, g1 + s * 3584 + 128), asbf(bv[s]), a1);
        }
        if (r32 < 2) {
          const u32 o = WS_UV + (j4 * 16 + t4) * 2048 + w4 * 256 + r32 * 128 + hh * 16;
#pragma unroll
          for (int g = 0; g < 4; ++g) {
            __builtin_amdgcn_raw_buffer_store_b128(__builtin_bit_cast(u32x4, (f32x4){a0[4 * g], a0[4 * g + 1], a0[4 * g + 2], a0[4 * g + 3]}), rs, o + g * 32, 0, 16);
            if (w4 < 3) __builtin_amdgcn_raw_buffer_store_b128(__builtin_bit_cast(u32x4, (f32x4){a1[4 * g], a1[4 * g + 1], a1[4 * g + 2], a1[4 * g + 3]}), rs, o + 1024 + g * 32, 0, 16);
          }
        }
      }
      load_gu(2, gu_slot(2));
    }
    BAR();
    f32x4 lo, hi;
    red_read(L_WO, 2048, 16384, lo, hi);
    const f32x4 ga0 = lds_ldf4(L, cso + M_GA), ga1 = lds_ldf4(L, cso + M_GA + 16);
    float v[8];
    v[0] = rb(bf_lo(xr[0]) + rb(ga0[0] * rb(lo[0]))); v[1] = rb(bf_hi(xr[0]) + rb(ga0[1] * rb(lo[1])));
    v[2] = rb(bf_lo(xr[1]) + rb(ga0[2] * rb(lo[2]))); v[3] = rb(bf_hi(xr[1]) + rb(ga0[3] * rb(lo[3])));
    v[4] = rb(bf_lo(xr[2]) + rb(ga1[0] * rb(hi[0]))); v[5] = rb(bf_hi(xr[2]) + rb(ga1[1] * rb(hi[1])));
    v[6] = rb(bf_lo(xr[3]) + rb(ga1[2] * rb(hi[2]))); v[7] = rb(bf_hi(xr[3]) + rb(ga1[3] * rb(hi[3])));
    x1p = (u32x4){pk(v[0], v[1]), pk(v[2], v[3]), pk(v[4], v[5]), pk(v[6], v[7])};
    PIN(x1p);
    {
      const f32x4 c0 = lds_ldf4(L, cso + M_C1B + cg * 256), c1 = lds_ldf4(L, cso + M_C1B + cg * 256 + 16);
      const u32x4 y = {pk(v[0] * c0[0], v[1] * c0[1]), pk(v[2] * c0[2], v[3] * c0[3]), pk(v[4] * c1[0], v[5] * c1[1]), pk(v[6] * c1[2], v[7] * c1[3])};
      __builtin_amdgcn_raw_buffer_store_b128(y, rs, WS_X1 + ((t3 * 4 + 2 * hf + (w >> 2)) * 32 + 4 * cg + (l7 >> 1)) * 1024 + ((l7 & 1) * 32 + 8 * w4 + l3) * 16, 0, 16);
    }
    const float sm = sum8(((v[0] + v[1]) + (v[2] + v[3])) + ((v[4] + v[5]) + (v[6] + v[7])));
    const float mu = sm * (1.f / 64.f);
    float m2 = 0.f;
#pragma unroll
    for (int k = 0; k < 8; ++k) m2 = fmaf(v[k] - mu, v[k] - mu, m2);
    m2 = sum8(m2);
    if (l7 == 0) __hip_atomic_store((GAS u64*)(ws + WS_ST) + orow * 8 + cg, ((u64)__float_as_uint(m2) << 32) | __float_as_uint(sm), RLX_AGENT);
    WAITV(0);
    if (lane == 0) __hip_atomic_store(FLAG(WS_FX, (t3 * 4 + 2 * hf + (w >> 2)) * 32 + cg * 4 + w4), tag, RLX_AGENT);
    if (!mw && lane == 1) __hip_atomic_store(FLAG(WS_FUV, j4 * 64 + t4 * 4 + w4), tag, RLX_AGENT);
  }
  BAR();

  if (mw) {
    u32 ln = lane_id();
    const u32 bk = (ln >> 4) & 1, gl = bk * 14336 + (8 * (ln >> 5) + ((ln & 15) >> 2)) * 224 + 8 * (ln & 3);
    u32 g0 = gl + (bk ? 896u : 0u) + 36 * KB, g1 = gl + (bk ? 0u : 896u) + 36 * KB, g0h = g0 + 56 * KB, g1h = g1 + 56 * KB;
    PIN(g0); PIN(g1); PIN(g0h); PIN(g1h);
    f32x16 ag[7] = {};
    bf16x8 wf_[7];
    u32x4 yf[32];
#define P4S(n) (((n) >> 2) & 3)
#define P4O(n) ((P4S(n) & 1 ? 0 : 28 * KB) + ((n) & 3) * 3584)
#define P4W(n, f) wf_[f] = lds_tr(L, (P4S(n) == 1 || P4S(n) == 2 ? g0h : g0) + P4O(n) + (f) * 32, (P4S(n) == 1 || P4S(n) == 2 ? g1h : g1) + P4O(n) + (f) * 32)
    bf16x8 pre[3][7];
#pragma unroll
    for (int n = 0; n < 4; ++n)
#pragma unroll
      for (int f = 0; f < 7; ++f) { P4W(n, f); if (n < 3) pre[n][f] = wf_[f]; }
    const u32 a_done = lbase + L_DONE + w * 4;
    lds_post(a_done, 1);

    wait_flags(FLAG(WS_FX, (t4 * 4 + w) * 32 + (ln & 31)), tag);
    auto ld_y = [&](int n) { yf[n] = __builtin_amdgcn_raw_buffer_load_b128(rs, ln * 16, WS_X1 + ((t4 * 4 + w) * 32 + ((n + 4 * rot4) & 31)) * 1024, 16); };
#pragma unroll
    for (int n = 0; n < 5; ++n) ld_y(n);
    u32x4 land;
    static_for<0, 32>([&](auto nc) {
      constexpr int n = nc.value, m = n + 1;
      if constexpr (n + 2 >= 27 && n + 2 < 32) yf[n + 2] = lds_ld128(L, L_YL + (w * 5 + (n + 2 - 27)) * 1024 + ln * 16);
#pragma unroll
      for (int f = 0; f < 7; ++f) {
        SB();
        ag[f] = MFMA(n < 3 ? pre[n < 3 ? n : 0][f] : wf_[f], asbf(yf[n]), ag[f]);
        if ((m & 3) == 0 && f == 0) {
          if (m > 4) lds_post(a_done, m / 4);
          if (m >= 12) { PIN(ag[0]); if (!all_ge(land, m / 4)) lds_wait(L, L_LAND, m / 4); }
        }
        if ((m & 3) == 3 && m >= 11 && f == 0) land = *(volatile LAS u32x4*)(L + L_LAND);
        if (m >= 4 && m < 32) P4W(m, f);
        if constexpr (n < 22) { if (f == 1) ld_y(5 + n); }
      }
#pragma unroll
      for (int f = 0; f < 7; ++f) PIN(ag[f]);
    });
    const u32x4 bst = lds_ld128(L, L_BST + (32 * w + (ln & 31)) * 16);
    const u32x4 bfr = {ln < 32 ? bst[0] : 0u, ln < 32 ? bst[1] : 0u, 0u, 0u};
#pragma unroll
    for (int f = 0; f < 7; ++f) {
      const u32x2 uv = *(LAS u32x2*)(L + L_UVT + (f * 32 + (ln & 31)) * 8);
      ag[f] = MFMA(asbf((u32x4){uv[0], uv[1], 0u, 0u}), asbf(bfr), ag[f]);
    }
    const float rs_ = __uint_as_float(bst[2]), sd_ = bf_lo(bst[1]);
    const float k1 = -2.0f * 0.7978845608028654f * 1.4426950408889634f * rs_, k3 = k1 * 0.044715f * rs_ * rs_, i2 = sd_ * sd_;
    ln = lane_id();
    const u32 ao = WS_ACT + ((t4 * 4 + w) * 112 + 7 * j4) * 1024 + ln * 16;
#pragma unroll
    for (int f = 0; f < 7; ++f) {
      u32x4 o;
#pragma unroll
      for (int k = 0; k < 4; ++k) { const f32x2 a = geglu2((f32x2){ag[f][2 * k], ag[f][2 * k + 1]}, (f32x2){ag[f][8 + 2 * k], ag[f][9 + 2 * k]}, k1, k3, i2); o[k] = pk(a[0], a[1]); }
      __builtin_amdgcn_raw_buffer_store_b128(o, rs, ao + f * 1024, 0, 16);
    }
    WAITV(0);
    if (ln == 0) __hip_atomic_store(FLAG(WS_FA, (t4 * 4 + w) * 16 + j4), tag, RLX_AGENT);
  } else {
    const u32 lane = lane_id(), r32 = lane & 31, hh = lane >> 5;
    const u32 w8o = (lane >> 3) * 1024u + (((lane & 7) ^ (((lane >> 4) & 1) << 2)) << 4);
    const u32 wt0 = (4 * hh + ((lane & 15) >> 2)) * 128 + ((32 * ((lane >> 4) & 1) + 8 * (lane & 3)) ^ (((lane & 15) >> 3) << 6)), wt1 = wt0 ^ 64u;
    auto load_wd = [&](int i) {
      int ks = rot5; PINS(ks);
      ks += i; ks -= ks >= 28 ? 28 : 0;
      const u32 so = cg * 128 + (w4 * 448 + ks * 16) * 1024, ld = lbase + wd_base(i) + w4 * 2048;
      glds(srd_d, w8o, so, ld);
      glds(srd_d, w8o, so + 8192, ld + 1024);
    };
    const u32 a_land = lbase + L_LAND + w4 * 4;
    bf16x8 wp0[11], wp1[11];
    u32 dC0 = w4 * 2048 + wt0, dC1 = w4 * 2048 + wt1, dB0 = dC0 + 92 * KB, dB1 = dC1 + 92 * KB, dA0 = dC0 + 64 * KB, dA1 = dC1 + 64 * KB;
    PIN(dA0); PIN(dA1); PIN(dB0); PIN(dB1); PIN(dC0); PIN(dC1);
#define WD0(i) (wd_base(i) < 64 * KB ? dC0 + wd_base(i) : wd_base(i) < 92 * KB ? dA0 + (wd_base(i) - 64 * KB) : dB0 + (wd_base(i) - 92 * KB))
#define WD1(i) (wd_base(i) < 64 * KB ? dC1 + wd_base(i) : wd_base(i) < 92 * KB ? dA1 + (wd_base(i) - 64 * KB) : dB1 + (wd_base(i) - 92 * KB))
#define WDTURN(i)                                                                                                                   \
  do {                                                                                                                              \
    _Pragma("unroll") for (int k = (i); k < (i) + 2; ++k) { wp0[k] = lds_tr(L, WD0(k), WD0(k) + 1024); wp1[k] = lds_tr(L, WD1(k), WD1(k) + 1024); } \
    _Pragma("unroll") for (int k = (i); k < (i) + 2; ++k) { PIN(wp0[k]); PIN(wp1[k]); }                                             \
    load_wd((i) + 2); load_wd((i) + 3);                                                                                             \
  } while (0)
    load_gu(3, gu_slot(3));
    lds_wait(L, L_DONE, 1);
    load_gu(4, gu_slot(4));
    load_wd(0); load_wd(1);
    WAITV(11); lds_post(a_land, 3);
        wait_flags(FLAG(WS_FUV, j4 * 64 + lane), tag);
        {
          u32x4 pt[16];
          const u32 o = lane < 28 ? (w4 * 28 + lane) * 16 : 0x7fffffffu;
#pragma unroll
          for (int k = 0; k < 16; ++k) pt[k] = __builtin_amdgcn_raw_buffer_load_b128(rs, o, WS_UV + (j4 * 16 + k) * 2048, 16);
          SB();
          f32x4 a = {};
#pragma unroll
          for (int k = 0; k < 16; ++k) {
            const f32x4 t_ = __builtin_bit_cast(f32x4, pt[k]);
#pragma unroll
            for (int e = 0; e < 4; ++e) { float y_ = a[e] + t_[e]; asm("" : "+v"(y_)); a[e] = y_; }
          }
          if (lane < 28) {
            const u32 vi = (w4 * 28 + lane) * 4;
#pragma unroll
            for (int e = 0; e < 4; ++e) {
              const float hi = rb(a[e]);
              *(LAS u32*)(L + L_UVT + ((vi >> 6) * 32 + (vi & 31) + e) * 8 + ((vi >> 5) & 1) * 4) = pk(hi, a[e] - hi);
            }
          }
        }
    {
      lds_wait(L, L_DONE, 2);
      const u32 so = (t4 * 128 + 32 * w4 + r32) * 64 + hh * 32;
      const u32x4 s0 = __builtin_amdgcn_raw_buffer_load_b128(rs, so, WS_ST, 16), s1 = __builtin_amdgcn_raw_buffer_load_b128(rs, so + 16, WS_ST, 16);
      SB();
      load_gu(5, gu_slot(5));
      const u32x4 srd_w = make_srd(ws, WS_BYTES);
#pragma unroll
      for (int k = 0; k < 5; ++k) glds_sc1(srd_w, lane * 16, WS_X1 + ((t4 * 4 + w4) * 32 + ((27 + k + 4 * rot4) & 31)) * 1024, lbase + L_YL + (w4 * 5 + k) * 1024);
      WAITV(12); lds_post(a_land, 4); WDTURN(0);
      lds_wait(L, L_DONE, 3); load_gu(6, gu_slot(6)); WAITV(16); lds_post(a_land, 5);
      {
        auto xsum = [&](float v) {
          auto r_ = __builtin_amdgcn_permlane32_swap(__float_as_uint(v), __float_as_uint(v), false, false);
          return __uint_as_float(r_[0]) + __uint_as_float(r_[1]);
        };
        const float a0 = __uint_as_float(s0[0]), a1 = __uint_as_float(s0[2]), a2 = __uint_as_float(s1[0]), a3 = __uint_as_float(s1[2]);
        const float me = xsum((a0 + a1) + (a2 + a3)) * (1.f / 512.f);
        const float d0 = a0 * (1.f / 64.f) - me, d1 = a1 * (1.f / 64.f) - me, d2 = a2 * (1.f / 64.f) - me, d3 = a3 * (1.f / 64.f) - me;
        const float m2 = (__uint_as_float(s0[1]) + __uint_as_float(s0[3])) + (__uint_as_float(s1[1]) + __uint_as_float(s1[3]));
        float mb, sd;
        ln_scal(me, xsum(m2 + 64.f * ((d0 * d0 + d1 * d1) + (d2 * d2 + d3 * d3))) * (1.f / 512.f), mb, sd);
        if (lane < 32) lds_st128(L, L_BST + (32 * w4 + lane) * 16, (u32x4){pk(-mb, -mb), pk(sd, sd), __float_as_uint(__builtin_amdgcn_rcpf(sd)), 0u});
      }
      lds_wait(L, L_DONE, 4); load_gu(7, gu_slot(7)); WAITV(7); lds_post(a_land, 6); WDTURN(2); WAITV(4); lds_post(a_land, 8);
      lds_wait(L, L_DONE, 5); load_wd(8); load_wd(9); load_wd(10); WAITV(6); WDTURN(4);
      lds_wait(L, L_DONE, 6); load_wd(11); load_wd(12); load_wd(13);
      lds_wait(L, L_DONE, 7); load_wd(14); load_wd(15); load_wd(16); load_wd(17);
      lds_wait(L, L_DONE, 8);
    }
#pragma unroll
    for (int i = 18; i < 23; ++i) load_wd(i);
    WAITV(24);
#pragma unroll
    for (int k = 6; k < 11; ++k) { wp0[k] = lds_tr(L, WD0(k), WD0(k) + 1024); wp1[k] = lds_tr(L, WD1(k), WD1(k) + 1024); }
#pragma unroll
    for (int k = 6; k < 11; ++k) { PIN(wp0[k]); PIN(wp1[k]); }
#pragma unroll
    for (int i = 23; i < 28; ++i) load_wd(i);
    wait_flags(FLAG(WS_FA, (t3 * 4 + 2 * hf + ((lane >> 2) & 1)) * 16 + 4 * w4 + (lane & 3)), tag);
    const u32 ao = WS_ACT + ((t3 * 4 + 2 * hf) * 112 + 28 * w4) * 1024 + lane * 16;
    u32x4 af0[28], af1[28];
    auto ld_act = [&](int i) {
      int ks = i + rot5; ks -= ks >= 28 ? 28 : 0;
      af0[i] = __builtin_amdgcn_raw_buffer_load_b128(rs, ao, ks * 1024, 16);
      af1[i] = __builtin_amdgcn_raw_buffer_load_b128(rs, ao + 112 * 1024, ks * 1024, 16);
    };
#pragma unroll
    for (int i = 0; i < 5; ++i) ld_act(i);
    f32x16 a00 = {}, a01 = {}, a10 = {}, a11 = {};
    static_for<0, 28>([&](auto ic) {
      constexpr int i = ic.value;
      SB();
      a00 = MFMA(wp0[i % 11], asbf(af0[i]), a00);
      a01 = MFMA(wp1[i % 11], asbf(af0[i]), a01);
      SB();
      a10 = MFMA(wp0[i % 11], asbf(af1[i]), a10);
      if constexpr (i < 6) { ld_act(5 + 2 * i); ld_act(6 + 2 * i); }
      else if constexpr (i + 11 < 28) ld_act(i + 11);
      SB();
      a11 = MFMA(wp1[i % 11], asbf(af1[i]), a11);
      if constexpr (i >= 8 && i + 3 < 28) {
        wp0[(i + 3) % 11] = lds_tr(L, WD0(i + 3), WD0(i + 3) + 1024);
        wp1[(i + 3) % 11] = lds_tr(L, WD1(i + 3), WD1(i + 3) + 1024);
      }
      PIN(a00); PIN(a01); PIN(a10); PIN(a11);
    });
    red_write(20 * KB + w4 * 2048, 8192, a00, a01);
    red_write(92 * KB + w4 * 2048, 8192, a10, a11);
  }
  BAR();
  {
    f32x4 lo, hi;
    red_read(mw ? 20 * KB : 60 * KB, 8192, 2048, lo, hi);
    u32 ln = lane_id();
    const f32x4 gb0 = lds_ldf4(L, L_MISC + M_GB + (ln & 7) * 32), gb1 = lds_ldf4(L, L_MISC + M_GB + (ln & 7) * 32 + 16);
    u32x4 o;
    o[0] = pk(bf_lo(x1p[0]) + rb(gb0[0] * rb(lo[0])), bf_hi(x1p[0]) + rb(gb0[1] * rb(lo[1])));
    o[1] = pk(bf_lo(x1p[1]) + rb(gb0[2] * rb(lo[2])), bf_hi(x1p[1]) + rb(gb0[3] * rb(lo[3])));
    o[2] = pk(bf_lo(x1p[2]) + rb(gb1[0] * rb(hi[0])), bf_hi(x1p[2]) + rb(gb1[1] * rb(hi[1])));
    o[3] = pk(bf_lo(x1p[3]) + rb(gb1[2] * rb(hi[2])), bf_hi(x1p[3]) + rb(gb1[3] * rb(hi[3])));
    *(GAS u32x4*)((GAS char*)out + (t3 * 128 + hf * 64 + 8 * w) * 1024 + cg * 128 + (ln >> 3) * 1024 + (ln & 7) * 16) = o;
  }
  asm volatile(".Ladaln_end:");
}

extern "C" void kernel_launch(void* const* d_in, const int* in_sizes, int n_in, void* d_out, int out_size, void* d_ws, size_t ws_size,
                              hipStream_t stream) {
  adaln_block<<<256, 512, 0, stream>>>((const u16*)d_in[0], (const u16*)d_in[1], (const u16*)d_in[2], (const u16*)d_in[4], (const u16*)d_in[5],
                                       (const u16*)d_in[6], (const u16*)d_in[7], (const u16*)d_in[8], (u16*)d_out, (char*)d_ws);
}
```

```cpp
#include <hip/hip_runtime.h>
#include <stdint.h>

typedef unsigned short u16;
typedef unsigned int u32;
typedef unsigned long long u64;
typedef __attribute__((ext_vector_type(8))) __bf16 bf16x8;
typedef __attribute__((ext_vector_type(2))) __bf16 bf16x2;
typedef __attribute__((ext_vector_type(4))) short s16x4;
typedef __attribute__((ext_vector_type(8))) short s16x8;
typedef __attribute__((ext_vector_type(16))) float f32x16;
typedef __attribute__((ext_vector_type(4))) float f32x4;
typedef __attribute__((ext_vector_type(2))) float f32x2;
typedef __attribute__((ext_vector_type(4))) u32 u32x4;
typedef __attribute__((ext_vector_type(2))) u32 u32x2;
#define LAS __attribute__((address_space(3)))
#define GAS __attribute__((address_space(1)))
#define DI __device__ __forceinline__
#define RLX_AGENT __ATOMIC_RELAXED, __HIP_MEMORY_SCOPE_AGENT

constexpr u32 WS_EPOCH = 0;
constexpr u32 WS_ARR = 1024;
constexpr u32 WS_MODG = 4096;
constexpr u32 WS_ST = 65536;
constexpr u32 WS_UV = 196608;
constexpr u32 WS_KV = 1048576;
constexpr u32 WS_O = WS_KV + 4194304;
constexpr u32 WS_X1 = WS_O + 2097152;
constexpr u32 WS_ACT = WS_X1 + 2097152;
constexpr u32 FS = 16, MREP = 8;
constexpr u32 WS_FKV = WS_ACT + 7340032;
constexpr u32 WS_FO = WS_FKV + 1024 * 4 * FS;
constexpr u32 WS_FX = WS_FO + 2048 * 4 * FS;
constexpr u32 WS_FA = WS_FX + 2048 * 4 * FS;
constexpr u32 WS_FUV = WS_FA + 1024 * 4 * FS;
constexpr u32 WS_MOD1 = WS_FUV + 1024 * 4 * FS;
constexpr u32 WS_BYTES = WS_MOD1 + MREP * 4096;
static_assert(WS_UV + 256 * 2048 <= WS_KV && WS_BYTES <= (32u << 20), "workspace map");
#define FLAG(base, i) ((GAS u32*)(ws + (base)) + (i) * FS)

constexpr int SPIN_MAX = 1 << 17;
constexpr u32 KB = 1024;
constexpr u32 L_WO = 0;
constexpr u32 L_KV = 64 * KB;
constexpr u32 L_QX = 144 * KB;
constexpr u32 L_AR1 = 120 * KB;
constexpr u32 L_YL = 0;
constexpr u32 L_MISC = 148 * KB;
constexpr u32 L_C1 = L_MISC, L_SH = L_MISC + 2048, L_C1B = L_MISC + 4096, M_C1B = 4096;
constexpr u32 L_UVT = L_C1, L_BST = L_SH;
constexpr u32 L_ONES = L_MISC + 8192;
constexpr u32 L_GA = L_ONES + 256, M_GA = 8192 + 256, M_GB = 8192 + 512;
constexpr u32 L_DONE = L_ONES + 784, L_LAND = L_ONES + 800, L_MODF = L_ONES + 816;
constexpr u32 L_TAG = L_ONES + 768, L_JUNK = L_ONES + 1536;
constexpr u32 L_RED = L_MISC + 10240;
constexpr u32 LDS_BYTES = 163840;
DI constexpr u32 gu_slot(int c) { return (c & 3) == 3 ? 36 * KB : 64 * KB + (c & 3) * 28 * KB; }
DI constexpr u32 wd_base(int i) {
  return i < 8 ? 20 * KB + (i & 1) * 8192u : i < 11 ? 64 * KB + (i - 8) * 8192u : i < 18 ? 92 * KB + (i - 11) * 8192u : i < 21 ? 36 * KB + (i - 18) * 8192u
       : i < 23 ? (i - 21) * 8192u : i < 25 ? 20 * KB + (i - 23) * 8192u : 64 * KB + (i - 25) * 8192u;
}
constexpr u32 CODE_LINES = 12;

constexpr float NEGBIG = -1e30f;
constexpr float QSCALE = 0.25f * 1.4426950408889634f;

template <int I> struct IC { static constexpr int value = I; };
template <int I, int N, class F> DI void static_for(F&& f) { if constexpr (I < N) { f(IC<I>{}); static_for<I + 1, N>(f); } }
DI u32 lane_id() { u32 r; asm volatile("v_mbcnt_lo_u32_b32 %0, -1, 0\n\tv_mbcnt_hi_u32_b32 %0, -1, %0" : "=v"(r)); return r; }
DI float bf_lo(u32 v) { return __uint_as_float(v << 16); }
DI float bf_hi(u32 v) { return __uint_as_float(v & 0xffff0000u); }
DI u32 pk(float a, float b) { f32x2 v = {a, b}; return __builtin_bit_cast(u32, __builtin_convertvector(v, bf16x2)); }
DI float rb(float a) { return bf_lo(pk(a, a)); }
template <int O> DI u32x4 pack8(const f32x16& a) { return (u32x4){pk(a[O], a[O + 1]), pk(a[O + 2], a[O + 3]), pk(a[O + 4], a[O + 5]), pk(a[O + 6], a[O + 7])}; }
DI bf16x8 asbf(u32x4 v) { return __builtin_bit_cast(bf16x8, v); }
#define MFMA(a, b, c) __builtin_amdgcn_mfma_f32_32x32x16_bf16((a), (b), (c), 0, 0, 0)
template <int CTRL, int RM = 0xf> DI float dpp(float v) { return __builtin_bit_cast(float, __builtin_amdgcn_update_dpp(0, __builtin_bit_cast(int, v), CTRL, RM, 0xf, true)); }
DI float sum8(float v) { v += dpp<0xB1>(v); v += dpp<0x4E>(v); v += dpp<0x141>(v); return v; }
DI float dot2(u32 a, u32 b, float c) { return __builtin_amdgcn_fdot2_f32_bf16(__builtin_bit_cast(bf16x2, a), __builtin_bit_cast(bf16x2, b), c, false); }

DI u32x4 lds_ld128(LAS char* L, u32 off) { return *(LAS u32x4*)(L + off); }
DI f32x4 lds_ldf4(LAS char* L, u32 off) { return *(LAS f32x4*)(L + off); }
DI void lds_st128(LAS char* L, u32 off, u32x4 v) { *(LAS u32x4*)(L + off) = v; }
DI bf16x8 lds_tr(LAS char* L, u32 off0, u32 off1) {
  s16x4 a = __builtin_amdgcn_ds_read_tr16_b64_v4i16((LAS s16x4*)(L + off0));
  s16x4 b = __builtin_amdgcn_ds_read_tr16_b64_v4i16((LAS s16x4*)(L + off1));
  return __builtin_bit_cast(bf16x8, __builtin_shufflevector(a, b, 0, 1, 2, 3, 4, 5, 6, 7));
}
DI u32x4 make_srd(const void* p, u32 bytes) { const u64 a = (u64)p; return (u32x4){(u32)a, (u32)(a >> 32), bytes, 0x00020000u}; }
DI void glds(const u32x4& srd, u32 voff, u32 soff, u32 ldsaddr) {
  asm volatile("s_mov_b32 m0, %3\n\ts_nop 0\n\tbuffer_load_dwordx4 %0, %1, %2 offen lds" ::"v"(voff), "s"(srd), "s"(soff), "s"(ldsaddr) : "memory");
}
DI void glds_sc1(const u32x4& srd, u32 voff, u32 soff, u32 ldsaddr) {
  asm volatile("s_mov_b32 m0, %3\n\ts_nop 0\n\tbuffer_load_dwordx4 %0, %1, %2 offen sc1 lds" ::"v"(voff), "s"(srd), "s"(soff), "s"(ldsaddr) : "memory");
}
DI void touch(const u32x4& srd, u32 voff, u32 ldsaddr) {
  asm volatile("s_mov_b32 m0, %2\n\ts_nop 0\n\tbuffer_load_dword %0, %1, 0 offen lds" ::"v"(voff), "s"(srd), "s"(ldsaddr) : "memory");
}
DI void lds_post(u32 addr, u32 val) {
  asm volatile("s_mov_b64 exec, 1\n\tds_write_b32 %0, %1\n\ts_mov_b64 exec, -1" ::"v"(addr), "v"(val) : "memory");
}
DI bool all_ge(u32x4 v, u32 k) { return __builtin_amdgcn_readfirstlane(min(min(v[0], v[1]), min(v[2], v[3]))) >= k; }
DI void lds_wait(LAS char* L, u32 off, u32 k) {
#pragma nounroll
  for (int it = 0; it < (SPIN_MAX << 4); ++it) {
    if (all_ge(*(volatile LAS u32x4*)(L + off), k)) break;
    __builtin_amdgcn_s_sleep(3);
  }
  asm volatile("" ::: "memory");
}
#define BAR() asm volatile("s_waitcnt lgkmcnt(0)\n\ts_barrier" ::: "memory")
#define WAITV_BAR(N) asm volatile("s_waitcnt vmcnt(%0) lgkmcnt(0)\n\ts_barrier" ::"i"(N) : "memory")
#define WAITV(N) asm volatile("s_waitcnt vmcnt(%0)" ::"i"(N) : "memory")
#define SB() __builtin_amdgcn_sched_barrier(0)
#define PIN(x) asm volatile("" : "+v"(x))
#define PINS(x) asm volatile("" : "+s"(x))
#define ORDER() __builtin_amdgcn_fence(__ATOMIC_ACQUIRE, "wavefront")

DI void wait_flags(const GAS u32* p, u32 tag) {
  u32 v = tag + 1u;
#pragma nounroll
  for (int it = 0; it < SPIN_MAX; ++it) {
    if (v != tag) v = __hip_atomic_load(p, RLX_AGENT);
    if (__all(v == tag)) break;
    __builtin_amdgcn_s_sleep(4);
  }
  ORDER();
}
DI void ln_scal(float mean, float var, float& mb, float& sd) {
  mb = rb(mean);
  sd = rb(__builtin_amdgcn_sqrtf(rb(rb(fmaxf(var, 0.f)) + 1e-5f)));
}
DI float fma1(float a, float b, float c) { float r = fmaf(a, b, c); asm("" : "+v"(r)); return r; }
typedef __attribute__((ext_vector_type(2))) _Float16 f16x2;
DI u32x4 znorm8(u32x4 x, float r, float mr) {
  u32x4 o;
#pragma unroll
  for (int e = 0; e < 4; ++e) {
    const f32x2 v = {fmaf(bf_lo(x[e]), r, -mr), fmaf(bf_hi(x[e]), r, -mr)};
    o[e] = __builtin_bit_cast(u32, __builtin_convertvector(v, f16x2));
  }
  return o;
}
DI u32 xform2(u32 z_, u32 c_, u32 s_) {
  const f16x2 z = __builtin_bit_cast(f16x2, z_), c = __builtin_bit_cast(f16x2, c_), s = __builtin_bit_cast(f16x2, s_);
  return pk(fma1((float)z[0], (float)c[0], (float)s[0]), fma1((float)z[1], (float)c[1], (float)s[1]));
}
DI u32x4 xform8(u32x4 x, u32x4 c, u32x4 s) { return (u32x4){xform2(x[0], c[0], s[0]), xform2(x[1], c[1], s[1]), xform2(x[2], c[2], s[2]), xform2(x[3], c[3], s[3])}; }
DI u32 pkh(float a, float b) { const f32x2 v = {a, b}; return __builtin_bit_cast(u32, __builtin_convertvector(v, f16x2)); }
DI f32x2 geglu2(f32x2 ag, f32x2 au, float k1, float k3, float i2) {
  const f32x2 a = ag * (ag * ag * k3 + k1);
  const f32x2 d = (f32x2){__builtin_amdgcn_exp2f(a[0]), __builtin_amdgcn_exp2f(a[1])} * i2 + i2;
  return ag * au * (f32x2){__builtin_amdgcn_rcpf(d[0]), __builtin_amdgcn_rcpf(d[1])};
}

__global__ __launch_bounds__(512, 2) void adaln_block(const u16* __restrict__ x, const u16* __restrict__ wqkv, const u16* __restrict__ wo,
                                                      const u16* __restrict__ wg, const u16* __restrict__ wu, const u16* __restrict__ wd,
                                                      const u16* __restrict__ cvec, const u16* __restrict__ wm, u16* __restrict__ out,
                                                      char* __restrict__ ws) {
  __shared__ __attribute__((aligned(1024))) char lds[LDS_BYTES];
  LAS char* L = (LAS char*)lds;
  const u32 lbase = (u32)(uintptr_t)L;
  const int tid = threadIdx.x, lane = tid & 63;
  const int w = __builtin_amdgcn_readfirstlane(tid >> 6), w4 = w & 3;
  const bool mw = w < 4;
  const int b = blockIdx.x, xcd = b & 7, ii = b >> 3;
  if (w == 7) {
    u64 pc;
    int len;
    asm volatile("s_getpc_b64 %0\n.Ladaln_pf:\n\ts_mov_b32 %1, .Ladaln_end-.Ladaln_pf" : "=s"(pc), "=s"(len));
    touch(make_srd((const void*)pc, len > 0 ? (u32)len : 0u), ((lane < CODE_LINES ? lane : 0) * 32 + ii) * 128u, lbase + L_JUNK);
  }
  const int u1 = 31 - (8 * (xcd >> 1) + (ii >> 2)), q1 = 4 * (xcd & 1) + (ii & 3);
  const int t3 = 2 * xcd + (ii >> 4), hf = (ii >> 3) & 1, cg = ii & 7;
  const int t4 = 8 * (xcd >> 2) + (ii >> 2), j4 = 4 * (xcd & 3) + (ii & 3);
  const int rot1 = (u1 + 2 * q1) & 7;
  const int rot4 = ii >> 2, rot5 = 7 * (ii >> 3);
  const int r32 = lane & 31, hh = lane >> 5, blk = (lane >> 4) & 1, q4 = (lane & 15) >> 2, p4 = lane & 3, l3 = lane >> 3, l7 = lane & 7;
  const __amdgpu_buffer_rsrc_t rs = __builtin_amdgcn_make_buffer_rsrc(ws, 0, WS_BYTES, 0x00020000);
  u32 tag = 0;
  const int orow = t3 * 128 + hf * 64 + 8 * w + l3;
  const int mg = b;
  const u32x4 srd_q = make_srd(wqkv, 512 * 3072);
  const u32 q8o = l3 * 3072u + ((l7 ^ (((lane >> 4) & 1) << 2)) << 4);
  auto load_wq = [&](int c) {
    const u32 so = ((c + rot1) & 7) * 196608 + w * 1024 + q1 * 128;
#pragma unroll
    for (int e = 0; e < 8; ++e) glds(srd_q, q8o, so + e * 24576, lbase + (c % 5) * 24576 + w * 8192 + e * 1024);
  };
  u32x4 X[16];
  u32x4 xr;
  const int hp = w4 >> 1, hd = w >> 2, qb = (w ^ hd) & 1;
  bf16x8 qf_[15], kf_[15], vf_[15];
  u32 wl[3];
#define P1O(n) (wl[(((n) >> 2) % 5) >> 1] + ((((n) >> 2) % 5) & 1) * 24576 + ((n) & 3) * 2048)
#define P1I(n) ((n) < 12 ? (n) : 12 + (n) % 3)
  u32 cso = L_MISC + l7 * 32;
  PIN(cso);
  const u32x4 srd_g = make_srd(wg, 512 * 3584), srd_u = make_srd(wu, 512 * 3584), srd_d = make_srd(wd, 1792 * 1024), srd_o = make_srd(wo, 512 * 1024);

  u32 guo[7];
  const u32x4 srd_3 = w4 < 2 ? srd_g : srd_u;
  auto load_gu = [&](int c, u32 slot) {
    int r_ = rot4; PINS(r_);
    const u32 so = j4 * 224 + ((c + r_) & 7) * (64 * 3584), ld = lbase + slot + w4 * 1024;
#pragma unroll
    for (int n = 0; n < 7; ++n) glds(n < 3 ? srd_g : n == 3 ? srd_3 : srd_u, guo[n], so, ld + n * 4096);
  };
  const u32 w8o = l3 * 1024u + ((l7 ^ (((lane >> 4) & 1) << 2)) << 4);
  bf16x8 qf;
  if (mw) {
    u32x4 M[16];
    u32 cv = 0;
    if (mg < 48) {
      const GAS char* mb = (const GAS char*)wm + mg * 128 + (128 * w + 16 * l3) * 6144 + l7 * 16;
      cv = ((const GAS u32*)cvec)[64 * w + lane];
#pragma unroll
      for (int n = 0; n < 16; ++n) M[n] = *(const GAS u32x4*)(mb + n * 6144);
    }
    const u32 ep = __hip_atomic_load((const GAS u32*)(ws + WS_EPOCH), RLX_AGENT);
    if (lane < 12) *(LAS u32*)(L + L_DONE + lane * 4) = 0u;
    SB();
    BAR();
    if (mg < 48) {
      {
        const float c0 = bf_lo(cv), c1 = bf_hi(cv);
        *(LAS u32*)(L + L_RED + 1024 + w * 256 + lane * 4) = pk(c0 * __builtin_amdgcn_rcpf(1.0f + __builtin_amdgcn_exp2f(-1.4426950408889634f * c0)),
                                                               c1 * __builtin_amdgcn_rcpf(1.0f + __builtin_amdgcn_exp2f(-1.4426950408889634f * c1)));
      }
      const u32x4 s0 = lds_ld128(L, L_RED + 1024 + w * 256 + l3 * 32), s1 = lds_ld128(L, L_RED + 1024 + w * 256 + l3 * 32 + 16);
      f32x2 a[4] = {};
#pragma unroll
      for (int n = 0; n < 8; ++n) {
        const u32 sp = n < 4 ? s0[n & 3] : s1[n & 3];
#pragma unroll
        for (int e = 0; e < 4; ++e) {
          const u32 r0 = M[2 * n][e], r1 = M[2 * n + 1][e];
          a[e][0] = dot2(__builtin_amdgcn_perm(r1, r0, 0x05040100u), sp, a[e][0]);
          a[e][1] = dot2(__builtin_amdgcn_perm(r1, r0, 0x07060302u), sp, a[e][1]);
        }
      }
      float v[8] = {a[0][0], a[0][1], a[1][0], a[1][1], a[2][0], a[2][1], a[3][0], a[3][1]};
#pragma unroll
      for (int e = 0; e < 8; ++e) {
        v[e] += dpp<0x128>(v[e]);
        auto r1_ = __builtin_amdgcn_permlane16_swap(__float_as_uint(v[e]), __float_as_uint(v[e]), false, false);
        v[e] = __uint_as_float(r1_[0]) + __uint_as_float(r1_[1]);
        auto r2_ = __builtin_amdgcn_permlane32_swap(__float_as_uint(v[e]), __float_as_uint(v[e]), false, false);
        v[e] = __uint_as_float(r2_[0]) + __uint_as_float(r2_[1]);
      }
      if (lane < 8) {
        *(LAS f32x4*)(L + L_RED + w * 256 + lane * 32) = (f32x4){v[0], v[1], v[2], v[3]};
        *(LAS f32x4*)(L + L_RED + w * 256 + lane * 32 + 16) = (f32x4){v[4], v[5], v[6], v[7]};
      }
      lds_post(lbase + L_MODF + w * 4, 1);
      if (w == 3) {
        lds_wait(L, L_MODF, 1);
        float s = 0.f;
#pragma unroll
        for (int k = 0; k < 4; ++k) s += *(LAS float*)(L + L_RED + k * 256 + lane * 4);
        const float o = dpp<0xB1>(s);
        const u32 tg = __builtin_amdgcn_readfirstlane(ep) + 1u;
        const u64 gr = ((u64)tg << 32) | ((lane & 1) ? pk(o, s) : pk(s, o));
        if (mg < 16) {
#pragma unroll
          for (int r = 0; r < MREP / 2 + (MREP & 1); ++r)
            if (2 * r + (lane & 1) < MREP) __hip_atomic_store((GAS u64*)(ws + WS_MOD1) + (2 * r + (lane & 1)) * 512 + 32 * mg + (lane >> 1), gr, RLX_AGENT);
        } else if (!(lane & 1)) __hip_atomic_store((GAS u64*)(ws + WS_MODG) + 32 * mg + (lane >> 1), gr, RLX_AGENT);
      }
    }
    xr = *(const GAS u32x4*)((const GAS char*)x + orow * 1024 + cg * 128 + l7 * 16);
    tag = __builtin_amdgcn_readfirstlane(ep) + 1u;
    if (w < 3) {
      load_wq(0); load_wq(1); load_wq(2); load_wq(3); load_wq(4);
      WAITV(16);
    }
    lds_post(lbase + L_MODF + w * 4, 2);
    wl[0] = (8 * hh + q4) * 128 + ((64 * hp + 32 * blk + 8 * p4) ^ ((q4 >> 1) << 6));
    wl[1] = wl[0] + 48 * KB; wl[2] = wl[0] + 96 * KB;
    PIN(wl[1]); PIN(wl[2]);
    lds_wait(L, L_MODF, 2);
#pragma unroll
    for (int n = 0; n < 12; ++n) { kf_[n] = lds_tr(L, P1O(n) + 8192, P1O(n) + 8704); vf_[n] = lds_tr(L, P1O(n) + 16384, P1O(n) + 16896); qf_[n] = lds_tr(L, P1O(n), P1O(n) + 512); }
    lds_post(lbase + L_MODF + w * 4, 3);
#pragma unroll
    for (int n = 0; n < 12; ++n) { PIN(kf_[n]); PIN(vf_[n]); PIN(qf_[n]); }
    if (w < 3) {
      lds_wait(L, L_MODF, 3);
      load_wq(5); load_wq(6); load_wq(7);
    } else {
      if (lane < 32) lds_st128(L, L_ONES + (lane & 15) * 16 + (lane >> 4) * 1024, (u32x4){0x3f803f80u, 0x3f803f80u, 0x3f803f80u, 0x3f803f80u});
      if (lane == 0) *(LAS u32*)(L + L_TAG) = tag;
      u32x4 v[4];
#pragma nounroll
      for (int it = 0; it < SPIN_MAX; ++it) {
        bool ok = true;
#pragma unroll
        for (int k = 0; k < 4; ++k) { v[k] = __builtin_amdgcn_raw_buffer_load_b128(rs, lane * 16, WS_MOD1 + (xcd % MREP) * 4096 + k * 1024, 16); ok &= v[k][1] == tag && v[k][3] == tag; }
        if (__all(ok)) break;
        __builtin_amdgcn_s_sleep(2);
      }
#pragma unroll
      for (int k = 0; k < 2; ++k) {
        const u32 o = L_C1 + ((lane + 64 * k) >> 1) * 32 + (lane & 1) * 8;
        *(LAS u32x2*)(L + o + 16) = (u32x2){pkh(bf_lo(v[k][0]), bf_hi(v[k][0])), pkh(bf_lo(v[k][2]), bf_hi(v[k][2]))};
        *(LAS u32x2*)(L + o) = (u32x2){pkh(rb(1.f + bf_lo(v[2 + k][0])), rb(1.f + bf_hi(v[2 + k][0]))), pkh(rb(1.f + bf_lo(v[2 + k][2])), rb(1.f + bf_hi(v[2 + k][2])))};
      }
    }
    BAR();
    f32x16 aq = {}, ak = {}, av = {};
    u32 ard[4];
    {
      const int row = 32 * qb + r32, f = (row & 7) ^ ((row >> 3) & 1);
#pragma unroll
      for (int s = 0; s < 4; ++s) { ard[s] = L_AR1 + row * 128 + ((((2 * s) | hh) ^ f) << 4); PIN(ard[s]); }
    }
    bf16x8 hf_[3];
#define P1H(n) hf_[(n) % 3] = asbf(lds_ld128(L, ard[(n) & 3] + (((n) >> 2) % 3) * 8192))
    BAR();
    P1H(0); P1H(1);
    static_for<0, 32>([&](auto nc) {
      constexpr int n = nc.value, m = n + 2;
      constexpr bool bnd = (m & 3) == 0, rd = m >= 12 && m < 32;
      SB();
      ak = MFMA(kf_[P1I(n)], hf_[n % 3], ak);
      if constexpr (m < 32 && !bnd) { P1H(m); if constexpr (rd) kf_[P1I(m)] = lds_tr(L, P1O(m) + 8192, P1O(m) + 8704); }
      SB();
      av = MFMA(hf_[n % 3], vf_[P1I(n)], av);
      if constexpr (m < 32 && bnd) {
        PIN(ak); PIN(av);
        WAITV_BAR(m >= 12 ? 56 - 2 * m : 63);
        P1H(m);
        if constexpr (rd) kf_[P1I(m)] = lds_tr(L, P1O(m) + 8192, P1O(m) + 8704);
      }
      if constexpr (rd) vf_[P1I(m)] = lds_tr(L, P1O(m) + 16384, P1O(m) + 16896);
      SB();
      aq = MFMA(qf_[P1I(n)], hf_[n % 3], aq);
      if constexpr (rd) qf_[P1I(m)] = lds_tr(L, P1O(m), P1O(m) + 512);
      PIN(ak); PIN(av); PIN(aq);
    });
    const u32x4 f0 = pack8<0>(ak), f1 = pack8<8>(ak), f2 = pack8<0>(av), f3 = pack8<8>(av);
    const int kvi = ((u1 >> 1) * 16 + 2 * q1 + hp) * 4 + 2 * (u1 & 1) + qb;
    const u32 o = WS_KV + kvi * 4096 + lane * 16;
    __builtin_amdgcn_raw_buffer_store_b128(f0, rs, o, 0, 16);
    __builtin_amdgcn_raw_buffer_store_b128(f1, rs, o + 1024, 0, 16);
    __builtin_amdgcn_raw_buffer_store_b128(f2, rs, o + 2048, 0, 16);
    __builtin_amdgcn_raw_buffer_store_b128(f3, rs, o + 3072, 0, 16);
    aq *= QSCALE;
    qf = asbf(pack8<0>(aq));
    const u32x4 f5 = pack8<8>(aq);
    BAR();
    const u32 ok = L_KV + hp * 40960 + (8 + qb) * 4096 + lane * 16;
    lds_st128(L, ok, f0); lds_st128(L, ok + 1024, f1); lds_st128(L, ok + 2048, f2); lds_st128(L, ok + 3072, f3);
    lds_st128(L, L_QX + w * 1024 + lane * 16, f5);
    BAR();
    WAITV(0);
    if (lane == 0) __hip_atomic_store(FLAG(WS_FKV, kvi), tag, RLX_AGENT);
  } else {
    const __amdgpu_buffer_rsrc_t rx = __builtin_amdgcn_make_buffer_rsrc((void*)x, 0, 2048 * 1024, 0x00020000);
    if (mg < 48) BAR();
#pragma unroll
    for (int c = 0; c < 8; ++c)
#pragma unroll
      for (int g = 0; g < 2; ++g)
        X[2 * c + g] = __builtin_amdgcn_raw_buffer_load_b128(rx, l3 * 1024u + l7 * 16u, (u1 * 64 + 16 * w4 + 8 * g) * 1024 + ((c + rot1) & 7) * 128, 0);
    xr = *(const GAS u32x4*)((const GAS char*)x + orow * 1024 + cg * 128 + l7 * 16);
    SB();
    if (mg >= 48) BAR();
    float sm[2] = {}, sq[2] = {};
#pragma unroll
    for (int c = 0; c < 8; ++c) {
      SB();
#pragma unroll
      for (int e = 0; e < 4; ++e)
#pragma unroll
        for (int g = 0; g < 2; ++g) { sm[g] = dot2(X[2 * c + g][e], 0x3f803f80u, sm[g]); sq[g] = dot2(X[2 * c + g][e], X[2 * c + g][e], sq[g]); }
    }
    float rr[2], mr[2];
#pragma unroll
    for (int g = 0; g < 2; ++g) {
      const float me = sum8(sm[g]) * (1.f / 512.f);
      float mb, sd;
      ln_scal(me, sum8(sq[g]) * (1.f / 512.f) - me * me, mb, sd);
      rr[g] = __builtin_amdgcn_rcpf(sd); mr[g] = mb * rr[g];
    }
#pragma unroll
    for (int n = 0; n < 16; ++n) { X[n] = znorm8(X[n], rr[n & 1], mr[n & 1]); PIN(X[n]); }
    if (w4 < 3) {
      const u32x4 srd_a = make_srd(w4 == 0 ? wo : w4 == 1 ? wg : wu, w4 == 0 ? 512 * 1024 : 512 * 3584);
      const int e1 = 64 + lane;
      const u32 ta0 = w4 ? (16 * ii + lane / 7) * 3584 + (xcd & 3) * 896 + (lane % 7) * 128 : ii * 16384 + lane * 128;
      const u32 ta1 = w4 ? (lane < 48 ? (16 * ii + e1 / 7) * 3584 + (xcd & 3) * 896 + (e1 % 7) * 128 : 0x7fffffffu) : ta0 + 8192;
      touch(srd_a, ta0, lbase + L_JUNK);
      touch(srd_a, ta1, lbase + L_JUNK);
    }
    BAR();
    tag = __builtin_amdgcn_readfirstlane(*(LAS u32*)(L + L_TAG));
    if (w == 4 && lane == 0) __hip_atomic_store((GAS u32*)(ws + WS_ARR) + b, tag, RLX_AGENT);
    const u32 awE = L_AR1 + (16 * w4 + l3) * 128 + ((l7 ^ l3) << 4), awO = (awE ^ 16u) + 1024;
    u32x4 ca[2], sa[2];
#define CSLOAD(c) do { const u32 o_ = cso + (((c) + rot1) & 7) * 256; ca[(c) & 1] = lds_ld128(L, o_); sa[(c) & 1] = lds_ld128(L, o_ + 16); } while (0)
    CSLOAD(0);
#pragma unroll
    for (int c = 0; c < 8; ++c) {
      if (c < 7) CSLOAD(c + 1);
      SB();
      PIN(X[2 * c]);
      lds_st128(L, awE + (c % 3) * 8192, xform8(X[2 * c], ca[c & 1], sa[c & 1]));
      PIN(X[2 * c + 1]);
      lds_st128(L, awO + (c % 3) * 8192, xform8(X[2 * c + 1], ca[c & 1], sa[c & 1]));
      if (c == 0) BAR();
      if (c >= 2) asm volatile("s_waitcnt lgkmcnt(2)\n\ts_barrier" ::: "memory");
    }
    BAR();
#pragma unroll
    for (int n = 0; n < 7; ++n) {
      const int pc = w4 + 4 * n, up = pc >= 14, e = (pc - 14 * up) * 64 + lane, rho = e / 14, cc = e - 14 * rho;
      guo[n] = (u32)((up ? (rho ^ 4) : rho) * 3584 + cc * 16);
    }
    BAR();
    BAR();
    qf = asbf(lds_ld128(L, L_QX + (w4 ^ 1) * 1024 + lane * 16));
  }
  PIN(xr);
  if (!mw && qb) {
    for (int n = hp * 32; n < hp * 32 + 32; ++n) glds(srd_o, w8o, cg * 128 + n * 8192, lbase + L_WO + n * 1024);
  }

  {
    const u32 ka = L_KV + hp * 40960 + hd * 1024 + lane * 16;
    const bool vdat = (r32 >> 4) == hd;
    const u32 va = vdat ? L_KV + hp * 40960 + 2048 + lane * 16 : L_ONES + (lane & 15) * 16;
    const u32 vs = vdat ? 4096u : 0u;
    f32x16 cm;
#pragma unroll
    for (int g = 0; g < 16; ++g) cm[g] = (g & 3) + 8 * (g >> 2) + 4 * hh <= r32 ? 0.f : NEGBIG;
    const f32x16 zero = {};
    f32x16 O = {};
    float m = NEGBIG;
    auto rowmax = [&](const f32x16& s, float mm) {
#pragma unroll
      for (int g = 0; g < 16; g += 2) mm = fmaxf(fmaxf(mm, s[g]), s[g + 1]);
      return mm;
    };
    auto ldk = [&](int lb) { return asbf(lds_ld128(L, ka + lb * 4096)); };
#pragma nounroll
    for (int st = 0; st < 2; ++st) {
      int lm = 8 + qb, lo = 8, hi = 8 + qb;
      if (st) {
        if (u1 == 0) break;
        if (!qb) {
          const int g0 = 2 * u1 - 8 + 4 * hd, gl = g0 + (lane & 3);
          wait_flags(FLAG(WS_FKV, gl < 0 ? ((u1 >> 1) * 16 + 2 * q1 + hp) * 4 + 2 * (u1 & 1) : ((gl >> 2) * 16 + 2 * q1 + hp) * 4 + (gl & 3)), tag);
          const u32x4 srd_w = make_srd(ws, WS_BYTES);
#pragma unroll
          for (int k = 0; k < 4; ++k) {
            const int g = g0 + k;
            const u32 so = g < 0 ? 0x7ff00000u : WS_KV + (((g >> 2) * 16 + 2 * q1 + hp) * 4 + (g & 3)) * 4096;
#pragma unroll
            for (int n = 0; n < 4; ++n) glds_sc1(srd_w, lane * 16, so + n * 1024, lbase + L_KV + hp * 40960 + hd * 16384 + (4 * k + n) * 1024);
          }
          WAITV(0);
        }
        const int lmin = 8 - 2 * u1;
#pragma unroll
        for (int g = 0; g < 16; ++g) { float u = lmin > qb ? NEGBIG : NEGBIG - cm[g]; asm("" : "+v"(u)); cm[g] = u; }
        lm = qb; lo = lmin > qb ? lmin : qb + 1; hi = 8;
        BAR();
      }
      bf16x8 kA = ldk(lo), kB = ldk(lo + 1);
      f32x16 sA = MFMA(ldk(lm), qf, cm), sB, sC;
      const bf16x8 k0 = kA;
      float mn = m;
      for (int lb = lo; lb < hi; lb += 2) {
        sB = MFMA(kA, qf, zero);
        sC = MFMA(kB, qf, zero);
        kA = ldk(lb + 2); kB = ldk(lb + 3);
        SB();
        mn = rowmax(sB, mn);
        if (lb + 1 < hi) mn = rowmax(sC, mn);
      }
      mn = rowmax(sA, mn);
      {
        auto r_ = __builtin_amdgcn_permlane32_swap(__float_as_uint(mn), __float_as_uint(mn), false, false);
        mn = fmaxf(__uint_as_float(r_[0]), __uint_as_float(r_[1]));
      }
      u32 vv = va + lm * vs;
      bf16x8 v0 = asbf(lds_ld128(L, vv)), v1 = asbf(lds_ld128(L, vv + 1024));
      f32x16 negm;
#pragma unroll
      for (int g = 0; g < 16; ++g) negm[g] = -mn;
#pragma unroll
      for (int g = 0; g < 16; ++g) { float u = sA[g] - mn; asm("" : "+v"(u)); sA[g] = u; }
      if (st) {
        const float al = __builtin_amdgcn_exp2f(m - mn);
#pragma unroll
        for (int g = 0; g < 16; ++g) { float o = O[g] * al; asm("" : "+v"(o)); O[g] = o; }
      }
      m = mn;
      kA = k0;
      vv = va + lo * vs;
#define P2STEP(CUR, NXT)                                                                      \
  {                                                                                           \
    NXT = MFMA(kA, qf, negm);                                                                 \
    PIN(NXT);                                                                                 \
    kA = ldk(lb + 1);                                                                         \
    const bf16x8 n0_ = asbf(lds_ld128(L, vv)), n1_ = asbf(lds_ld128(L, vv + 1024));           \
    vv += vs;                                                                                 \
    SB();                                                                                     \
    _Pragma("unroll") for (int g = 0; g < 8; ++g) CUR[g] = __builtin_amdgcn_exp2f(CUR[g]);    \
    O = MFMA(v0, asbf(pack8<0>(CUR)), O);                                                     \
    _Pragma("unroll") for (int g = 8; g < 16; ++g) CUR[g] = __builtin_amdgcn_exp2f(CUR[g]);   \
    O = MFMA(v1, asbf(pack8<8>(CUR)), O);                                                     \
    SB();                                                                                     \
    v0 = n0_; v1 = n1_;                                                                       \
  }
      for (int lb = lo;; ++lb) {
        P2STEP(sA, sB);
        if (lb >= hi) break;
        ++lb;
        P2STEP(sB, sA);
        if (lb >= hi) break;
      }
    }
    u32x4 of;
    {
      const float inv = __builtin_amdgcn_rcpf(hd ? O[0] : O[8]);
      float o[8];
#pragma unroll
      for (int g = 0; g < 8; ++g) { o[g] = (hd ? O[8 + g] : O[g]) * inv; asm("" : "+v"(o[g])); }
      of = (u32x4){pk(o[0], o[1]), pk(o[2], o[3]), pk(o[4], o[5]), pk(o[6], o[7])};
    }
    const int oi = (2 * u1 + qb) * 32 + 4 * q1 + 2 * hp + hd;
    __builtin_amdgcn_raw_buffer_store_b128(of, rs, WS_O + oi * 1024 + lane * 16, 0, 16);
    WAITV(0);
    if (lane == 0) __hip_atomic_store(FLAG(WS_FO, oi), tag, RLX_AGENT);
  }
  {
    if (u1 == 0 && q1 == 0 && w == 7) {
#pragma nounroll
      for (int it = 0; it < SPIN_MAX; ++it) {
        bool ok = true;
#pragma unroll
        for (int k = 0; k < 4; ++k) ok &= __hip_atomic_load((const GAS u32*)(ws + WS_ARR) + lane + 64 * k, RLX_AGENT) == tag;
        if (__all(ok)) break;
        __builtin_amdgcn_s_sleep(1);
      }
      if (lane == 0) __hip_atomic_store((GAS u32*)(ws + WS_EPOCH), tag, RLX_AGENT);
    }
  }
  BAR();

  const u32 wt0 = (4 * hh + q4) * 128 + ((32 * blk + 8 * p4) ^ ((q4 >> 1) << 6)), wt1 = wt0 ^ 64u;
  auto red_write = [&](u32 base, u32 S8, const f32x16& a0, const f32x16& a1) {
    u32 ln = lane_id();
    const u32 o = base + ((ln & 31) >> 3) * S8 + (ln & 7) * 256, sw = (ln & 7) ^ (ln >> 5);
#pragma unroll
    for (int g = 0; g < 4; ++g) {
      *(LAS f32x4*)(L + o + (((2 * g) ^ sw) << 4)) = (f32x4){a0[4 * g], a0[4 * g + 1], a0[4 * g + 2], a0[4 * g + 3]};
      *(LAS f32x4*)(L + o + (((2 * g) ^ sw) << 4) + 128) = (f32x4){a1[4 * g], a1[4 * g + 1], a1[4 * g + 2], a1[4 * g + 3]};
    }
  };
  auto red_read = [&](u32 base, u32 S8, u32 SK, f32x4& lo, f32x4& hi) {
    u32 ln = lane_id();
    const u32 o0 = base + w * S8 + (ln >> 3) * 256 + (((2 * (ln & 7)) ^ (ln >> 3)) << 4), o1 = o0 ^ 16u;
    lo = lds_ldf4(L, o0); hi = lds_ldf4(L, o1);
#pragma unroll
    for (int k = 1; k < 4; ++k) { lo += lds_ldf4(L, o0 + k * SK); hi += lds_ldf4(L, o1 + k * SK); }
  };
  u32x4 x1p;
  {
    if (mw) {
      bf16x8 wa[8], wb[8];
#pragma unroll
      for (int i = 0; i < 8; ++i) {
        const u32 o = L_WO + w * 16384 + i * 2048;
        wa[i] = lds_tr(L, o + wt0, o + wt0 + 1024); wb[i] = lds_tr(L, o + wt1, o + wt1 + 1024);
      }
      wait_flags(FLAG(WS_FO, (t3 * 4 + 2 * hf + (l3 & 1)) * 32 + 8 * w + l7), tag);
      u32x4 of0[8], of1[8];
      const u32 oo = WS_O + ((t3 * 4 + 2 * hf) * 32 + 8 * w) * 1024 + lane * 16;
#pragma unroll
      for (int i = 0; i < 8; ++i) {
        of0[i] = __builtin_amdgcn_raw_buffer_load_b128(rs, oo + i * 1024, 0, 16);
        of1[i] = __builtin_amdgcn_raw_buffer_load_b128(rs, oo + 32768 + i * 1024, 0, 16);
      }
      SB();
      f32x16 a00 = {}, a01 = {}, a10 = {}, a11 = {};
#pragma unroll
      for (int i = 0; i < 8; ++i) {
        a00 = MFMA(wa[i], asbf(of0[i]), a00); a01 = MFMA(wb[i], asbf(of0[i]), a01);
        a10 = MFMA(wa[i], asbf(of1[i]), a10); a11 = MFMA(wb[i], asbf(of1[i]), a11);
      }
      red_write(L_WO + w * 16384, 2048, a00, a01);
      red_write(L_WO + w * 16384 + 8192, 2048, a10, a11);
    } else {
      load_gu(0, gu_slot(0));
      u32x4 bv[2];
      {
        u32x4 q[4];
        u32 v[3];
        const GAS u64* g = (const GAS u64*)(ws + WS_MODG);
        const u32 qo = (((r32 & 1) ? 768 : 1024) + rot4 * 32 + (t4 >> 3) * 16 + 4 * hh) * 8;
#pragma nounroll
        for (int it = 0; it < SPIN_MAX; ++it) {
          const u64 x1_ = __hip_atomic_load(g + 1024 + (tid - 256), RLX_AGENT);
          const u64 x2 = __hip_atomic_load(g + (lane < 32 ? 512 : 1248) + cg * 32 + lane, RLX_AGENT);
          bool ok = (u32)(x1_ >> 32) == tag && (u32)(x2 >> 32) == tag;
#pragma unroll
          for (int k = 0; k < 4; ++k) { q[k] = __builtin_amdgcn_raw_buffer_load_b128(rs, qo + (k >> 1) * 64 + (k & 1) * 16, WS_MODG, 16); ok &= q[k][1] == tag && q[k][3] == tag; }
          v[1] = (u32)x1_; v[2] = (u32)x2;
          if (__all(ok)) break;
          __builtin_amdgcn_s_sleep(1);
        }
        *(LAS f32x2*)(L + L_C1B + (tid - 256) * 8) = (f32x2){rb(1.f + bf_lo(v[1])), rb(1.f + bf_hi(v[1]))};
        if (w == 4) *(LAS f32x2*)(L + L_GA + lane * 8) = (f32x2){bf_lo(v[2]), bf_hi(v[2])};
        const float one = (r32 & 1) ? 0.f : 1.f;
#pragma unroll
        for (int s = 0; s < 2; ++s)
          bv[s] = (u32x4){pk(one + bf_lo(q[2 * s][0]), one + bf_hi(q[2 * s][0])), pk(one + bf_lo(q[2 * s][2]), one + bf_hi(q[2 * s][2])),
                          pk(one + bf_lo(q[2 * s + 1][0]), one + bf_hi(q[2 * s + 1][0])), pk(one + bf_lo(q[2 * s + 1][2]), one + bf_hi(q[2 * s + 1][2]))};
      }
      lds_post(lbase + L_LAND + w4 * 4, 1);
      load_gu(1, gu_slot(1));
      lds_wait(L, L_LAND, 1);
      {
        const u32 gl = blk * 14336 + (8 * hh + q4) * 224 + 8 * p4 + w4 * 32 + (t4 >> 3) * 7168 + gu_slot(0);
        const u32 g0 = gl + (blk ? 896u : 0u), g1 = gl + (blk ? 0u : 896u);
        f32x16 a0 = {}, a1 = {};
#pragma unroll
        for (int s = 0; s < 2; ++s) {
          a0 = MFMA(lds_tr(L, g0 + s * 3584, g1 + s * 3584), asbf(bv[s]), a0);
          a1 = MFMA(lds_tr(L, g0 + s * 3584 + 128, g1 + s * 3584 + 128), asbf(bv[s]), a1);
        }
        if (r32 < 2) {
          const u32 o = WS_UV + (j4 * 16 + t4) * 2048 + w4 * 256 + r32 * 128 + hh * 16;
#pragma unroll
          for (int g = 0; g < 4; ++g) {
            __builtin_amdgcn_raw_buffer_store_b128(__builtin_bit_cast(u32x4, (f32x4){a0[4 * g], a0[4 * g + 1], a0[4 * g + 2], a0[4 * g + 3]}), rs, o + g * 32, 0, 16);
            if (w4 < 3) __builtin_amdgcn_raw_buffer_store_b128(__builtin_bit_cast(u32x4, (f32x4){a1[4 * g], a1[4 * g + 1], a1[4 * g + 2], a1[4 * g + 3]}), rs, o + 1024 + g * 32, 0, 16);
          }
        }
      }
      load_gu(2, gu_slot(2));
    }
    BAR();
    f32x4 lo, hi;
    red_read(L_WO, 2048, 16384, lo, hi);
    const f32x4 ga0 = lds_ldf4(L, cso + M_GA), ga1 = lds_ldf4(L, cso + M_GA + 16);
    float v[8];
    v[0] = rb(bf_lo(xr[0]) + rb(ga0[0] * rb(lo[0]))); v[1] = rb(bf_hi(xr[0]) + rb(ga0[1] * rb(lo[1])));
    v[2] = rb(bf_lo(xr[1]) + rb(ga0[2] * rb(lo[2]))); v[3] = rb(bf_hi(xr[1]) + rb(ga0[3] * rb(lo[3])));
    v[4] = rb(bf_lo(xr[2]) + rb(ga1[0] * rb(hi[0]))); v[5] = rb(bf_hi(xr[2]) + rb(ga1[1] * rb(hi[1])));
    v[6] = rb(bf_lo(xr[3]) + rb(ga1[2] * rb(hi[2]))); v[7] = rb(bf_hi(xr[3]) + rb(ga1[3] * rb(hi[3])));
    x1p = (u32x4){pk(v[0], v[1]), pk(v[2], v[3]), pk(v[4], v[5]), pk(v[6], v[7])};
    PIN(x1p);
    {
      const f32x4 c0 = lds_ldf4(L, cso + M_C1B + cg * 256), c1 = lds_ldf4(L, cso + M_C1B + cg * 256 + 16);
      const u32x4 y = {pk(v[0] * c0[0], v[1] * c0[1]), pk(v[2] * c0[2], v[3] * c0[3]), pk(v[4] * c1[0], v[5] * c1[1]), pk(v[6] * c1[2], v[7] * c1[3])};
      __builtin_amdgcn_raw_buffer_store_b128(y, rs, WS_X1 + ((t3 * 4 + 2 * hf + (w >> 2)) * 32 + 4 * cg + (l7 >> 1)) * 1024 + ((l7 & 1) * 32 + 8 * w4 + l3) * 16, 0, 16);
    }
    const float sm = sum8(((v[0] + v[1]) + (v[2] + v[3])) + ((v[4] + v[5]) + (v[6] + v[7])));
    const float mu = sm * (1.f / 64.f);
    float m2 = 0.f;
#pragma unroll
    for (int k = 0; k < 8; ++k) m2 = fmaf(v[k] - mu, v[k] - mu, m2);
    m2 = sum8(m2);
    if (l7 == 0) __hip_atomic_store((GAS u64*)(ws + WS_ST) + orow * 8 + cg, ((u64)__float_as_uint(m2) << 32) | __float_as_uint(sm), RLX_AGENT);
    WAITV(0);
    if (lane == 0) __hip_atomic_store(FLAG(WS_FX, (t3 * 4 + 2 * hf + (w >> 2)) * 32 + cg * 4 + w4), tag, RLX_AGENT);
    if (!mw && lane == 1) __hip_atomic_store(FLAG(WS_FUV, j4 * 64 + t4 * 4 + w4), tag, RLX_AGENT);
  }
  BAR();

  if (mw) {
    u32 ln = lane_id();
    const u32 bk = (ln >> 4) & 1, gl = bk * 14336 + (8 * (ln >> 5) + ((ln & 15) >> 2)) * 224 + 8 * (ln & 3);
    u32 g0 = gl + (bk ? 896u : 0u) + 36 * KB, g1 = gl + (bk ? 0u : 896u) + 36 * KB, g0h = g0 + 56 * KB, g1h = g1 + 56 * KB;
    PIN(g0); PIN(g1); PIN(g0h); PIN(g1h);
    f32x16 ag[7] = {};
    bf16x8 wf_[7];
    u32x4 yf[32];
#define P4S(n) (((n) >> 2) & 3)
#define P4O(n) ((P4S(n) & 1 ? 0 : 28 * KB) + ((n) & 3) * 3584)
#define P4W(n, f) wf_[f] = lds_tr(L, (P4S(n) == 1 || P4S(n) == 2 ? g0h : g0) + P4O(n) + (f) * 32, (P4S(n) == 1 || P4S(n) == 2 ? g1h : g1) + P4O(n) + (f) * 32)
    bf16x8 pre[3][7];
#pragma unroll
    for (int n = 0; n < 4; ++n)
#pragma unroll
      for (int f = 0; f < 7; ++f) { P4W(n, f); if (n < 3) pre[n][f] = wf_[f]; }
    const u32 a_done = lbase + L_DONE + w * 4;
    lds_post(a_done, 1);

    wait_flags(FLAG(WS_FX, (t4 * 4 + w) * 32 + (ln & 31)), tag);
    auto ld_y = [&](int n) { yf[n] = __builtin_amdgcn_raw_buffer_load_b128(rs, ln * 16, WS_X1 + ((t4 * 4 + w) * 32 + ((n + 4 * rot4) & 31)) * 1024, 16); };
#pragma unroll
    for (int n = 0; n < 5; ++n) ld_y(n);
    u32x4 land;
    static_for<0, 32>([&](auto nc) {
      constexpr int n = nc.value, m = n + 1;
      if constexpr (n + 2 >= 27 && n + 2 < 32) yf[n + 2] = lds_ld128(L, L_YL + (w * 5 + (n + 2 - 27)) * 1024 + ln * 16);
#pragma unroll
      for (int f = 0; f < 7; ++f) {
        SB();
        ag[f] = MFMA(n < 3 ? pre[n < 3 ? n : 0][f] : wf_[f], asbf(yf[n]), ag[f]);
        if ((m & 3) == 0 && f == 0) {
          if (m > 4) lds_post(a_done, m / 4);
          if (m >= 12) { PIN(ag[0]); if (!all_ge(land, m / 4)) lds_wait(L, L_LAND, m / 4); }
        }
        if ((m & 3) == 3 && m >= 11 && f == 0) land = *(volatile LAS u32x4*)(L + L_LAND);
        if (m >= 4 && m < 32) P4W(m, f);
        if constexpr (n < 22) { if (f == 1) ld_y(5 + n); }
      }
#pragma unroll
      for (int f = 0; f < 7; ++f) PIN(ag[f]);
    });
    const u32x4 bst = lds_ld128(L, L_BST + (32 * w + (ln & 31)) * 16);
    const u32x4 bfr = {ln < 32 ? bst[0] : 0u, ln < 32 ? bst[1] : 0u, 0u, 0u};
#pragma unroll
    for (int f = 0; f < 7; ++f) {
      const u32x2 uv = *(LAS u32x2*)(L + L_UVT + (f * 32 + (ln & 31)) * 8);
      ag[f] = MFMA(asbf((u32x4){uv[0], uv[1], 0u, 0u}), asbf(bfr), ag[f]);
    }
    const float rs_ = __uint_as_float(bst[2]), sd_ = bf_lo(bst[1]);
    const float k1 = -2.0f * 0.7978845608028654f * 1.4426950408889634f * rs_, k3 = k1 * 0.044715f * rs_ * rs_, i2 = sd_ * sd_;
    ln = lane_id();
    const u32 ao = WS_ACT + ((t4 * 4 + w) * 112 + 7 * j4) * 1024 + ln * 16;
#pragma unroll
    for (int f = 0; f < 7; ++f) {
      u32x4 o;
#pragma unroll
      for (int k = 0; k < 4; ++k) { const f32x2 a = geglu2((f32x2){ag[f][2 * k], ag[f][2 * k + 1]}, (f32x2){ag[f][8 + 2 * k], ag[f][9 + 2 * k]}, k1, k3, i2); o[k] = pk(a[0], a[1]); }
      __builtin_amdgcn_raw_buffer_store_b128(o, rs, ao + f * 1024, 0, 16);
    }
    WAITV(0);
    if (ln == 0) __hip_atomic_store(FLAG(WS_FA, (t4 * 4 + w) * 16 + j4), tag, RLX_AGENT);
  } else {
    const u32 lane = lane_id(), r32 = lane & 31, hh = lane >> 5;
    const u32 w8o = (lane >> 3) * 1024u + (((lane & 7) ^ (((lane >> 4) & 1) << 2)) << 4);
    const u32 wt0 = (4 * hh + ((lane & 15) >> 2)) * 128 + ((32 * ((lane >> 4) & 1) + 8 * (lane & 3)) ^ (((lane & 15) >> 3) << 6)), wt1 = wt0 ^ 64u;
    auto load_wd = [&](int i) {
      int ks = rot5; PINS(ks);
      ks += i; ks -= ks >= 28 ? 28 : 0;
      const u32 so = cg * 128 + (w4 * 448 + ks * 16) * 1024, ld = lbase + wd_base(i) + w4 * 2048;
      glds(srd_d, w8o, so, ld);
      glds(srd_d, w8o, so + 8192, ld + 1024);
    };
    const u32 a_land = lbase + L_LAND + w4 * 4;
    bf16x8 wp0[11], wp1[11];
    u32 dC0 = w4 * 2048 + wt0, dC1 = w4 * 2048 + wt1, dB0 = dC0 + 92 * KB, dB1 = dC1 + 92 * KB, dA0 = dC0 + 64 * KB, dA1 = dC1 + 64 * KB;
    PIN(dA0); PIN(dA1); PIN(dB0); PIN(dB1); PIN(dC0); PIN(dC1);
#define WD0(i) (wd_base(i) < 64 * KB ? dC0 + wd_base(i) : wd_base(i) < 92 * KB ? dA0 + (wd_base(i) - 64 * KB) : dB0 + (wd_base(i) - 92 * KB))
#define WD1(i) (wd_base(i) < 64 * KB ? dC1 + wd_base(i) : wd_base(i) < 92 * KB ? dA1 + (wd_base(i) - 64 * KB) : dB1 + (wd_base(i) - 92 * KB))
#define WDTURN(i)                                                                                                                   \
  do {                                                                                                                              \
    _Pragma("unroll") for (int k = (i); k < (i) + 2; ++k) { wp0[k] = lds_tr(L, WD0(k), WD0(k) + 1024); wp1[k] = lds_tr(L, WD1(k), WD1(k) + 1024); } \
    _Pragma("unroll") for (int k = (i); k < (i) + 2; ++k) { PIN(wp0[k]); PIN(wp1[k]); }                                             \
    load_wd((i) + 2); load_wd((i) + 3);                                                                                             \
  } while (0)
    load_gu(3, gu_slot(3));
    lds_wait(L, L_DONE, 1);
    load_gu(4, gu_slot(4));
    load_wd(0); load_wd(1);
    WAITV(11); lds_post(a_land, 3);
    wait_flags(FLAG(WS_FUV, j4 * 64 + lane), tag);
    {
      u32x4 pt[16];
      const u32 o = lane < 28 ? (w4 * 28 + lane) * 16 : 0x7fffffffu;
#pragma unroll
      for (int k = 0; k < 16; ++k) pt[k] = __builtin_amdgcn_raw_buffer_load_b128(rs, o, WS_UV + (j4 * 16 + k) * 2048, 16);
      SB();
      f32x4 a = {};
#pragma unroll
      for (int k = 0; k < 16; ++k) {
        const f32x4 t_ = __builtin_bit_cast(f32x4, pt[k]);
#pragma unroll
        for (int e = 0; e < 4; ++e) { float y_ = a[e] + t_[e]; asm("" : "+v"(y_)); a[e] = y_; }
      }
      if (lane < 28) {
        const u32 vi = (w4 * 28 + lane) * 4;
#pragma unroll
        for (int e = 0; e < 4; ++e) {
          const float hi = rb(a[e]);
          *(LAS u32*)(L + L_UVT + ((vi >> 6) * 32 + (vi & 31) + e) * 8 + ((vi >> 5) & 1) * 4) = pk(hi, a[e] - hi);
        }
      }
    }
    {
      lds_wait(L, L_DONE, 2);
      const u32 so = (t4 * 128 + 32 * w4 + r32) * 64 + hh * 32;
      const u32x4 s0 = __builtin_amdgcn_raw_buffer_load_b128(rs, so, WS_ST, 16), s1 = __builtin_amdgcn_raw_buffer_load_b128(rs, so + 16, WS_ST, 16);
      SB();
      load_gu(5, gu_slot(5));
      const u32x4 srd_w = make_srd(ws, WS_BYTES);
#pragma unroll
      for (int k = 0; k < 5; ++k) glds_sc1(srd_w, lane * 16, WS_X1 + ((t4 * 4 + w4) * 32 + ((27 + k + 4 * rot4) & 31)) * 1024, lbase + L_YL + (w4 * 5 + k) * 1024);
      WAITV(12); lds_post(a_land, 4); WDTURN(0);
      lds_wait(L, L_DONE, 3); load_gu(6, gu_slot(6)); WAITV(16); lds_post(a_land, 5);
      {
        auto xsum = [&](float v) {
          auto r_ = __builtin_amdgcn_permlane32_swap(__float_as_uint(v), __float_as_uint(v), false, false);
          return __uint_as_float(r_[0]) + __uint_as_float(r_[1]);
        };
        const float a0 = __uint_as_float(s0[0]), a1 = __uint_as_float(s0[2]), a2 = __uint_as_float(s1[0]), a3 = __uint_as_float(s1[2]);
        const float me = xsum((a0 + a1) + (a2 + a3)) * (1.f / 512.f);
        const float d0 = a0 * (1.f / 64.f) - me, d1 = a1 * (1.f / 64.f) - me, d2 = a2 * (1.f / 64.f) - me, d3 = a3 * (1.f / 64.f) - me;
        const float m2 = (__uint_as_float(s0[1]) + __uint_as_float(s0[3])) + (__uint_as_float(s1[1]) + __uint_as_float(s1[3]));
        float mb, sd;
        ln_scal(me, xsum(m2 + 64.f * ((d0 * d0 + d1 * d1) + (d2 * d2 + d3 * d3))) * (1.f / 512.f), mb, sd);
        if (lane < 32) lds_st128(L, L_BST + (32 * w4 + lane) * 16, (u32x4){pk(-mb, -mb), pk(sd, sd), __float_as_uint(__builtin_amdgcn_rcpf(sd)), 0u});
      }
      lds_wait(L, L_DONE, 4); load_gu(7, gu_slot(7)); WAITV(7); lds_post(a_land, 6); WDTURN(2); WAITV(4); lds_post(a_land, 8);
      lds_wait(L, L_DONE, 5); load_wd(8); load_wd(9); load_wd(10); WAITV(6); WDTURN(4);
      lds_wait(L, L_DONE, 6); load_wd(11); load_wd(12); load_wd(13);
      lds_wait(L, L_DONE, 7); load_wd(14); load_wd(15); load_wd(16); load_wd(17);
      lds_wait(L, L_DONE, 8);
    }
#pragma unroll
    for (int i = 18; i < 23; ++i) load_wd(i);
    WAITV(24);
#pragma unroll
    for (int k = 6; k < 11; ++k) { wp0[k] = lds_tr(L, WD0(k), WD0(k) + 1024); wp1[k] = lds_tr(L, WD1(k), WD1(k) + 1024); }
#pragma unroll
    for (int k = 6; k < 11; ++k) { PIN(wp0[k]); PIN(wp1[k]); }
#pragma unroll
    for (int i = 23; i < 28; ++i) load_wd(i);
    wait_flags(FLAG(WS_FA, (t3 * 4 + 2 * hf + ((lane >> 2) & 1)) * 16 + 4 * w4 + (lane & 3)), tag);
    const u32 ao = WS_ACT + ((t3 * 4 + 2 * hf) * 112 + 28 * w4) * 1024 + lane * 16;
    u32x4 af0[28], af1[28];
    auto ld_act = [&](int i) {
      int ks = i + rot5; ks -= ks >= 28 ? 28 : 0;
      af0[i] = __builtin_amdgcn_raw_buffer_load_b128(rs, ao, ks * 1024, 16);
      af1[i] = __builtin_amdgcn_raw_buffer_load_b128(rs, ao + 112 * 1024, ks * 1024, 16);
    };
#pragma unroll
    for (int i = 0; i < 5; ++i) ld_act(i);
    f32x16 a00 = {}, a01 = {}, a10 = {}, a11 = {};
    static_for<0, 28>([&](auto ic) {
      constexpr int i = ic.value;
      SB();
      a00 = MFMA(wp0[i % 11], asbf(af0[i]), a00);
      a01 = MFMA(wp1[i % 11], asbf(af0[i]), a01);
      SB();
      a10 = MFMA(wp0[i % 11], asbf(af1[i]), a10);
      if constexpr (i < 6) { ld_act(5 + 2 * i); ld_act(6 + 2 * i); }
      else if constexpr (i + 11 < 28) ld_act(i + 11);
      SB();
      a11 = MFMA(wp1[i % 11], asbf(af1[i]), a11);
      if constexpr (i >= 8 && i + 3 < 28) {
        wp0[(i + 3) % 11] = lds_tr(L, WD0(i + 3), WD0(i + 3) + 1024);
        wp1[(i + 3) % 11] = lds_tr(L, WD1(i + 3), WD1(i + 3) + 1024);
      }
      PIN(a00); PIN(a01); PIN(a10); PIN(a11);
    });
    red_write(20 * KB + w4 * 2048, 8192, a00, a01);
    red_write(92 * KB + w4 * 2048, 8192, a10, a11);
  }
  BAR();
  {
    f32x4 lo, hi;
    red_read(mw ? 20 * KB : 60 * KB, 8192, 2048, lo, hi);
    u32 ln = lane_id();
    const f32x4 gb0 = lds_ldf4(L, L_MISC + M_GB + (ln & 7) * 32), gb1 = lds_ldf4(L, L_MISC + M_GB + (ln & 7) * 32 + 16);
    u32x4 o;
    o[0] = pk(bf_lo(x1p[0]) + rb(gb0[0] * rb(lo[0])), bf_hi(x1p[0]) + rb(gb0[1] * rb(lo[1])));
    o[1] = pk(bf_lo(x1p[1]) + rb(gb0[2] * rb(lo[2])), bf_hi(x1p[1]) + rb(gb0[3] * rb(lo[3])));
    o[2] = pk(bf_lo(x1p[2]) + rb(gb1[0] * rb(hi[0])), bf_hi(x1p[2]) + rb(gb1[1] * rb(hi[1])));
    o[3] = pk(bf_lo(x1p[3]) + rb(gb1[2] * rb(hi[2])), bf_hi(x1p[3]) + rb(gb1[3] * rb(hi[3])));
    *(GAS u32x4*)((GAS char*)out + (t3 * 128 + hf * 64 + 8 * w) * 1024 + cg * 128 + (ln >> 3) * 1024 + (ln & 7) * 16) = o;
  }
  asm volatile(".Ladaln_end:");
}

extern "C" void kernel_launch(void* const* d_in, const int* in_sizes, int n_in, void* d_out, int out_size, void* d_ws, size_t ws_size,
                              hipStream_t stream) {
  adaln_block<<<256, 512, 0, stream>>>((const u16*)d_in[0], (const u16*)d_in[1], (const u16*)d_in[2], (const u16*)d_in[4], (const u16*)d_in[5],
                                       (const u16*)d_in[6], (const u16*)d_in[7], (const u16*)d_in[8], (u16*)d_out, (char*)d_ws);
}
```

```cpp
#include <hip/hip_runtime.h>
#include <stdint.h>

typedef unsigned short u16;
typedef unsigned int u32;
typedef unsigned long long u64;
typedef __attribute__((ext_vector_type(8))) __bf16 bf16x8;
typedef __attribute__((ext_vector_type(2))) __bf16 bf16x2;
typedef __attribute__((ext_vector_type(4))) short s16x4;
typedef __attribute__((ext_vector_type(8))) short s16x8;
typedef __attribute__((ext_vector_type(16))) float f32x16;
typedef __attribute__((ext_vector_type(4))) float f32x4;
typedef __attribute__((ext_vector_type(2))) float f32x2;
typedef __attribute__((ext_vector_type(4))) u32 u32x4;
typedef __attribute__((ext_vector_type(2))) u32 u32x2;
#define LAS __attribute__((address_space(3)))
#define GAS __attribute__((address_space(1)))
#define DI __device__ __forceinline__
#define RLX_AGENT __ATOMIC_RELAXED, __HIP_MEMORY_SCOPE_AGENT

constexpr u32 WS_EPOCH = 0;
constexpr u32 WS_ARR = 1024;
constexpr u32 WS_MODG = 4096;
constexpr u32 WS_ST = 65536;
constexpr u32 WS_UV = 196608;
constexpr u32 WS_KV = 1048576;
constexpr u32 WS_O = WS_KV + 4194304;
constexpr u32 WS_X1 = WS_O + 2097152;
constexpr u32 WS_ACT = WS_X1 + 2097152;
constexpr u32 FS = 16, MREP = 8;
constexpr u32 WS_FKV = WS_ACT + 7340032;
constexpr u32 WS_FO = WS_FKV + 1024 * 4 * FS;
constexpr u32 WS_FX = WS_FO + 2048 * 4 * FS;
constexpr u32 WS_FA = WS_FX + 2048 * 4 * FS;
constexpr u32 WS_FUV = WS_FA + 1024 * 4 * FS;
constexpr u32 WS_MOD1 = WS_FUV + 1024 * 4 * FS;
constexpr u32 WS_BYTES = WS_MOD1 + MREP * 4096;
static_assert(WS_UV + 256 * 2048 <= WS_KV && WS_BYTES <= (32u << 20), "workspace map");
#define FLAG(base, i) ((GAS u32*)(ws + (base)) + (i) * FS)

constexpr int SPIN_MAX = 1 << 17;
constexpr u32 KB = 1024;
constexpr u32 L_WO = 0;
constexpr u32 L_KV = 64 * KB;
constexpr u32 L_QX = 144 * KB;
constexpr u32 L_AR1 = 120 * KB;
constexpr u32 L_MISC = 148 * KB;
constexpr u32 L_C1 = L_MISC, L_SH = L_MISC + 2048, L_C1B = L_MISC + 4096, M_C1B = 4096;
constexpr u32 L_UVT = L_C1, L_BST = L_SH;
constexpr u32 L_ONES = L_MISC + 8192;
constexpr u32 L_GA = L_ONES + 256, M_GA = 8192 + 256, M_GB = 8192 + 512;
constexpr u32 L_DONE = L_ONES + 784, L_LAND = L_ONES + 800, L_MODF = L_ONES + 816;
constexpr u32 L_TAG = L_ONES + 768, L_JUNK = L_ONES + 1536;
constexpr u32 L_RED = L_MISC + 10240;
constexpr u32 LDS_BYTES = 163840;
DI constexpr u32 gu_slot(int c) { return (c & 3) == 3 ? 36 * KB : 64 * KB + (c & 3) * 28 * KB; }
DI constexpr u32 wd_base(int i) {
  return i < 8 ? 20 * KB + (i & 1) * 8192u : i < 11 ? 64 * KB + (i - 8) * 8192u : i < 18 ? 92 * KB + (i - 11) * 8192u : i < 21 ? 36 * KB + (i - 18) * 8192u
       : i < 23 ? (i - 21) * 8192u : i < 25 ? 20 * KB + (i - 23) * 8192u : 64 * KB + (i - 25) * 8192u;
}
constexpr u32 CODE_LINES = 12;

constexpr float NEGBIG = -1e30f;
constexpr float QSCALE = 0.25f * 1.4426950408889634f;

template <int I> struct IC { static constexpr int value = I; };
template <int I, int N, class F> DI void static_for(F&& f) { if constexpr (I < N) { f(IC<I>{}); static_for<I + 1, N>(f); } }
DI u32 lane_id() { u32 r; asm volatile("v_mbcnt_lo_u32_b32 %0, -1, 0\n\tv_mbcnt_hi_u32_b32 %0, -1, %0" : "=v"(r)); return r; }
DI float bf_lo(u32 v) { return __uint_as_float(v << 16); }
DI float bf_hi(u32 v) { return __uint_as_float(v & 0xffff0000u); }
DI u32 pk(float a, float b) { f32x2 v = {a, b}; return __builtin_bit_cast(u32, __builtin_convertvector(v, bf16x2)); }
DI float rb(float a) { return bf_lo(pk(a, a)); }
template <int O> DI u32x4 pack8(const f32x16& a) { return (u32x4){pk(a[O], a[O + 1]), pk(a[O + 2], a[O + 3]), pk(a[O + 4], a[O + 5]), pk(a[O + 6], a[O + 7])}; }
DI bf16x8 asbf(u32x4 v) { return __builtin_bit_cast(bf16x8, v); }
#define MFMA(a, b, c) __builtin_amdgcn_mfma_f32_32x32x16_bf16((a), (b), (c), 0, 0, 0)
template <int CTRL, int RM = 0xf> DI float dpp(float v) { return __builtin_bit_cast(float, __builtin_amdgcn_update_dpp(0, __builtin_bit_cast(int, v), CTRL, RM, 0xf, true)); }
DI float sum8(float v) { v += dpp<0xB1>(v); v += dpp<0x4E>(v); v += dpp<0x141>(v); return v; }
DI float dot2(u32 a, u32 b, float c) { return __builtin_amdgcn_fdot2_f32_bf16(__builtin_bit_cast(bf16x2, a), __builtin_bit_cast(bf16x2, b), c, false); }

DI u32x4 lds_ld128(LAS char* L, u32 off) { return *(LAS u32x4*)(L + off); }
DI f32x4 lds_ldf4(LAS char* L, u32 off) { return *(LAS f32x4*)(L + off); }
DI void lds_st128(LAS char* L, u32 off, u32x4 v) { *(LAS u32x4*)(L + off) = v; }
DI bf16x8 lds_tr(LAS char* L, u32 off0, u32 off1) {
  s16x4 a = __builtin_amdgcn_ds_read_tr16_b64_v4i16((LAS s16x4*)(L + off0));
  s16x4 b = __builtin_amdgcn_ds_read_tr16_b64_v4i16((LAS s16x4*)(L + off1));
  return __builtin_bit_cast(bf16x8, __builtin_shufflevector(a, b, 0, 1, 2, 3, 4, 5, 6, 7));
}
DI u32x4 make_srd(const void* p, u32 bytes) { const u64 a = (u64)p; return (u32x4){(u32)a, (u32)(a >> 32), bytes, 0x00020000u}; }
DI void glds(const u32x4& srd, u32 voff, u32 soff, u32 ldsaddr) {
  asm volatile("s_mov_b32 m0, %3\n\ts_nop 0\n\tbuffer_load_dwordx4 %0, %1, %2 offen lds" ::"v"(voff), "s"(srd), "s"(soff), "s"(ldsaddr) : "memory");
}
DI void glds_sc1(const u32x4& srd, u32 voff, u32 soff, u32 ldsaddr) {
  asm volatile("s_mov_b32 m0, %3\n\ts_nop 0\n\tbuffer_load_dwordx4 %0, %1, %2 offen sc1 lds" ::"v"(voff), "s"(srd), "s"(soff), "s"(ldsaddr) : "memory");
}
DI void touch(const u32x4& srd, u32 voff, u32 ldsaddr) {
  asm volatile("s_mov_b32 m0, %2\n\ts_nop 0\n\tbuffer_load_dword %0, %1, 0 offen lds" ::"v"(voff), "s"(srd), "s"(ldsaddr) : "memory");
}
DI void lds_post(u32 addr, u32 val) {
  asm volatile("s_mov_b64 exec, 1\n\tds_write_b32 %0, %1\n\ts_mov_b64 exec, -1" ::"v"(addr), "v"(val) : "memory");
}
DI bool all_ge(u32x4 v, u32 k) { return __builtin_amdgcn_readfirstlane(min(min(v[0], v[1]), min(v[2], v[3]))) >= k; }
DI void lds_wait(LAS char* L, u32 off, u32 k) {
#pragma nounroll
  for (int it = 0; it < (SPIN_MAX << 4); ++it) {
    if (all_ge(*(volatile LAS u32x4*)(L + off), k)) break;
    __builtin_amdgcn_s_sleep(3);
  }
  asm volatile("" ::: "memory");
}
#define BAR() asm volatile("s_waitcnt lgkmcnt(0)\n\ts_barrier" ::: "memory")
#define WAITV_BAR(N) asm volatile("s_waitcnt vmcnt(%0) lgkmcnt(0)\n\ts_barrier" ::"i"(N) : "memory")
#define WAITV(N) asm volatile("s_waitcnt vmcnt(%0)" ::"i"(N) : "memory")
#define SB() __builtin_amdgcn_sched_barrier(0)
#define PIN(x) asm volatile("" : "+v"(x))
#define PINS(x) asm volatile("" : "+s"(x))
#define ORDER() __builtin_amdgcn_fence(__ATOMIC_ACQUIRE, "wavefront")

DI void wait_flags(const GAS u32* p, u32 tag) {
  u32 v = tag + 1u;
#pragma nounroll
  for (int it = 0; it < SPIN_MAX; ++it) {
    if (v != tag) v = __hip_atomic_load(p, RLX_AGENT);
    if (__all(v == tag)) break;
    __builtin_amdgcn_s_sleep(4);
  }
  ORDER();
}
DI void ln_scal(float mean, float var, float& mb, float& sd) {
  mb = rb(mean);
  sd = rb(__builtin_amdgcn_sqrtf(rb(rb(fmaxf(var, 0.f)) + 1e-5f)));
}
DI float fma1(float a, float b, float c) { float r = fmaf(a, b, c); asm("" : "+v"(r)); return r; }
typedef __attribute__((ext_vector_type(2))) _Float16 f16x2;
DI u32x4 znorm8(u32x4 x, float r, float mr) {
  u32x4 o;
#pragma unroll
  for (int e = 0; e < 4; ++e) {
    const f32x2 v = {fmaf(bf_lo(x[e]), r, -mr), fmaf(bf_hi(x[e]), r, -mr)};
    o[e] = __builtin_bit_cast(u32, __builtin_convertvector(v, f16x2));
  }
  return o;
}
DI u32 xform2(u32 z_, u32 c_, u32 s_) {
  const f16x2 z = __builtin_bit_cast(f16x2, z_), c = __builtin_bit_cast(f16x2, c_), s = __builtin_bit_cast(f16x2, s_);
  return pk(fma1((float)z[0], (float)c[0], (float)s[0]), fma1((float)z[1], (float)c[1], (float)s[1]));
}
DI u32x4 xform8(u32x4 x, u32x4 c, u32x4 s) { return (u32x4){xform2(x[0], c[0], s[0]), xform2(x[1], c[1], s[1]), xform2(x[2], c[2], s[2]), xform2(x[3], c[3], s[3])}; }
DI u32 pkh(float a, float b) { const f32x2 v = {a, b}; return __builtin_bit_cast(u32, __builtin_convertvector(v, f16x2)); }
DI f32x2 geglu2(f32x2 ag, f32x2 au, float k1, float k3, float i2) {
  const f32x2 a = ag * (ag * ag * k3 + k1);
  const f32x2 d = (f32x2){__builtin_amdgcn_exp2f(a[0]), __builtin_amdgcn_exp2f(a[1])} * i2 + i2;
  return ag * au * (f32x2){__builtin_amdgcn_rcpf(d[0]), __builtin_amdgcn_rcpf(d[1])};
}

__global__ __launch_bounds__(512, 2) void adaln_block(const u16* __restrict__ x, const u16* __restrict__ wqkv, const u16* __restrict__ wo,
                                                      const u16* __restrict__ wg, const u16* __restrict__ wu, const u16* __restrict__ wd,
                                                      const u16* __restrict__ cvec, const u16* __restrict__ wm, u16* __restrict__ out,
                                                      char* __restrict__ ws) {
  __shared__ __attribute__((aligned(1024))) char lds[LDS_BYTES];
  LAS char* L = (LAS char*)lds;
  const u32 lbase = (u32)(uintptr_t)L;
  const int tid = threadIdx.x, lane = tid & 63;
  const int w = __builtin_amdgcn_readfirstlane(tid >> 6), w4 = w & 3;
  const bool mw = w < 4;
  const int b = blockIdx.x, xcd = b & 7, ii = b >> 3;
  if (w == 7) {
    u64 pc;
    int len;
    asm volatile("s_getpc_b64 %0\n.Ladaln_pf:\n\ts_mov_b32 %1, .Ladaln_end-.Ladaln_pf" : "=s"(pc), "=s"(len));
    touch(make_srd((const void*)pc, len > 0 ? (u32)len : 0u), ((lane < CODE_LINES ? lane : 0) * 32 + ii) * 128u, lbase + L_JUNK);
  }
  const int u1 = 31 - (8 * (xcd >> 1) + (ii >> 2)), q1 = 4 * (xcd & 1) + (ii & 3);
  const int t3 = 2 * xcd + (ii >> 4), hf = (ii >> 3) & 1, cg = ii & 7;
  const int t4 = 8 * (xcd >> 2) + (ii >> 2), j4 = 4 * (xcd & 3) + (ii & 3);
  const int rot1 = (u1 + 2 * q1) & 7;
  const int rot4 = ii >> 2, rot5 = 7 * (ii >> 3);
  const int r32 = lane & 31, hh = lane >> 5, blk = (lane >> 4) & 1, q4 = (lane & 15) >> 2, p4 = lane & 3, l3 = lane >> 3, l7 = lane & 7;
  const __amdgpu_buffer_rsrc_t rs = __builtin_amdgcn_make_buffer_rsrc(ws, 0, WS_BYTES, 0x00020000);
  u32 tag = 0;
  const int orow = t3 * 128 + hf * 64 + 8 * w + l3;
  const int mg = b;
  const u32x4 srd_q = make_srd(wqkv, 512 * 3072);
  const u32 q8o = l3 * 3072u + ((l7 ^ (((lane >> 4) & 1) << 2)) << 4);
  auto load_wq = [&](int c) {
    const u32 so = ((c + rot1) & 7) * 196608 + w * 1024 + q1 * 128;
#pragma unroll
    for (int e = 0; e < 8; ++e) glds(srd_q, q8o, so + e * 24576, lbase + (c % 5) * 24576 + w * 8192 + e * 1024);
  };
  u32x4 X[16];
  u32x4 xr;
  const int hp = w4 >> 1, hd = w >> 2, qb = (w ^ hd) & 1;
  bf16x8 qf_[15], kf_[15], vf_[15];
  u32 wl[3];
#define P1O(n) (wl[(((n) >> 2) % 5) >> 1] + ((((n) >> 2) % 5) & 1) * 24576 + ((n) & 3) * 2048)
#define P1I(n) ((n) < 12 ? (n) : 12 + (n) % 3)
  u32 cso = L_MISC + l7 * 32;
  PIN(cso);
  const u32x4 srd_g = make_srd(wg, 512 * 3584), srd_u = make_srd(wu, 512 * 3584), srd_d = make_srd(wd, 1792 * 1024), srd_o = make_srd(wo, 512 * 1024);

  u32 guo[7];
  const u32x4 srd_3 = w4 < 2 ? srd_g : srd_u;
  auto load_gu = [&](int c, u32 slot) {
    int r_ = rot4; PINS(r_);
    const u32 so = j4 * 224 + ((c + r_) & 7) * (64 * 3584), ld = lbase + slot + w4 * 1024;
#pragma unroll
    for (int n = 0; n < 7; ++n) glds(n < 3 ? srd_g : n == 3 ? srd_3 : srd_u, guo[n], so, ld + n * 4096);
  };
  const u32 w8o = l3 * 1024u + ((l7 ^ (((lane >> 4) & 1) << 2)) << 4);
  bf16x8 qf;
  if (mw) {
    u32x4 M[16];
    u32 cv = 0;
    if (mg < 48) {
      const GAS char* mb = (const GAS char*)wm + mg * 128 + (128 * w + 16 * l3) * 6144 + l7 * 16;
      cv = ((const GAS u32*)cvec)[64 * w + lane];
#pragma unroll
      for (int n = 0; n < 16; ++n) M[n] = *(const GAS u32x4*)(mb + n * 6144);
    }
    const u32 ep = __hip_atomic_load((const GAS u32*)(ws + WS_EPOCH), RLX_AGENT);
    if (lane < 12) *(LAS u32*)(L + L_DONE + lane * 4) = 0u;
    SB();
    BAR();
    if (mg < 48) {
      {
        const float c0 = bf_lo(cv), c1 = bf_hi(cv);
        *(LAS u32*)(L + L_RED + 1024 + w * 256 + lane * 4) = pk(c0 * __builtin_amdgcn_rcpf(1.0f + __builtin_amdgcn_exp2f(-1.4426950408889634f * c0)),
                                                               c1 * __builtin_amdgcn_rcpf(1.0f + __builtin_amdgcn_exp2f(-1.4426950408889634f * c1)));
      }
      const u32x4 s0 = lds_ld128(L, L_RED + 1024 + w * 256 + l3 * 32), s1 = lds_ld128(L, L_RED + 1024 + w * 256 + l3 * 32 + 16);
      f32x2 a[4] = {};
#pragma unroll
      for (int n = 0; n < 8; ++n) {
        const u32 sp = n < 4 ? s0[n & 3] : s1[n & 3];
#pragma unroll
        for (int e = 0; e < 4; ++e) {
          const u32 r0 = M[2 * n][e], r1 = M[2 * n + 1][e];
          a[e][0] = dot2(__builtin_amdgcn_perm(r1, r0, 0x05040100u), sp, a[e][0]);
          a[e][1] = dot2(__builtin_amdgcn_perm(r1, r0, 0x07060302u), sp, a[e][1]);
        }
      }
      float v[8] = {a[0][0], a[0][1], a[1][0], a[1][1], a[2][0], a[2][1], a[3][0], a[3][1]};
#pragma unroll
      for (int e = 0; e < 8; ++e) {
        v[e] += dpp<0x128>(v[e]);
        auto r1_ = __builtin_amdgcn_permlane16_swap(__float_as_uint(v[e]), __float_as_uint(v[e]), false, false);
        v[e] = __uint_as_float(r1_[0]) + __uint_as_float(r1_[1]);
        auto r2_ = __builtin_amdgcn_permlane32_swap(__float_as_uint(v[e]), __float_as_uint(v[e]), false, false);
        v[e] = __uint_as_float(r2_[0]) + __uint_as_float(r2_[1]);
      }
      if (lane < 8) {
        *(LAS f32x4*)(L + L_RED + w * 256 + lane * 32) = (f32x4){v[0], v[1], v[2], v[3]};
        *(LAS f32x4*)(L + L_RED + w * 256 + lane * 32 + 16) = (f32x4){v[4], v[5], v[6], v[7]};
      }
      lds_post(lbase + L_MODF + w * 4, 1);
      if (w == 3) {
        lds_wait(L, L_MODF, 1);
        float s = 0.f;
#pragma unroll
        for (int k = 0; k < 4; ++k) s += *(LAS float*)(L + L_RED + k * 256 + lane * 4);
        const float o = dpp<0xB1>(s);
        const u32 tg = __builtin_amdgcn_readfirstlane(ep) + 1u;
        const u64 gr = ((u64)tg << 32) | ((lane & 1) ? pk(o, s) : pk(s, o));
        if (mg < 16) {
#pragma unroll
          for (int r = 0; r < MREP / 2 + (MREP & 1); ++r)
            if (2 * r + (lane & 1) < MREP) __hip_atomic_store((GAS u64*)(ws + WS_MOD1) + (2 * r + (lane & 1)) * 512 + 32 * mg + (lane >> 1), gr, RLX_AGENT);
        } else if (!(lane & 1)) __hip_atomic_store((GAS u64*)(ws + WS_MODG) + 32 * mg + (lane >> 1), gr, RLX_AGENT);
      }
    }
    xr = *(const GAS u32x4*)((const GAS char*)x + orow * 1024 + cg * 128 + l7 * 16);
    tag = __builtin_amdgcn_readfirstlane(ep) + 1u;
    if (w < 3) {
      load_wq(0); load_wq(1); load_wq(2); load_wq(3); load_wq(4);
      WAITV(16);
    }
    lds_post(lbase + L_MODF + w * 4, 2);
    wl[0] = (8 * hh + q4) * 128 + ((64 * hp + 32 * blk + 8 * p4) ^ ((q4 >> 1) << 6));
    wl[1] = wl[0] + 48 * KB; wl[2] = wl[0] + 96 * KB;
    PIN(wl[1]); PIN(wl[2]);
    lds_wait(L, L_MODF, 2);
#pragma unroll
    for (int n = 0; n < 12; ++n) { kf_[n] = lds_tr(L, P1O(n) + 8192, P1O(n) + 8704); vf_[n] = lds_tr(L, P1O(n) + 16384, P1O(n) + 16896); qf_[n] = lds_tr(L, P1O(n), P1O(n) + 512); }
    lds_post(lbase + L_MODF + w * 4, 3);
#pragma unroll
    for (int n = 0; n < 12; ++n) { PIN(kf_[n]); PIN(vf_[n]); PIN(qf_[n]); }
    if (w < 3) {
      lds_wait(L, L_MODF, 3);
      load_wq(5); load_wq(6); load_wq(7);
    } else {
      if (lane < 32) lds_st128(L, L_ONES + (lane & 15) * 16 + (lane >> 4) * 1024, (u32x4){0x3f803f80u, 0x3f803f80u, 0x3f803f80u, 0x3f803f80u});
      if (lane == 0) *(LAS u32*)(L + L_TAG) = tag;
      u32x4 v[4];
#pragma nounroll
      for (int it = 0; it < SPIN_MAX; ++it) {
        bool ok = true;
#pragma unroll
        for (int k = 0; k < 4; ++k) { v[k] = __builtin_amdgcn_raw_buffer_load_b128(rs, lane * 16, WS_MOD1 + (xcd % MREP) * 4096 + k * 1024, 16); ok &= v[k][1] == tag && v[k][3] == tag; }
        if (__all(ok)) break;
        __builtin_amdgcn_s_sleep(2);
      }
#pragma unroll
      for (int k = 0; k < 2; ++k) {
        const u32 o = L_C1 + ((lane + 64 * k) >> 1) * 32 + (lane & 1) * 8;
        *(LAS u32x2*)(L + o + 16) = (u32x2){pkh(bf_lo(v[k][0]), bf_hi(v[k][0])), pkh(bf_lo(v[k][2]), bf_hi(v[k][2]))};
        *(LAS u32x2*)(L + o) = (u32x2){pkh(rb(1.f + bf_lo(v[2 + k][0])), rb(1.f + bf_hi(v[2 + k][0]))), pkh(rb(1.f + bf_lo(v[2 + k][2])), rb(1.f + bf_hi(v[2 + k][2])))};
      }
    }
    BAR();
    f32x16 aq = {}, ak = {}, av = {};
    u32 ard[4];
    {
      const int row = 32 * qb + r32, f = (row & 7) ^ ((row >> 3) & 1);
#pragma unroll
      for (int s = 0; s < 4; ++s) { ard[s] = L_AR1 + row * 128 + ((((2 * s) | hh) ^ f) << 4); PIN(ard[s]); }
    }
    bf16x8 hf_[3];
#define P1H(n) hf_[(n) % 3] = asbf(lds_ld128(L, ard[(n) & 3] + (((n) >> 2) % 3) * 8192))
    BAR();
    P1H(0); P1H(1);
    static_for<0, 32>([&](auto nc) {
      constexpr int n = nc.value, m = n + 2;
      constexpr bool bnd = (m & 3) == 0, rd = m >= 12 && m < 32;
      SB();
      ak = MFMA(kf_[P1I(n)], hf_[n % 3], ak);
      if constexpr (m < 32 && !bnd) { P1H(m); if constexpr (rd) kf_[P1I(m)] = lds_tr(L, P1O(m) + 8192, P1O(m) + 8704); }
      SB();
      av = MFMA(hf_[n % 3], vf_[P1I(n)], av);
      if constexpr (m < 32 && bnd) {
        PIN(ak); PIN(av);
        WAITV_BAR(m >= 12 ? 56 - 2 * m : 63);
        P1H(m);
        if constexpr (rd) kf_[P1I(m)] = lds_tr(L, P1O(m) + 8192, P1O(m) + 8704);
      }
      if constexpr (rd) vf_[P1I(m)] = lds_tr(L, P1O(m) + 16384, P1O(m) + 16896);
      SB();
      aq = MFMA(qf_[P1I(n)], hf_[n % 3], aq);
      if constexpr (rd) qf_[P1I(m)] = lds_tr(L, P1O(m), P1O(m) + 512);
      PIN(ak); PIN(av); PIN(aq);
    });
    const u32x4 f0 = pack8<0>(ak), f1 = pack8<8>(ak), f2 = pack8<0>(av), f3 = pack8<8>(av);
    const int kvi = ((u1 >> 1) * 16 + 2 * q1 + hp) * 4 + 2 * (u1 & 1) + qb;
    const u32 o = WS_KV + kvi * 4096 + lane * 16;
    __builtin_amdgcn_raw_buffer_store_b128(f0, rs, o, 0, 16);
    __builtin_amdgcn_raw_buffer_store_b128(f1, rs, o + 1024, 0, 16);
    __builtin_amdgcn_raw_buffer_store_b128(f2, rs, o + 2048, 0, 16);
    __builtin_amdgcn_raw_buffer_store_b128(f3, rs, o + 3072, 0, 16);
    aq *= QSCALE;
    qf = asbf(pack8<0>(aq));
    const u32x4 f5 = pack8<8>(aq);
    BAR();
    const u32 ok = L_KV + hp * 40960 + (8 + qb) * 4096 + lane * 16;
    lds_st128(L, ok, f0); lds_st128(L, ok + 1024, f1); lds_st128(L, ok + 2048, f2); lds_st128(L, ok + 3072, f3);
    lds_st128(L, L_QX + w * 1024 + lane * 16, f5);
    BAR();
    WAITV(0);
    if (lane == 0) __hip_atomic_store(FLAG(WS_FKV, kvi), tag, RLX_AGENT);
  } else {
    const __amdgpu_buffer_rsrc_t rx = __builtin_amdgcn_make_buffer_rsrc((void*)x, 0, 2048 * 1024, 0x00020000);
    if (mg < 48) BAR();
#pragma unroll
    for (int c = 0; c < 8; ++c)
#pragma unroll
      for (int g = 0; g < 2; ++g)
        X[2 * c + g] = __builtin_amdgcn_raw_buffer_load_b128(rx, l3 * 1024u + l7 * 16u, (u1 * 64 + 16 * w4 + 8 * g) * 1024 + ((c + rot1) & 7) * 128, 0);
    xr = *(const GAS u32x4*)((const GAS char*)x + orow * 1024 + cg * 128 + l7 * 16);
    SB();
    if (mg >= 48) BAR();
    float sm[2] = {}, sq[2] = {};
#pragma unroll
    for (int c = 0; c < 8; ++c) {
      SB();
#pragma unroll
      for (int e = 0; e < 4; ++e)
#pragma unroll
        for (int g = 0; g < 2; ++g) { sm[g] = dot2(X[2 * c + g][e], 0x3f803f80u, sm[g]); sq[g] = dot2(X[2 * c + g][e], X[2 * c + g][e], sq[g]); }
    }
    float rr[2], mr[2];
#pragma unroll
    for (int g = 0; g < 2; ++g) {
      const float me = sum8(sm[g]) * (1.f / 512.f);
      float mb, sd;
      ln_scal(me, sum8(sq[g]) * (1.f / 512.f) - me * me, mb, sd);
      rr[g] = __builtin_amdgcn_rcpf(sd); mr[g] = mb * rr[g];
    }
#pragma unroll
    for (int n = 0; n < 16; ++n) { X[n] = znorm8(X[n], rr[n & 1], mr[n & 1]); PIN(X[n]); }
    if (w4 < 3) {
      const u32x4 srd_a = make_srd(w4 == 0 ? wo : w4 == 1 ? wg : wu, w4 == 0 ? 512 * 1024 : 512 * 3584);
      const int e1 = 64 + lane;
      const u32 ta0 = w4 ? (16 * ii + lane / 7) * 3584 + (xcd & 3) * 896 + (lane % 7) * 128 : ii * 16384 + lane * 128;
      const u32 ta1 = w4 ? (lane < 48 ? (16 * ii + e1 / 7) * 3584 + (xcd & 3) * 896 + (e1 % 7) * 128 : 0x7fffffffu) : ta0 + 8192;
      touch(srd_a, ta0, lbase + L_JUNK);
      touch(srd_a, ta1, lbase + L_JUNK);
    }
    BAR();
    tag = __builtin_amdgcn_readfirstlane(*(LAS u32*)(L + L_TAG));
    if (w == 4 && lane == 0) __hip_atomic_store((GAS u32*)(ws + WS_ARR) + b, tag, RLX_AGENT);
    const u32 awE = L_AR1 + (16 * w4 + l3) * 128 + ((l7 ^ l3) << 4), awO = (awE ^ 16u) + 1024;
    u32x4 ca[2], sa[2];
#define CSLOAD(c) do { const u32 o_ = cso + (((c) + rot1) & 7) * 256; ca[(c) & 1] = lds_ld128(L, o_); sa[(c) & 1] = lds_ld128(L, o_ + 16); } while (0)
    CSLOAD(0);
#pragma unroll
    for (int c = 0; c < 8; ++c) {
      if (c < 7) CSLOAD(c + 1);
      SB();
      PIN(X[2 * c]);
      lds_st128(L, awE + (c % 3) * 8192, xform8(X[2 * c], ca[c & 1], sa[c & 1]));
      PIN(X[2 * c + 1]);
      lds_st128(L, awO + (c % 3) * 8192, xform8(X[2 * c + 1], ca[c & 1], sa[c & 1]));
      if (c == 0) BAR();
      if (c >= 2) asm volatile("s_waitcnt lgkmcnt(2)\n\ts_barrier" ::: "memory");
    }
    BAR();
#pragma unroll
    for (int n = 0; n < 7; ++n) {
      const int pc = w4 + 4 * n, up = pc >= 14, e = (pc - 14 * up) * 64 + lane, rho = e / 14, cc = e - 14 * rho;
      guo[n] = (u32)((up ? (rho ^ 4) : rho) * 3584 + cc * 16);
    }
    BAR();
    BAR();
    qf = asbf(lds_ld128(L, L_QX + (w4 ^ 1) * 1024 + lane * 16));
  }
  PIN(xr);
  if (!mw && qb) {
    for (int n = hp * 32; n < hp * 32 + 32; ++n) glds(srd_o, w8o, cg * 128 + n * 8192, lbase + L_WO + n * 1024);
  }

  {
    const u32 ka = L_KV + hp * 40960 + hd * 1024 + lane * 16;
    const bool vdat = (r32 >> 4) == hd;
    const u32 va = vdat ? L_KV + hp * 40960 + 2048 + lane * 16 : L_ONES + (lane & 15) * 16;
    const u32 vs = vdat ? 4096u : 0u;
    f32x16 cm;
#pragma unroll
    for (int g = 0; g < 16; ++g) cm[g] = (g & 3) + 8 * (g >> 2) + 4 * hh <= r32 ? 0.f : NEGBIG;
    const f32x16 zero = {};
    f32x16 O = {};
    float m = NEGBIG;
    auto rowmax = [&](const f32x16& s, float mm) {
#pragma unroll
      for (int g = 0; g < 16; g += 2) mm = fmaxf(fmaxf(mm, s[g]), s[g + 1]);
      return mm;
    };
    auto ldk = [&](int lb) { return asbf(lds_ld128(L, ka + lb * 4096)); };
#pragma nounroll
    for (int st = 0; st < 2; ++st) {
      int lm = 8 + qb, lo = 8, hi = 8 + qb;
      if (st) {
        if (u1 == 0) break;
        if (!qb) {
          const int g0 = 2 * u1 - 8 + 4 * hd, gl = g0 + (lane & 3);
          wait_flags(FLAG(WS_FKV, gl < 0 ? ((u1 >> 1) * 16 + 2 * q1 + hp) * 4 + 2 * (u1 & 1) : ((gl >> 2) * 16 + 2 * q1 + hp) * 4 + (gl & 3)), tag);
          const u32x4 srd_w = make_srd(ws, WS_BYTES);
#pragma unroll
          for (int k = 0; k < 4; ++k) {
            const int g = g0 + k;
            const u32 so = g < 0 ? 0x7ff00000u : WS_KV + (((g >> 2) * 16 + 2 * q1 + hp) * 4 + (g & 3)) * 4096;
#pragma unroll
            for (int n = 0; n < 4; ++n) glds_sc1(srd_w, lane * 16, so + n * 1024, lbase + L_KV + hp * 40960 + hd * 16384 + (4 * k + n) * 1024);
          }
          WAITV(0);
        }
        const int lmin = 8 - 2 * u1;
#pragma unroll
        for (int g = 0; g < 16; ++g) { float u = lmin > qb ? NEGBIG : NEGBIG - cm[g]; asm("" : "+v"(u)); cm[g] = u; }
        lm = qb; lo = lmin > qb ? lmin : qb + 1; hi = 8;
        BAR();
      }
      bf16x8 kA = ldk(lo), kB = ldk(lo + 1);
      f32x16 sA = MFMA(ldk(lm), qf, cm), sB, sC;
      const bf16x8 k0 = kA;
      float mn = m;
      for (int lb = lo; lb < hi; lb += 2) {
        sB = MFMA(kA, qf, zero);
        sC = MFMA(kB, qf, zero);
        kA = ldk(lb + 2); kB = ldk(lb + 3);
        SB();
        mn = rowmax(sB, mn);
        if (lb + 1 < hi) mn = rowmax(sC, mn);
      }
      mn = rowmax(sA, mn);
      {
        auto r_ = __builtin_amdgcn_permlane32_swap(__float_as_uint(mn), __float_as_uint(mn), false, false);
        mn = fmaxf(__uint_as_float(r_[0]), __uint_as_float(r_[1]));
      }
      u32 vv = va + lm * vs;
      bf16x8 v0 = asbf(lds_ld128(L, vv)), v1 = asbf(lds_ld128(L, vv + 1024));
      f32x16 negm;
#pragma unroll
      for (int g = 0; g < 16; ++g) negm[g] = -mn;
#pragma unroll
      for (int g = 0; g < 16; ++g) { float u = sA[g] - mn; asm("" : "+v"(u)); sA[g] = u; }
      if (st) {
        const float al = __builtin_amdgcn_exp2f(m - mn);
#pragma unroll
        for (int g = 0; g < 16; ++g) { float o = O[g] * al; asm("" : "+v"(o)); O[g] = o; }
      }
      m = mn;
      kA = k0;
      vv = va + lo * vs;
#define P2STEP(CUR, NXT)                                                                      \
  {                                                                                           \
    NXT = MFMA(kA, qf, negm);                                                                 \
    PIN(NXT);                                                                                 \
    kA = ldk(lb + 1);                                                                         \
    const bf16x8 n0_ = asbf(lds_ld128(L, vv)), n1_ = asbf(lds_ld128(L, vv + 1024));           \
    vv += vs;                                                                                 \
    SB();                                                                                     \
    _Pragma("unroll") for (int g = 0; g < 8; ++g) CUR[g] = __builtin_amdgcn_exp2f(CUR[g]);    \
    O = MFMA(v0, asbf(pack8<0>(CUR)), O);                                                     \
    _Pragma("unroll") for (int g = 8; g < 16; ++g) CUR[g] = __builtin_amdgcn_exp2f(CUR[g]);   \
    O = MFMA(v1, asbf(pack8<8>(CUR)), O);                                                     \
    SB();                                                                                     \
    v0 = n0_; v1 = n1_;                                                                       \
  }
      for (int lb = lo;; ++lb) {
        P2STEP(sA, sB);
        if (lb >= hi) break;
        ++lb;
        P2STEP(sB, sA);
        if (lb >= hi) break;
      }
    }
    u32x4 of;
    {
      const float inv = __builtin_amdgcn_rcpf(hd ? O[0] : O[8]);
      float o[8];
#pragma unroll
      for (int g = 0; g < 8; ++g) { o[g] = (hd ? O[8 + g] : O[g]) * inv; asm("" : "+v"(o[g])); }
      of = (u32x4){pk(o[0], o[1]), pk(o[2], o[3]), pk(o[4], o[5]), pk(o[6], o[7])};
    }
    const int oi = (2 * u1 + qb) * 32 + 4 * q1 + 2 * hp + hd;
    __builtin_amdgcn_raw_buffer_store_b128(of, rs, WS_O + oi * 1024 + lane * 16, 0, 16);
    WAITV(0);
    if (lane == 0) __hip_atomic_store(FLAG(WS_FO, oi), tag, RLX_AGENT);
  }
  {
    if (u1 == 0 && q1 == 0 && w == 7) {
#pragma nounroll
      for (int it = 0; it < SPIN_MAX; ++it) {
        bool ok = true;
#pragma unroll
        for (int k = 0; k < 4; ++k) ok &= __hip_atomic_load((const GAS u32*)(ws + WS_ARR) + lane + 64 * k, RLX_AGENT) == tag;
        if (__all(ok)) break;
        __builtin_amdgcn_s_sleep(1);
      }
      if (lane == 0) __hip_atomic_store((GAS u32*)(ws + WS_EPOCH), tag, RLX_AGENT);
    }
  }
  BAR();

  const u32 wt0 = (4 * hh + q4) * 128 + ((32 * blk + 8 * p4) ^ ((q4 >> 1) << 6)), wt1 = wt0 ^ 64u;
  auto red_write = [&](u32 base, u32 S8, const f32x16& a0, const f32x16& a1) {
    u32 ln = lane_id();
    const u32 o = base + ((ln & 31) >> 3) * S8 + (ln & 7) * 256, sw = (ln & 7) ^ (ln >> 5);
#pragma unroll
    for (int g = 0; g < 4; ++g) {
      *(LAS f32x4*)(L + o + (((2 * g) ^ sw) << 4)) = (f32x4){a0[4 * g], a0[4 * g + 1], a0[4 * g + 2], a0[4 * g + 3]};
      *(LAS f32x4*)(L + o + (((2 * g) ^ sw) << 4) + 128) = (f32x4){a1[4 * g], a1[4 * g + 1], a1[4 * g + 2], a1[4 * g + 3]};
    }
  };
  auto red_read = [&](u32 base, u32 S8, u32 SK, f32x4& lo, f32x4& hi) {
    u32 ln = lane_id();
    const u32 o0 = base + w * S8 + (ln >> 3) * 256 + (((2 * (ln & 7)) ^ (ln >> 3)) << 4), o1 = o0 ^ 16u;
    lo = lds_ldf4(L, o0); hi = lds_ldf4(L, o1);
#pragma unroll
    for (int k = 1; k < 4; ++k) { lo += lds_ldf4(L, o0 + k * SK); hi += lds_ldf4(L, o1 + k * SK); }
  };
  u32x4 x1p;
  {
    if (mw) {
      bf16x8 wa[8], wb[8];
#pragma unroll
      for (int i = 0; i < 8; ++i) {
        const u32 o = L_WO + w * 16384 + i * 2048;
        wa[i] = lds_tr(L, o + wt0, o + wt0 + 1024); wb[i] = lds_tr(L, o + wt1, o + wt1 + 1024);
      }
      wait_flags(FLAG(WS_FO, (t3 * 4 + 2 * hf + (l3 & 1)) * 32 + 8 * w + l7), tag);
      u32x4 of0[8], of1[8];
      const u32 oo = WS_O + ((t3 * 4 + 2 * hf) * 32 + 8 * w) * 1024 + lane * 16;
#pragma unroll
      for (int i = 0; i < 8; ++i) {
        of0[i] = __builtin_amdgcn_raw_buffer_load_b128(rs, oo + i * 1024, 0, 16);
        of1[i] = __builtin_amdgcn_raw_buffer_load_b128(rs, oo + 32768 + i * 1024, 0, 16);
      }
      SB();
      f32x16 a00 = {}, a01 = {}, a10 = {}, a11 = {};
#pragma unroll
      for (int i = 0; i < 8; ++i) {
        a00 = MFMA(wa[i], asbf(of0[i]), a00); a01 = MFMA(wb[i], asbf(of0[i]), a01);
        a10 = MFMA(wa[i], asbf(of1[i]), a10); a11 = MFMA(wb[i], asbf(of1[i]), a11);
      }
      red_write(L_WO + w * 16384, 2048, a00, a01);
      red_write(L_WO + w * 16384 + 8192, 2048, a10, a11);
    } else {
      load_gu(0, gu_slot(0));
      u32x4 bv[2];
      {
        u32x4 q[4];
        u32 v[3];
        const GAS u64* g = (const GAS u64*)(ws + WS_MODG);
        const u32 qo = (((r32 & 1) ? 768 : 1024) + rot4 * 32 + (t4 >> 3) * 16 + 4 * hh) * 8;
#pragma nounroll
        for (int it = 0; it < SPIN_MAX; ++it) {
          const u64 x1_ = __hip_atomic_load(g + 1024 + (tid - 256), RLX_AGENT);
          const u64 x2 = __hip_atomic_load(g + (lane < 32 ? 512 : 1248) + cg * 32 + lane, RLX_AGENT);
          bool ok = (u32)(x1_ >> 32) == tag && (u32)(x2 >> 32) == tag;
#pragma unroll
          for (int k = 0; k < 4; ++k) { q[k] = __builtin_amdgcn_raw_buffer_load_b128(rs, qo + (k >> 1) * 64 + (k & 1) * 16, WS_MODG, 16); ok &= q[k][1] == tag && q[k][3] == tag; }
          v[1] = (u32)x1_; v[2] = (u32)x2;
          if (__all(ok)) break;
          __builtin_amdgcn_s_sleep(1);
        }
        *(LAS f32x2*)(L + L_C1B + (tid - 256) * 8) = (f32x2){rb(1.f + bf_lo(v[1])), rb(1.f + bf_hi(v[1]))};
        if (w == 4) *(LAS f32x2*)(L + L_GA + lane * 8) = (f32x2){bf_lo(v[2]), bf_hi(v[2])};
        const float one = (r32 & 1) ? 0.f : 1.f;
#pragma unroll
        for (int s = 0; s < 2; ++s)
          bv[s] = (u32x4){pk(one + bf_lo(q[2 * s][0]), one + bf_hi(q[2 * s][0])), pk(one + bf_lo(q[2 * s][2]), one + bf_hi(q[2 * s][2])),
                          pk(one + bf_lo(q[2 * s + 1][0]), one + bf_hi(q[2 * s + 1][0])), pk(one + bf_lo(q[2 * s + 1][2]), one + bf_hi(q[2 * s + 1][2]))};
      }
      lds_post(lbase + L_LAND + w4 * 4, 1);
      load_gu(1, gu_slot(1));
      lds_wait(L, L_LAND, 1);
      {
        const u32 gl = blk * 14336 + (8 * hh + q4) * 224 + 8 * p4 + w4 * 32 + (t4 >> 3) * 7168 + gu_slot(0);
        const u32 g0 = gl + (blk ? 896u : 0u), g1 = gl + (blk ? 0u : 896u);
        f32x16 a0 = {}, a1 = {};
#pragma unroll
        for (int s = 0; s < 2; ++s) {
          a0 = MFMA(lds_tr(L, g0 + s * 3584, g1 + s * 3584), asbf(bv[s]), a0);
          a1 = MFMA(lds_tr(L, g0 + s * 3584 + 128, g1 + s * 3584 + 128), asbf(bv[s]), a1);
        }
        if (r32 < 2) {
          const u32 o = WS_UV + (j4 * 16 + t4) * 2048 + w4 * 256 + r32 * 128 + hh * 16;
#pragma unroll
          for (int g = 0; g < 4; ++g) {
            __builtin_amdgcn_raw_buffer_store_b128(__builtin_bit_cast(u32x4, (f32x4){a0[4 * g], a0[4 * g + 1], a0[4 * g + 2], a0[4 * g + 3]}), rs, o + g * 32, 0, 16);
            if (w4 < 3) __builtin_amdgcn_raw_buffer_store_b128(__builtin_bit_cast(u32x4, (f32x4){a1[4 * g], a1[4 * g + 1], a1[4 * g + 2], a1[4 * g + 3]}), rs, o + 1024 + g * 32, 0, 16);
          }
        }
      }
      load_gu(2, gu_slot(2));
    }
    BAR();
    f32x4 lo, hi;
    red_read(L_WO, 2048, 16384, lo, hi);
    const f32x4 ga0 = lds_ldf4(L, cso + M_GA), ga1 = lds_ldf4(L, cso + M_GA + 16);
    float v[8];
    v[0] = rb(bf_lo(xr[0]) + rb(ga0[0] * rb(lo[0]))); v[1] = rb(bf_hi(xr[0]) + rb(ga0[1] * rb(lo[1])));
    v[2] = rb(bf_lo(xr[1]) + rb(ga0[2] * rb(lo[2]))); v[3] = rb(bf_hi(xr[1]) + rb(ga0[3] * rb(lo[3])));
    v[4] = rb(bf_lo(xr[2]) + rb(ga1[0] * rb(hi[0]))); v[5] = rb(bf_hi(xr[2]) + rb(ga1[1] * rb(hi[1])));
    v[6] = rb(bf_lo(xr[3]) + rb(ga1[2] * rb(hi[2]))); v[7] = rb(bf_hi(xr[3]) + rb(ga1[3] * rb(hi[3])));
    x1p = (u32x4){pk(v[0], v[1]), pk(v[2], v[3]), pk(v[4], v[5]), pk(v[6], v[7])};
    PIN(x1p);
    {
      const f32x4 c0 = lds_ldf4(L, cso + M_C1B + cg * 256), c1 = lds_ldf4(L, cso + M_C1B + cg * 256 + 16);
      const u32x4 y = {pk(v[0] * c0[0], v[1] * c0[1]), pk(v[2] * c0[2], v[3] * c0[3]), pk(v[4] * c1[0], v[5] * c1[1]), pk(v[6] * c1[2], v[7] * c1[3])};
      __builtin_amdgcn_raw_buffer_store_b128(y, rs, WS_X1 + ((t3 * 4 + 2 * hf + (w >> 2)) * 32 + 4 * cg + (l7 >> 1)) * 1024 + ((l7 & 1) * 32 + 8 * w4 + l3) * 16, 0, 16);
    }
    const float sm = sum8(((v[0] + v[1]) + (v[2] + v[3])) + ((v[4] + v[5]) + (v[6] + v[7])));
    const float mu = sm * (1.f / 64.f);
    float m2 = 0.f;
#pragma unroll
    for (int k = 0; k < 8; ++k) m2 = fmaf(v[k] - mu, v[k] - mu, m2);
    m2 = sum8(m2);
    if (l7 == 0) __hip_atomic_store((GAS u64*)(ws + WS_ST) + orow * 8 + cg, ((u64)__float_as_uint(m2) << 32) | __float_as_uint(sm), RLX_AGENT);
    WAITV(0);
    if (lane == 0) __hip_atomic_store(FLAG(WS_FX, (t3 * 4 + 2 * hf + (w >> 2)) * 32 + cg * 4 + w4), tag, RLX_AGENT);
    if (!mw && lane == 1) __hip_atomic_store(FLAG(WS_FUV, j4 * 64 + t4 * 4 + w4), tag, RLX_AGENT);
  }
  BAR();

  if (mw) {
    u32 ln = lane_id();
    const u32 bk = (ln >> 4) & 1, gl = bk * 14336 + (8 * (ln >> 5) + ((ln & 15) >> 2)) * 224 + 8 * (ln & 3);
    u32 g0 = gl + (bk ? 896u : 0u) + 36 * KB, g1 = gl + (bk ? 0u : 896u) + 36 * KB, g0h = g0 + 56 * KB, g1h = g1 + 56 * KB;
    PIN(g0); PIN(g1); PIN(g0h); PIN(g1h);
    f32x16 ag[7] = {};
    bf16x8 wf_[7];
    u32x4 yf[32];
#define P4S(n) (((n) >> 2) & 3)
#define P4O(n) ((P4S(n) & 1 ? 0 : 28 * KB) + ((n) & 3) * 3584)
#define P4W(n, f) wf_[f] = lds_tr(L, (P4S(n) == 1 || P4S(n) == 2 ? g0h : g0) + P4O(n) + (f) * 32, (P4S(n) == 1 || P4S(n) == 2 ? g1h : g1) + P4O(n) + (f) * 32)
    bf16x8 pre[3][7];
#pragma unroll
    for (int n = 0; n < 4; ++n)
#pragma unroll
      for (int f = 0; f < 7; ++f) { P4W(n, f); if (n < 3) pre[n][f] = wf_[f]; }
    const u32 a_done = lbase + L_DONE + w * 4;
    lds_post(a_done, 1);

    wait_flags(FLAG(WS_FX, (t4 * 4 + w) * 32 + (ln & 31)), tag);
    auto ld_y = [&](int n) { yf[n] = __builtin_amdgcn_raw_buffer_load_b128(rs, ln * 16, WS_X1 + ((t4 * 4 + w) * 32 + ((n + 4 * rot4) & 31)) * 1024, 16); };
#pragma unroll
    for (int n = 0; n < 5; ++n) ld_y(n);
    u32x4 land;
    static_for<0, 32>([&](auto nc) {
      constexpr int n = nc.value, m = n + 1;
#pragma unroll
      for (int f = 0; f < 7; ++f) {
        SB();
        ag[f] = MFMA(n < 3 ? pre[n < 3 ? n : 0][f] : wf_[f], asbf(yf[n]), ag[f]);
        if ((m & 3) == 0 && f == 0) {
          if (m > 4) lds_post(a_done, m / 4);
          if (m >= 12) { PIN(ag[0]); if (!all_ge(land, m / 4)) lds_wait(L, L_LAND, m / 4); }
        }
        if ((m & 3) == 3 && m >= 11 && f == 0) land = *(volatile LAS u32x4*)(L + L_LAND);
        if (m >= 4 && m < 32) P4W(m, f);
        if constexpr (n < 27) { if (f == 1) ld_y(5 + n); }
      }
#pragma unroll
      for (int f = 0; f < 7; ++f) PIN(ag[f]);
    });
    const u32x4 bst = lds_ld128(L, L_BST + (32 * w + (ln & 31)) * 16);
    const u32x4 bfr = {ln < 32 ? bst[0] : 0u, ln < 32 ? bst[1] : 0u, 0u, 0u};
#pragma unroll
    for (int f = 0; f < 7; ++f) {
      const u32x2 uv = *(LAS u32x2*)(L + L_UVT + (f * 32 + (ln & 31)) * 8);
      ag[f] = MFMA(asbf((u32x4){uv[0], uv[1], 0u, 0u}), asbf(bfr), ag[f]);
    }
    const float rs_ = __uint_as_float(bst[2]), sd_ = bf_lo(bst[1]);
    const float k1 = -2.0f * 0.7978845608028654f * 1.4426950408889634f * rs_, k3 = k1 * 0.044715f * rs_ * rs_, i2 = sd_ * sd_;
    ln = lane_id();
    const u32 ao = WS_ACT + ((t4 * 4 + w) * 112 + 7 * j4) * 1024 + ln * 16;
#pragma unroll
    for (int f = 0; f < 7; ++f) {
      u32x4 o;
#pragma unroll
      for (int k = 0; k < 4; ++k) { const f32x2 a = geglu2((f32x2){ag[f][2 * k], ag[f][2 * k + 1]}, (f32x2){ag[f][8 + 2 * k], ag[f][9 + 2 * k]}, k1, k3, i2); o[k] = pk(a[0], a[1]); }
      __builtin_amdgcn_raw_buffer_store_b128(o, rs, ao + f * 1024, 0, 16);
    }
    WAITV(0);
    if (ln == 0) __hip_atomic_store(FLAG(WS_FA, (t4 * 4 + w) * 16 + j4), tag, RLX_AGENT);
  } else {
    const u32 lane = lane_id(), r32 = lane & 31, hh = lane >> 5;
    const u32 w8o = (lane >> 3) * 1024u + (((lane & 7) ^ (((lane >> 4) & 1) << 2)) << 4);
    const u32 wt0 = (4 * hh + ((lane & 15) >> 2)) * 128 + ((32 * ((lane >> 4) & 1) + 8 * (lane & 3)) ^ (((lane & 15) >> 3) << 6)), wt1 = wt0 ^ 64u;
    auto load_wd = [&](int i) {
      int ks = rot5; PINS(ks);
      ks += i; ks -= ks >= 28 ? 28 : 0;
      const u32 so = cg * 128 + (w4 * 448 + ks * 16) * 1024, ld = lbase + wd_base(i) + w4 * 2048;
      glds(srd_d, w8o, so, ld);
      glds(srd_d, w8o, so + 8192, ld + 1024);
    };
    const u32 a_land = lbase + L_LAND + w4 * 4;
    bf16x8 wp0[11], wp1[11];
    u32 dC0 = w4 * 2048 + wt0, dC1 = w4 * 2048 + wt1, dB0 = dC0 + 92 * KB, dB1 = dC1 + 92 * KB, dA0 = dC0 + 64 * KB, dA1 = dC1 + 64 * KB;
    PIN(dA0); PIN(dA1); PIN(dB0); PIN(dB1); PIN(dC0); PIN(dC1);
#define WD0(i) (wd_base(i) < 64 * KB ? dC0 + wd_base(i) : wd_base(i) < 92 * KB ? dA0 + (wd_base(i) - 64 * KB) : dB0 + (wd_base(i) - 92 * KB))
#define WD1(i) (wd_base(i) < 64 * KB ? dC1 + wd_base(i) : wd_base(i) < 92 * KB ? dA1 + (wd_base(i) - 64 * KB) : dB1 + (wd_base(i) - 92 * KB))
#define WDTURN(i)                                                                                                                   \
  do {                                                                                                                              \
    _Pragma("unroll") for (int k = (i); k < (i) + 2; ++k) { wp0[k] = lds_tr(L, WD0(k), WD0(k) + 1024); wp1[k] = lds_tr(L, WD1(k), WD1(k) + 1024); } \
    _Pragma("unroll") for (int k = (i); k < (i) + 2; ++k) { PIN(wp0[k]); PIN(wp1[k]); }                                             \
    load_wd((i) + 2); load_wd((i) + 3);                                                                                             \
  } while (0)
    load_gu(3, gu_slot(3));
    lds_wait(L, L_DONE, 1);
    load_gu(4, gu_slot(4));
    load_wd(0); load_wd(1); load_wd(21); load_wd(22);
    WAITV(15); lds_post(a_land, 3);
    wait_flags(FLAG(WS_FUV, j4 * 64 + lane), tag);
    lds_post(a_land, 4);
    {
      u32x4 pt[16];
      const u32 o = lane < 28 ? (w4 * 28 + lane) * 16 : 0x7fffffffu;
#pragma unroll
      for (int k = 0; k < 16; ++k) pt[k] = __builtin_amdgcn_raw_buffer_load_b128(rs, o, WS_UV + (j4 * 16 + k) * 2048, 16);
      SB();
      f32x4 a = {};
#pragma unroll
      for (int k = 0; k < 16; ++k) {
        const f32x4 t_ = __builtin_bit_cast(f32x4, pt[k]);
#pragma unroll
        for (int e = 0; e < 4; ++e) { float y_ = a[e] + t_[e]; asm("" : "+v"(y_)); a[e] = y_; }
      }
      if (lane < 28) {
        const u32 vi = (w4 * 28 + lane) * 4;
#pragma unroll
        for (int e = 0; e < 4; ++e) {
          const float hi = rb(a[e]);
          *(LAS u32*)(L + L_UVT + ((vi >> 6) * 32 + (vi & 31) + e) * 8 + ((vi >> 5) & 1) * 4) = pk(hi, a[e] - hi);
        }
      }
    }
    {
      lds_wait(L, L_DONE, 2);
      const u32 so = (t4 * 128 + 32 * w4 + r32) * 64 + hh * 32;
      const u32x4 s0 = __builtin_amdgcn_raw_buffer_load_b128(rs, so, WS_ST, 16), s1 = __builtin_amdgcn_raw_buffer_load_b128(rs, so + 16, WS_ST, 16);
      SB();
      load_gu(5, gu_slot(5));
      WDTURN(0);
      lds_wait(L, L_DONE, 3); load_gu(6, gu_slot(6)); WAITV(11); lds_post(a_land, 5);
      {
        auto xsum = [&](float v) {
          auto r_ = __builtin_amdgcn_permlane32_swap(__float_as_uint(v), __float_as_uint(v), false, false);
          return __uint_as_float(r_[0]) + __uint_as_float(r_[1]);
        };
        const float a0 = __uint_as_float(s0[0]), a1 = __uint_as_float(s0[2]), a2 = __uint_as_float(s1[0]), a3 = __uint_as_float(s1[2]);
        const float me = xsum((a0 + a1) + (a2 + a3)) * (1.f / 512.f);
        const float d0 = a0 * (1.f / 64.f) - me, d1 = a1 * (1.f / 64.f) - me, d2 = a2 * (1.f / 64.f) - me, d3 = a3 * (1.f / 64.f) - me;
        const float m2 = (__uint_as_float(s0[1]) + __uint_as_float(s0[3])) + (__uint_as_float(s1[1]) + __uint_as_float(s1[3]));
        float mb, sd;
        ln_scal(me, xsum(m2 + 64.f * ((d0 * d0 + d1 * d1) + (d2 * d2 + d3 * d3))) * (1.f / 512.f), mb, sd);
        if (lane < 32) lds_st128(L, L_BST + (32 * w4 + lane) * 16, (u32x4){pk(-mb, -mb), pk(sd, sd), __float_as_uint(__builtin_amdgcn_rcpf(sd)), 0u});
      }
      lds_wait(L, L_DONE, 4); load_gu(7, gu_slot(7)); WAITV(7); lds_post(a_land, 6); WDTURN(2); WAITV(4); lds_post(a_land, 8);
      lds_wait(L, L_DONE, 5); load_wd(8); load_wd(9); load_wd(10); WAITV(6); WDTURN(4);
      lds_wait(L, L_DONE, 6); load_wd(11); load_wd(12); load_wd(13);
      lds_wait(L, L_DONE, 7); load_wd(14); load_wd(15); load_wd(16); load_wd(17);
      lds_wait(L, L_DONE, 8);
    }
#pragma unroll
    for (int i = 18; i < 21; ++i) load_wd(i);
    WAITV(20);
#pragma unroll
    for (int k = 6; k < 11; ++k) { wp0[k] = lds_tr(L, WD0(k), WD0(k) + 1024); wp1[k] = lds_tr(L, WD1(k), WD1(k) + 1024); }
#pragma unroll
    for (int k = 6; k < 11; ++k) { PIN(wp0[k]); PIN(wp1[k]); }
#pragma unroll
    for (int i = 23; i < 28; ++i) load_wd(i);
    wait_flags(FLAG(WS_FA, (t3 * 4 + 2 * hf + ((lane >> 2) & 1)) * 16 + 4 * w4 + (lane & 3)), tag);
    const u32 ao = WS_ACT + ((t3 * 4 + 2 * hf) * 112 + 28 * w4) * 1024 + lane * 16;
    u32x4 af0[28], af1[28];
    auto ld_act = [&](int i) {
      int ks = i + rot5; ks -= ks >= 28 ? 28 : 0;
      af0[i] = __builtin_amdgcn_raw_buffer_load_b128(rs, ao, ks * 1024, 16);
      af1[i] = __builtin_amdgcn_raw_buffer_load_b128(rs, ao + 112 * 1024, ks * 1024, 16);
    };
#pragma unroll
    for (int i = 0; i < 5; ++i) ld_act(i);
    f32x16 a00 = {}, a01 = {}, a10 = {}, a11 = {};
    static_for<0, 28>([&](auto ic) {
      constexpr int i = ic.value;
      SB();
      a00 = MFMA(wp0[i % 11], asbf(af0[i]), a00);
      a01 = MFMA(wp1[i % 11], asbf(af0[i]), a01);
      SB();
      a10 = MFMA(wp0[i % 11], asbf(af1[i]), a10);
      if constexpr (i < 6) { ld_act(5 + 2 * i); ld_act(6 + 2 * i); }
      else if constexpr (i + 11 < 28) ld_act(i + 11);
      SB();
      a11 = MFMA(wp1[i % 11], asbf(af1[i]), a11);
      if constexpr (i >= 8 && i + 3 < 28) {
        wp0[(i + 3) % 11] = lds_tr(L, WD0(i + 3), WD0(i + 3) + 1024);
        wp1[(i + 3) % 11] = lds_tr(L, WD1(i + 3), WD1(i + 3) + 1024);
      }
      PIN(a00); PIN(a01); PIN(a10); PIN(a11);
    });
    red_write(20 * KB + w4 * 2048, 8192, a00, a01);
    red_write(92 * KB + w4 * 2048, 8192, a10, a11);
  }
  BAR();
  {
    f32x4 lo, hi;
    red_read(mw ? 20 * KB : 60 * KB, 8192, 2048, lo, hi);
    u32 ln = lane_id();
    const f32x4 gb0 = lds_ldf4(L, L_MISC + M_GB + (ln & 7) * 32), gb1 = lds_ldf4(L, L_MISC + M_GB + (ln & 7) * 32 + 16);
    u32x4 o;
    o[0] = pk(bf_lo(x1p[0]) + rb(gb0[0] * rb(lo[0])), bf_hi(x1p[0]) + rb(gb0[1] * rb(lo[1])));
    o[1] = pk(bf_lo(x1p[1]) + rb(gb0[2] * rb(lo[2])), bf_hi(x1p[1]) + rb(gb0[3] * rb(lo[3])));
    o[2] = pk(bf_lo(x1p[2]) + rb(gb1[0] * rb(hi[0])), bf_hi(x1p[2]) + rb(gb1[1] * rb(hi[1])));
    o[3] = pk(bf_lo(x1p[3]) + rb(gb1[2] * rb(hi[2])), bf_hi(x1p[3]) + rb(gb1[3] * rb(hi[3])));
    *(GAS u32x4*)((GAS char*)out + (t3 * 128 + hf * 64 + 8 * w) * 1024 + cg * 128 + (ln >> 3) * 1024 + (ln & 7) * 16) = o;
  }
  asm volatile(".Ladaln_end:");
}

extern "C" void kernel_launch(void* const* d_in, const int* in_sizes, int n_in, void* d_out, int out_size, void* d_ws, size_t ws_size,
                              hipStream_t stream) {
  adaln_block<<<256, 512, 0, stream>>>((const u16*)d_in[0], (const u16*)d_in[1], (const u16*)d_in[2], (const u16*)d_in[4], (const u16*)d_in[5],
                                       (const u16*)d_in[6], (const u16*)d_in[7], (const u16*)d_in[8], (u16*)d_out, (char*)d_ws);
}
```
